```python
import torch

def _softmax(x, axis=-1):
    return torch.softmax(x, dim=axis)
def _gelu(x):
    return torch.nn.functional.gelu(x, approximate="tanh")
def _silu(x):
    return torch.nn.functional.silu(x)
def _sigmoid(x):
    return torch.sigmoid(x)
def _layernorm(x, axis=-1, eps=1e-5):
    m = x.mean(dim=axis, keepdim=True)
    v = x.var(dim=axis, keepdim=True, unbiased=False)
    return (x - m) / torch.sqrt(v + eps)
def _scatter_add(tgt, idx, src, axis=0):
    return tgt.clone().index_add_(axis, idx, src)
def _segment_sum(src, seg, n):
    out = torch.zeros((n,) + src.shape[1:], dtype=src.dtype)
    return out.index_add_(0, seg, src)
def _pad(x, pw):
    flat = tuple(v for pair in reversed(pw) for v in pair)
    return torch.nn.functional.pad(x, flat)

kernel_name = 'interleave_layers__ssm_conv_s4d__glu_gated_glu__prompt'

def setup_inputs():

    _w = (lambda a: (lambda t: t.to(torch.bfloat16) if t.is_floating_point() else t)(torch.as_tensor(a)))
    def _A_setup():
        Bsz, L, D, N = 1, 2048, 512, 128
        return {
            "u": _w((lambda *s: torch.randn(*s, dtype=torch.bfloat16) * 0.05)(Bsz, L, D)),
            "log_A_real": _w((lambda *s: torch.randn(*s, dtype=torch.bfloat16) * 3.2)(D, N) * 0.5),
            "A_imag": _w((lambda *s: torch.randn(*s, dtype=torch.bfloat16) * 0.1)(D, N) * 3.14),
            "C_real": _w((lambda *s: torch.randn(*s, dtype=torch.bfloat16) * 0.1)(D, N) / N ** 0.5),
            "C_imag": _w((lambda *s: torch.randn(*s, dtype=torch.bfloat16) * 0.1)(D, N) / N ** 0.5),
            "log_dt": _w((lambda *s: torch.randn(*s, dtype=torch.bfloat16) * 0.1)(D) * 0.5 - 4.0),
            "D_skip": _w((lambda *s: torch.randn(*s, dtype=torch.bfloat16) * 0.1)(D)),
        }
    _A_in = _A_setup()

    def _B_setup():
        B, S, D, F = 1, 2048, 512, 1376
        return {
            "x": _w((lambda *s: torch.randn(*s, dtype=torch.bfloat16) * 0.05)(B, S, D)),
            "w_gate": _w((lambda *s: torch.randn(*s, dtype=torch.bfloat16) * 0.05)(D, F)),
            "w_up": _w((lambda *s: torch.randn(*s, dtype=torch.bfloat16) * 0.05)(D, F)),
            "w_down": _w((lambda *s: torch.randn(*s, dtype=torch.bfloat16) * 0.05)(F, D)),
        }
    _B_in = _B_setup()

    return {
        "A_u": _A_in["u"],
        "A_log_A_real": _A_in["log_A_real"],
        "A_A_imag": _A_in["A_imag"],
        "A_C_real": _A_in["C_real"],
        "A_C_imag": _A_in["C_imag"],
        "A_log_dt": _A_in["log_dt"],
        "A_D_skip": _A_in["D_skip"],
        "B_x": _B_in["x"],
        "B_w_gate": _B_in["w_gate"],
        "B_w_up": _B_in["w_up"],
        "B_w_down": _B_in["w_down"],
    }

def reference(A_u, A_log_A_real, A_A_imag, A_C_real, A_C_imag, A_log_dt, A_D_skip, B_x, B_w_gate, B_w_up, B_w_down):
    def _A_ref(u, log_A_real, A_imag, C_real, C_imag, log_dt, D_skip):
        L = u.shape[1]
        dt = torch.exp(log_dt)
        A = -torch.exp(log_A_real) + 1j * A_imag
        C = (C_real + 1j * C_imag) * dt[:, None]
        pos = (lambda x, dt: x.to(dt))(torch.arange(L), log_dt.dtype)
        P = torch.exp((A * dt[:, None])[:, :, None] * pos[None, None, :])
        K = 2.0 * torch.einsum("dn,dnl->dl", C, P).real
        L2 = 2 * L
        u_t = (lambda x, *d: x.permute(*d))(u, 0, 2, 1)
        Kf = (lambda x, *a, **k: torch.fft.rfft(x.float(), *a, **k))(K, L2)
        Uf = (lambda x, *a, **k: torch.fft.rfft(x.float(), *a, **k))(u_t, L2)
        y = (lambda x, *a, **k: torch.fft.irfft(x, *a, **k).to(torch.bfloat16))(Uf * Kf[None, :, :], L2)[:, :, :L]
        y = (lambda x, *d: x.permute(*d))(y, 0, 2, 1)
        return y + u * D_skip[None, None, :]

    def _B_ref(x, w_gate, w_up, w_down):
        g = torch.einsum("bsd,df->bsf", x, w_gate)
        u = torch.einsum("bsd,df->bsf", x, w_up)
        h = _sigmoid(g) * u
        return torch.einsum("bsf,fd->bsd", h, w_down)

    x = A_u
    x = x + _A_ref(_layernorm(x, axis=-1), A_log_A_real, A_A_imag, A_C_real, A_C_imag, A_log_dt, A_D_skip)
    x = x + _B_ref(_layernorm(x, axis=-1), B_w_gate, B_w_up, B_w_down)
    x = x + _A_ref(_layernorm(x, axis=-1), A_log_A_real, A_A_imag, A_C_real, A_C_imag, A_log_dt, A_D_skip)
    x = x + _B_ref(_layernorm(x, axis=-1), B_w_gate, B_w_up, B_w_down)
    return x
```

```cpp
#include <hip/hip_runtime.h>
#include <hip/hip_bf16.h>
#include <stdint.h>
#include <utility>

typedef unsigned short u16;
typedef unsigned int u32;
typedef __bf16 bf16x8 __attribute__((ext_vector_type(8)));
typedef __bf16 bf16x4 __attribute__((ext_vector_type(4)));
typedef __bf16 bf16x2 __attribute__((ext_vector_type(2)));
typedef _Float16 f16x8 __attribute__((ext_vector_type(8)));
typedef _Float16 f16x2 __attribute__((ext_vector_type(2)));
typedef float f32x16 __attribute__((ext_vector_type(16)));
typedef float f32x4 __attribute__((ext_vector_type(4)));
typedef float f32x2 __attribute__((ext_vector_type(2)));
typedef u32 u32x4 __attribute__((ext_vector_type(4)));
typedef u32 u32x2 __attribute__((ext_vector_type(2)));

#define LAS __attribute__((address_space(3)))
#define GAS __attribute__((address_space(1)))
#define DI __device__ __forceinline__

constexpr int SEQ = 2048, DM = 512, NST = 128, FF = 1376, NU = FF / 16;

DI float bf2f(u16 h) { return __uint_as_float((u32)h << 16); }
DI float bflo(u32 w) { return __uint_as_float(w << 16); }
DI float bfhi(u32 w) { return __uint_as_float(w & 0xffff0000u); }
DI u32 pk_bf16(float lo, float hi) { f32x2 v = {lo, hi}; return __builtin_bit_cast(u32, __builtin_convertvector(v, bf16x2)); }
DI float rbf(float x) { return bflo(pk_bf16(x, 0.f)); }
DI u32 pk_f16(float lo, float hi) { f32x2 v = {lo, hi}; return __builtin_bit_cast(u32, __builtin_convertvector(v, f16x2)); }
DI float dot2(u32 a, u32 b, float c) { return __builtin_amdgcn_fdot2_f32_bf16(__builtin_bit_cast(bf16x2, a), __builtin_bit_cast(bf16x2, b), c, false); }
DI u32 perm_lo(u32 hi, u32 lo) { return __builtin_amdgcn_perm(hi, lo, 0x05040100u); }
DI u32 perm_hi(u32 hi, u32 lo) { return __builtin_amdgcn_perm(hi, lo, 0x07060302u); }
DI int pi_fwd(int r) { return (((r >> 2) & 1) << 4) | ((r >> 3) << 2) | (r & 3); }
DI int pi_inv(int p) { return (((p >> 2) & 3) << 3) | ((p >> 4) << 2) | (p & 3); }

template <int N> DI void wait_vm() { asm volatile("s_waitcnt vmcnt(%0)" ::"n"(N) : "memory"); }
DI void wait_lgkm0() { asm volatile("s_waitcnt lgkmcnt(0)" ::: "memory"); }
DI void raw_barrier() { asm volatile("" ::: "memory"); __builtin_amdgcn_s_barrier(); asm volatile("" ::: "memory"); }
DI void glds16(const void* src, LAS char* dst) {
    __builtin_amdgcn_global_load_lds((const GAS void*)src, (LAS void*)dst, 16, 0, 0);
}
DI bf16x8 tr_frag(LAS char* a0, LAS char* a1) {
    bf16x4 lo = __builtin_amdgcn_ds_read_tr16_b64_v4bf16((LAS bf16x4*)a0);
    bf16x4 hi = __builtin_amdgcn_ds_read_tr16_b64_v4bf16((LAS bf16x4*)a1);
    return __builtin_shufflevector(lo, hi, 0, 1, 2, 3, 4, 5, 6, 7);
}
#define MFMA32(a, b, c) __builtin_amdgcn_mfma_f32_32x32x16_bf16((a), (b), (c), 0, 0, 0)

template <class F, int... I> DI void static_for_impl(F&& f, std::integer_sequence<int, I...>) { (f(std::integral_constant<int, I>{}), ...); }
template <int N, class F> DI void static_for(F&& f) { static_for_impl(f, std::make_integer_sequence<int, N>{}); }

DI float wave_sum(float v) {
#pragma unroll
    for (int m = 32; m >= 1; m >>= 1) v += __shfl_xor(v, m);
    return v;
}
DI void ln_scalars(float mean, float var, float& mb, float& sb) {
    mb = rbf(mean);
    float vb = rbf(fmaxf(var, 0.f));
    sb = rbf(__builtin_sqrtf(rbf(vb + 1e-5f)));
}

struct cplx { float x, y; };
DI cplx cmul(cplx a, cplx b) { return {a.x * b.x - a.y * b.y, a.x * b.y + a.y * b.x}; }
DI cplx cexp_p(float arl, float aiv, float p) {
    float mag = __builtin_amdgcn_exp2f(arl * p);
    float th = __builtin_amdgcn_fractf(aiv * p);
    return {mag * __builtin_amdgcn_cosf(th), mag * __builtin_amdgcn_sinf(th)};
}

constexpr int KG_ROW = 528;
constexpr int KG_B = 32 * KG_ROW, KG_TAB = 64 * KG_ROW, PREP_LDS = KG_TAB + 2304 * 2;
constexpr int PB_KGEN = 512, PB_TR = 128, PB_ST = 128, PB_W1 = NU, PB_W2 = 43;
constexpr int PREP_BLOCKS = PB_KGEN + PB_TR + PB_ST + PB_W1 + PB_W2;

DI void transpose8x8(const u32x4 (&in)[8], u32x4 (&out)[8]) {
#pragma unroll
    for (int q = 0; q < 4; ++q)
#pragma unroll
        for (int jj = 0; jj < 4; ++jj) {
            out[2 * q][jj] = perm_lo(in[2 * jj + 1][q], in[2 * jj][q]);
            out[2 * q + 1][jj] = perm_hi(in[2 * jj + 1][q], in[2 * jj][q]);
        }
}

__global__ __launch_bounds__(256) void prep_kernel(
    const u16* __restrict__ x0, const u16* __restrict__ logA, const u16* __restrict__ Aim,
    const u16* __restrict__ Cre, const u16* __restrict__ Cim, const u16* __restrict__ logdt,
    const u16* __restrict__ Wg, const u16* __restrict__ Wu, const u16* __restrict__ Wd,
    u16* __restrict__ xT, float* __restrict__ stats0, u16* __restrict__ Kg,
    u16* __restrict__ W1F, u16* __restrict__ W2F) {
    extern __shared__ __attribute__((aligned(16))) char smem_raw[];
    LAS char* L = (LAS char*)smem_raw;
    const int tid = threadIdx.x, lane = tid & 63;
    const int wave = __builtin_amdgcn_readfirstlane(tid >> 6);
    int b = blockIdx.x;
    if (b < PB_KGEN) {
        const int d = b, n = tid & 127;
        const float dt = rbf(expf(bf2f(logdt[d])));
        const float ar = -rbf(expf(bf2f(logA[d * NST + n]))) * dt;
        const float ai = bf2f(Aim[d * NST + n]) * dt;
        const float arl = ar * 1.44269504088896341f, aiv = ai * 0.159154943091895336f;
        float mx;
        {
            u32 a = ((const u32*)(Cre + d * NST))[lane], c = ((const u32*)(Cim + d * NST))[lane];
            mx = fmaxf(fmaxf(fabsf(bflo(a)), fabsf(bfhi(a))), fmaxf(fabsf(bflo(c)), fabsf(bfhi(c))));
#pragma unroll
            for (int m = 32; m >= 1; m >>= 1) mx = fmaxf(mx, __shfl_xor(mx, m));
        }
        const bool okm = mx > 1e-30f && mx < 1e30f;
        const float sc = okm ? 256.f / mx : 1.f;
        const float back = (okm ? mx * (1.f / 256.f) : 1.f) * dt * (2.f / 256.f);
        if (wave < 2) {
            cplx t0 = {bf2f(Cre[d * NST + n]) * sc, bf2f(Cim[d * NST + n]) * sc};
            cplx w1 = cexp_p(arl, aiv, 1.f), v = cexp_p(arl, aiv, 256.f);
            cplx w2 = cmul(w1, w1), w3 = cmul(w2, w1), w4 = cmul(w2, w2);
            cplx w5 = cmul(w4, w1), w6 = cmul(w4, w2), w7 = cmul(w4, w3);
            LAS u32* A = (LAS u32*)(L + 4 * n);
            auto put = [&](int row, cplx z) { A[row * (KG_ROW / 4)] = pk_f16(z.x, -z.y); };
            put(0, t0); put(1, cmul(t0, w1)); put(2, cmul(t0, w2)); put(3, cmul(t0, w3));
            put(4, cmul(t0, w4)); put(5, cmul(t0, w5)); put(6, cmul(t0, w6)); put(7, cmul(t0, w7));
            cplx t = cmul(t0, v);
            put(8, t); put(9, cmul(t, w2)); put(10, cmul(t, w4)); put(11, cmul(t, w6));
            t = cmul(t, v); put(12, t); put(13, cmul(t, w4));
            t = cmul(t, v); put(14, t); put(15, cmul(t, w4));
#pragma unroll
            for (int r = 16; r <= 20; ++r) { t = cmul(t, v); put(r, t); }
#pragma unroll
            for (int r = 21; r < 32; ++r) A[r * (KG_ROW / 4)] = 0u;
        } else {
            cplx w[8];
            w[0] = {1.f, 0.f};
            w[1] = cexp_p(arl, aiv, 8.f);
            cplx v = cexp_p(arl, aiv, 64.f);
            w[2] = cmul(w[1], w[1]); w[3] = cmul(w[2], w[1]); w[4] = cmul(w[2], w[2]);
            w[5] = cmul(w[4], w[1]); w[6] = cmul(w[4], w[2]); w[7] = cmul(w[4], w[3]);
            LAS u32* B = (LAS u32*)(L + KG_B + 4 * n);
            cplx t = {256.f, 0.f};
#pragma unroll
            for (int a = 0; a < 4; ++a) {
#pragma unroll
                for (int c = 0; c < 8; ++c) {
                    cplx z = (c == 0) ? t : cmul(t, w[c]);
                    B[(8 * a + c) * (KG_ROW / 4)] = pk_f16(z.x, z.y);
                }
                if (a < 3) t = cmul(t, v);
            }
        }
        __syncthreads();
        {
            const int rb = wave & 1, cb = wave >> 1;
            LAS char* pa = L + (16 * rb + (lane & 15)) * KG_ROW + (lane >> 4) * 16;
            LAS char* pb = L + KG_B + (16 * cb + (lane & 15)) * KG_ROW + (lane >> 4) * 16;
            f32x4 acc = {0.f, 0.f, 0.f, 0.f};
#pragma unroll
            for (int ks = 0; ks < 8; ++ks) {
                f16x8 fa = *(LAS f16x8*)(pa + 64 * ks), fb = *(LAS f16x8*)(pb + 64 * ks);
                acc = __builtin_amdgcn_mfma_f32_16x16x32_f16(fa, fb, acc, 0, 0, 0);
            }
            LAS u16* tab = (LAS u16*)(L + KG_TAB);
            const int col = 16 * cb + (lane & 15);
#pragma unroll
            for (int j = 0; j < 4; ++j) {
                const int row = 16 * rb + 4 * (lane >> 4) + j;
                int phi = row < 8 ? row : row < 12 ? 256 + 2 * (row - 8) : row < 16 ? 512 + 256 * ((row >> 1) & 1) + 4 * (row & 1) : 1024 + 256 * (row - 16);
                if (row <= 20) tab[phi + 8 * col] = (u16)pk_bf16(acc[j] * back, 0.f);
            }
        }
        __syncthreads();
        {
            LAS u16* tab = (LAS u16*)(L + KG_TAB);
            const int j0 = 8 * tid;
            const int sh = j0 < 256 ? 0 : j0 < 512 ? 1 : j0 < 1024 ? 2 : 3;
            u32 v[8];
#pragma unroll
            for (int e = 0; e < 8; ++e) {
                const int j = j0 + e;
                const int p = sh == 0 ? j : ((j + ((1 << sh) >> 1) - 1 + ((j >> sh) & 1)) >> sh) << sh;
                v[e] = tab[p];
            }
            u32x4 o = {v[0] | (v[1] << 16), v[2] | (v[3] << 16), v[4] | (v[5] << 16), v[6] | (v[7] << 16)};
            *(u32x4*)(Kg + d * SEQ + j0) = o;
        }
        return;
    }
    b -= PB_KGEN;
    if (b < PB_TR) {
        const int w = b * 4 + wave, tau = w >> 6, lg = (w >> 3) & 7, cgg = w & 7;
        const int ln = lg * 8 + (lane >> 3), cg = cgg * 8 + (lane & 7);
        u32x4 in[4];
#pragma unroll
        for (int g = 0; g < 4; ++g) in[g] = *(const u32x4*)(x0 + (256 * tau + 64 * g + ln) * DM + 8 * cg);
        u16* dst = xT + (8 * cg) * SEQ + 256 * tau + 4 * ln;
#pragma unroll
        for (int q = 0; q < 4; ++q) {
            u32x2 lo = {perm_lo(in[1][q], in[0][q]), perm_lo(in[3][q], in[2][q])};
            u32x2 hi = {perm_hi(in[1][q], in[0][q]), perm_hi(in[3][q], in[2][q])};
            *(u32x2*)(dst + (2 * q) * SEQ) = lo;
            *(u32x2*)(dst + (2 * q + 1) * SEQ) = hi;
        }
        return;
    }
    b -= PB_TR;
    if (b < PB_ST) {
        const int w = b * 4 + wave, tau = w >> 6, ln = w & 63;
        u32x4 in[4];
#pragma unroll
        for (int g = 0; g < 4; ++g) in[g] = *(const u32x4*)(x0 + (256 * tau + 64 * g + ln) * DM + 8 * lane);
        float mean[4], var[4];
#pragma unroll
        for (int g = 0; g < 4; ++g) {
            float s = 0.f;
#pragma unroll
            for (int q = 0; q < 4; ++q) s += bflo(in[g][q]) + bfhi(in[g][q]);
            mean[g] = wave_sum(s) * (1.f / DM);
        }
#pragma unroll
        for (int g = 0; g < 4; ++g) {
            float s = 0.f;
#pragma unroll
            for (int q = 0; q < 4; ++q) { float a = bflo(in[g][q]) - mean[g], c = bfhi(in[g][q]) - mean[g]; s += a * a + c * c; }
            var[g] = wave_sum(s) * (1.f / DM);
        }
        if (lane == 0) {
            f32x4* dst = (f32x4*)(stats0 + 2 * (256 * tau + 4 * ln));
            dst[0] = f32x4{mean[0], var[0], mean[1], var[1]};
            dst[1] = f32x4{mean[2], var[2], mean[3], var[3]};
        }
        return;
    }
    b -= PB_ST;
    if (b < PB_W1) {
        const int u = b, ko = tid >> 2, ty = (tid >> 1) & 1, oc = tid & 1;
        const u16* W = ty ? Wu : Wg;
        u32x4 in[8], out[8];
#pragma unroll
        for (int j = 0; j < 8; ++j) in[j] = *(const u32x4*)(W + (8 * ko + j) * FF + 16 * u + 8 * oc);
        transpose8x8(in, out);
        u32x4* dst = (u32x4*)W1F + ((u * 33 + (ko >> 1)) * 64 + (ko & 1) * 32 + 16 * ty + 4 * oc);
        LAS float* red = (LAS float*)L;
#pragma unroll
        for (int e = 0; e < 8; ++e) {
            dst[8 * (e >> 2) + (e & 3)] = out[e];
            float s = 0.f;
#pragma unroll
            for (int jj = 0; jj < 4; ++jj) s = dot2(out[e][jj], 0x3f803f80u, s);
            red[ko * 33 + 16 * ty + 8 * (e >> 2) + 4 * oc + (e & 3)] = s;
        }
        __syncthreads();
        if (tid < 64) {
            u32x4 o = {0u, 0u, 0u, 0u};
            if (tid < 32) {
                float s = 0.f;
#pragma unroll 8
                for (int k = 0; k < 64; ++k) s += red[k * 33 + tid];
                float hi = rbf(s), mid = rbf(s - hi), lo = rbf(s - hi - mid);
                o[0] = pk_bf16(hi, mid);
                o[1] = pk_bf16(lo, 0.f);
            }
            ((u32x4*)W1F)[(u * 33 + 32) * 64 + tid] = o;
        }
        return;
    }
    b -= PB_W1;
    {
        const int fo = b * 4 + wave, co = lane;
        u32x4 in[8], out[8];
#pragma unroll
        for (int j = 0; j < 8; ++j) in[j] = *(const u32x4*)(Wd + (8 * fo + j) * DM + 8 * co);
        transpose8x8(in, out);
        u32x4* dst = (u32x4*)W2F + (((co >> 2) * NU + (fo >> 1)) * 64 + (fo & 1) * 32);
#pragma unroll
        for (int e = 0; e < 8; ++e) dst[pi_inv(8 * (co & 3) + e)] = out[e];
    }
}

constexpr int CV_KROW = (64 + SEQ) * 2;
constexpr int CV_ZRS = 84 * 16;
constexpr int CV_Z = 2 * CV_KROW;
constexpr int CV_T = CV_Z + 2 * 4 * CV_ZRS;
constexpr int CV_C = CV_T + 2 * 8 * 64 * 8;
constexpr int CV_LDS = CV_C + 2 * 8 * 16;

template <int HALF> struct TauSet { static constexpr int t(int i) { return HALF == 0 ? (i == 0 ? 0 : i == 1 ? 2 : i == 2 ? 5 : 7) : (i == 0 ? 1 : i == 1 ? 3 : i == 2 ? 4 : 6); } };

DI float dpp_shr1_zero(float v) { return __int_as_float(__builtin_amdgcn_update_dpp(0, __float_as_int(v), 0x138, 0xf, 0xf, true)); }
DI float dpp_shr1_carry(float v, float prev) {
    int r = __builtin_amdgcn_update_dpp(0, __float_as_int(prev), 0x13c, 0xf, 0xf, false);
    return __int_as_float(__builtin_amdgcn_update_dpp(r, __float_as_int(v), 0x138, 0xf, 0xf, false));
}

template <int NPART, int HALF>
DI void conv_body(LAS char* L, const u16* __restrict__ xin, u16* __restrict__ xout, const float* __restrict__ stats,
                  const u16* __restrict__ Kg, const u16* __restrict__ Dskip, int ch, int lane) {
    using TS = TauSet<HALF>;
    const int d = 2 * blockIdx.x + ch, dO = 2 * blockIdx.x + (ch ^ 1);
    {
        const u16* src = Kg + d * SEQ + HALF * 1024 + lane * 8;
        LAS char* dst = L + ch * CV_KROW + 128 + HALF * 2048;
        glds16(src, dst);
        glds16(src + 512, dst + 1024);
    }
    u32x2 xm[4], xo[2];
#pragma unroll
    for (int i = 0; i < 4; ++i) xm[i] = *(const u32x2*)(xin + d * SEQ + 256 * TS::t(i) + 4 * lane);
    const int tn0 = ch ? TS::t(2) : TS::t(0), tn1 = ch ? TS::t(3) : TS::t(1);
    xo[0] = *(const u32x2*)(xin + dO * SEQ + 256 * tn0 + 4 * lane);
    xo[1] = *(const u32x2*)(xin + dO * SEQ + 256 * tn1 + 4 * lane);
    float mean[2][4], var[2][4];
    if constexpr (NPART == 1) {
#pragma unroll
        for (int j = 0; j < 2; ++j) {
            const f32x4* p = (const f32x4*)(stats + 2 * (256 * (j ? tn1 : tn0) + 4 * lane));
            f32x4 a = p[0], c = p[1];
            mean[j][0] = a[0]; var[j][0] = a[1]; mean[j][1] = a[2]; var[j][1] = a[3];
            mean[j][2] = c[0]; var[j][2] = c[1]; mean[j][3] = c[2]; var[j][3] = c[3];
        }
    } else {
        f32x4 a[2][NPART], c[2][NPART];
#pragma unroll
        for (int j = 0; j < 2; ++j)
#pragma unroll
            for (int q = 0; q < NPART; ++q) {
                const f32x4* p = (const f32x4*)(stats + 2 * (q * SEQ + 256 * (j ? tn1 : tn0) + 4 * lane));
                a[j][q] = p[0]; c[j][q] = p[1];
            }
#pragma unroll
        for (int j = 0; j < 2; ++j) {
            f32x4 sa = a[j][0], sc = c[j][0];
#pragma unroll
            for (int q = 1; q < NPART; ++q) { sa += a[j][q]; sc += c[j][q]; }
            float S[4] = {sa[0], sa[2], sc[0], sc[2]}, Q[4] = {sa[1], sa[3], sc[1], sc[3]};
#pragma unroll
            for (int g = 0; g < 4; ++g) {
                mean[j][g] = S[g] * (1.f / DM);
                var[j][g] = Q[g] * (1.f / DM) - mean[j][g] * mean[j][g];
            }
        }
    }
    const float Dm = bf2f(Dskip[d]), Do = bf2f(Dskip[dO]);
    if (HALF == 0) {
        if (lane < 8) *(LAS u32x4*)(L + ch * CV_KROW + 16 * lane) = u32x4{0u, 0u, 0u, 0u};
    } else {
        if (lane < 32) *(LAS u32x4*)(L + CV_Z + (ch * 4 + (lane >> 3)) * CV_ZRS + 16 * (lane & 7)) = u32x4{0u, 0u, 0u, 0u};
    }
    u32x2 tm[2];
#pragma unroll
    for (int j = 0; j < 2; ++j) {
        const int tn = j ? tn1 : tn0;
        const u32x2 xa = ch ? xm[2 + j] : xm[j], xb = xo[j];
        float zm[4], zo[4];
#pragma unroll
        for (int g = 0; g < 4; ++g) {
            float mb, sb;
            ln_scalars(mean[j][g], var[j][g], mb, sb);
            const float rs = 1.f / sb;
            const float a = (g & 1) ? bfhi(xa[g >> 1]) : bflo(xa[g >> 1]);
            const float c = (g & 1) ? bfhi(xb[g >> 1]) : bflo(xb[g >> 1]);
            const u32 df = pk_bf16(a - mb, c - mb);
            const u32 z = pk_bf16(bflo(df) * rs, bfhi(df) * rs);
            zm[g] = bflo(z); zo[g] = bfhi(z);
            const int off = (lane & 3) * CV_ZRS + 128 + 2 * (64 * tn + 16 * g + (lane >> 2));
            *(LAS u16*)(L + CV_Z + ch * 4 * CV_ZRS + off) = (u16)z;
            *(LAS u16*)(L + CV_Z + (ch ^ 1) * 4 * CV_ZRS + off) = (u16)(z >> 16);
        }
        tm[j] = u32x2{pk_bf16(zm[0] * Dm, zm[1] * Dm), pk_bf16(zm[2] * Dm, zm[3] * Dm)};
        *(LAS u32x2*)(L + CV_T + (((ch ^ 1) * 8 + tn) * 64 + lane) * 8) = u32x2{pk_bf16(zo[0] * Do, zo[1] * Do), pk_bf16(zo[2] * Do, zo[3] * Do)};
    }
    wait_vm<0>();
    wait_lgkm0();
    raw_barrier();

    constexpr int TMAX = TS::t(3);
    f32x16 acc[4];
#pragma unroll
    for (int i = 0; i < 4; ++i)
#pragma unroll
        for (int r = 0; r < 16; ++r) acc[i][r] = 0.f;
    const int hh = lane >> 5;
    LAS char* zb = L + CV_Z + (ch * 4 + (lane & 3)) * CV_ZRS + 128 + 16 * (((lane & 31) >> 2) + hh) - 16 * 7;
    LAS char* kb = L + ch * CV_KROW + 128 + 2 * (16 * ((lane >> 4) & 1) - 32 * hh - 4 * ((lane & 15) >> 2) + 4 * (lane & 3)) - 32;
    static_for<4>([&](auto EI) {
        constexpr int e = 2 * EI + 1;
        bf16x8 kf[TMAX + 1], zf[TMAX + 1];
#pragma unroll
        for (int m = 0; m <= TMAX; ++m) {
            zf[m] = *(LAS bf16x8*)(zb + 16 * (8 * m + 7 - e));
            kf[m] = tr_frag(kb + 32 + 64 * (8 * m + e), kb + 64 * (8 * m + e));
        }
#pragma unroll
        for (int mu = 0; mu <= TMAX; ++mu)
#pragma unroll
            for (int i = 0; i < 4; ++i)
                if (TS::t(i) >= mu) acc[i] = MFMA32(zf[TS::t(i) - mu], kf[mu], acc[i]);
    });

    float y[4][4];
#pragma unroll
    for (int i = 0; i < 4; ++i) {
        float s3p = 0.f, s2p = 0.f, s1p = 0.f;
#pragma unroll
        for (int g = 0; g < 4; ++g) {
            const float s3 = acc[i][4 * g + 3];
            const float s2 = acc[i][4 * g + 2] + (g ? dpp_shr1_carry(s3, s3p) : dpp_shr1_zero(s3));
            const float s1 = acc[i][4 * g + 1] + (g ? dpp_shr1_carry(s2, s2p) : dpp_shr1_zero(s2));
            y[i][g] = acc[i][4 * g] + (g ? dpp_shr1_carry(s1, s1p) : dpp_shr1_zero(s1));
            s3p = s3; s2p = s2; s1p = s1;
        }
        if (lane == 63) *(LAS f32x4*)(L + CV_C + (ch * 8 + TS::t(i)) * 16) = f32x4{s1p, s2p, s3p, 0.f};
    }
    wait_lgkm0();
    raw_barrier();
#pragma unroll
    for (int i = 0; i < 4; ++i) {
        constexpr int dummy = 0; (void)dummy;
        const int tau = TS::t(i);
        if (tau > 0) {
            const float cv = *(LAS float*)(L + CV_C + (ch * 8 + tau - 1) * 16 + 4 * (lane < 3 ? lane : 3));
            y[i][0] += cv;
        }
        const bool mine = ch ? (i >= 2) : (i < 2);
        const u32x2 tl = *(LAS u32x2*)(L + CV_T + ((ch * 8 + tau) * 64 + lane) * 8);
        const u32x2 t = mine ? tm[i & 1] : tl;
        u32x2 o;
#pragma unroll
        for (int p = 0; p < 2; ++p) {
            const u32 yb = pk_bf16(y[i][2 * p], y[i][2 * p + 1]);
            const u32 a = pk_bf16(bflo(yb) + bflo(t[p]), bfhi(yb) + bfhi(t[p]));
            o[p] = pk_bf16(bflo(xm[i][p]) + bflo(a), bfhi(xm[i][p]) + bfhi(a));
        }
        *(u32x2*)(xout + d * SEQ + 256 * tau + 4 * lane) = o;
    }
}

template <int NPART>
__global__ __launch_bounds__(256) void conv_kernel(const u16* xin, u16* xout, const float* __restrict__ stats,
                                                   const u16* __restrict__ Kg, const u16* __restrict__ Dskip) {
    extern __shared__ __attribute__((aligned(16))) char smem_raw[];
    LAS char* L = (LAS char*)smem_raw;
    const int lane = threadIdx.x & 63, wave = __builtin_amdgcn_readfirstlane(threadIdx.x >> 6);
    if (wave & 1) conv_body<NPART, 1>(L, xin, xout, stats, Kg, Dskip, wave >> 1, lane);
    else conv_body<NPART, 0>(L, xin, xout, stats, Kg, Dskip, wave >> 1, lane);
}

constexpr int G1_NS = 16, G1_R = 7, G1_SLOT = 20480, G1_ST = G1_R * G1_SLOT, G1_LDS = G1_ST + 128 * 8;

__global__ __launch_bounds__(512) void gemm1_kernel(const u16* __restrict__ xT, const u16* __restrict__ W1F, u16* __restrict__ hF) {
    extern __shared__ __attribute__((aligned(16))) char smem_raw[];
    LAS char* L = (LAS char*)smem_raw;
    const int lane = threadIdx.x & 63, wave = __builtin_amdgcn_readfirstlane(threadIdx.x >> 6);
    const int xcd = blockIdx.x & 7, sl = blockIdx.x >> 3;
    const int mt = (xcd & 1) * 8 + (sl & 7), fg = (xcd >> 1) * 4 + (sl >> 3);
    if (fg >= 15) return;
    const int u0 = fg * 6 < NU - 6 ? fg * 6 : NU - 6;
    const int hh = lane >> 5, q = (lane & 15) >> 2;
    auto xfrag_off = [&](int tb) { return (8 * hh + q) * 256 + ((tb ^ q) << 6) + ((lane >> 4) & 1) * 32 + (lane & 3) * 8; };

    if (wave >= 4) {
        const int lw = wave - 4;
        const int c16 = lane & 15, rl = lane >> 4;
        const u16* xsrc = xT + (8 * lw + rl) * SEQ + 128 * mt + 8 * ((((c16 >> 2) ^ rl) << 2) | (c16 & 3));
        const u16* wsrc = W1F + lane * 8;
        auto issue = [&](int s, int slot) {
            LAS char* dst = L + slot * G1_SLOT;
            glds16(xsrc + (32 * s) * SEQ, dst + (2 * lw) * 1024);
            glds16(xsrc + (32 * s + 4) * SEQ, dst + (2 * lw + 1) * 1024);
#pragma unroll
            for (int j = 0; j < 3; ++j) {
                const int idx = 3 * lw + j, ul = idx >> 1, kk = idx & 1;
                glds16(wsrc + ((u0 + ul) * 33 + 2 * s + kk) * 512, dst + 8192 + (kk * 6 + ul) * 1024);
            }
        };
        static_for<G1_R - 1>([&](auto S) { issue(S, S); });
        float S0 = 0.f, Q0 = 0.f, S1 = 0.f, Q1 = 0.f;
        const u32 xo = (u32)(uintptr_t)L + xfrag_off(lw);
        static_for<G1_NS>([&](auto SI) {
            constexpr int s = SI;
            constexpr int last = (s + G1_R - 2 < G1_NS - 1) ? s + G1_R - 2 : G1_NS - 1;
            wait_vm<5 * (last - s)>();
            raw_barrier();
            if constexpr (s + G1_R - 1 < G1_NS) issue(s + G1_R - 1, (s + G1_R - 1) % G1_R);
            const u32 base = xo + (s % G1_R) * G1_SLOT;
            u32x2 f0, f1, f2, f3;
            asm volatile("ds_read_b64_tr_b16 %0, %4\n\tds_read_b64_tr_b16 %1, %4 offset:1024\n\t"
                         "ds_read_b64_tr_b16 %2, %4 offset:4096\n\tds_read_b64_tr_b16 %3, %4 offset:5120\n\ts_waitcnt lgkmcnt(0)"
                         : "=&v"(f0), "=&v"(f1), "=&v"(f2), "=&v"(f3) : "v"(base) : "memory");
#pragma unroll
            for (int j = 0; j < 2; ++j) {
                S0 = dot2(f0[j], 0x3f803f80u, S0); Q0 = dot2(f0[j], f0[j], Q0);
                S1 = dot2(f1[j], 0x3f803f80u, S1); Q1 = dot2(f1[j], f1[j], Q1);
                S0 = dot2(f2[j], 0x3f803f80u, S0); Q0 = dot2(f2[j], f2[j], Q0);
                S1 = dot2(f3[j], 0x3f803f80u, S1); Q1 = dot2(f3[j], f3[j], Q1);
            }
        });
        float S = S0 + S1, Q = Q0 + Q1;
        S += __shfl_xor(S, 32); Q += __shfl_xor(Q, 32);
        const float mean = S * (1.f / DM), var = Q * (1.f / DM) - mean * mean;
        float mb, sb;
        ln_scalars(mean, var, mb, sb);
        if (lane < 32) *(LAS f32x2*)(L + G1_ST + (32 * lw + lane) * 8) = f32x2{mb, sb};
        wait_lgkm0();
        raw_barrier();
        return;
    }
    const int wm = wave & 1, wn = wave >> 1;
    bf16x8 csf[3];
#pragma unroll
    for (int i = 0; i < 3; ++i) csf[i] = *(const bf16x8*)(W1F + ((u0 + 3 * wn + i) * 33 + 32) * 512 + lane * 8);
    f32x16 acc[3][2];
#pragma unroll
    for (int i = 0; i < 3; ++i)
#pragma unroll
        for (int t = 0; t < 2; ++t)
#pragma unroll
            for (int r = 0; r < 16; ++r) acc[i][t][r] = 0.f;
    const int xo0 = xfrag_off(2 * wm), xo1 = xfrag_off(2 * wm + 1), wo = 8192 + 3 * wn * 1024 + lane * 16;
    bf16x8 xa[2], wa[3], xb[2], wb[3];
    auto rd = [&](bf16x8(&xf)[2], bf16x8(&wf)[3], int slot, int kk) {
        LAS char* base = L + slot * G1_SLOT;
        xf[0] = tr_frag(base + xo0 + kk * 4096, base + xo0 + kk * 4096 + 1024);
        xf[1] = tr_frag(base + xo1 + kk * 4096, base + xo1 + kk * 4096 + 1024);
#pragma unroll
        for (int i = 0; i < 3; ++i) wf[i] = *(LAS bf16x8*)(base + wo + (kk * 6 + i) * 1024);
    };
    auto mm = [&](bf16x8(&xf)[2], bf16x8(&wf)[3]) {
#pragma unroll
        for (int i = 0; i < 3; ++i)
#pragma unroll
            for (int t = 0; t < 2; ++t) acc[i][t] = MFMA32(wf[i], xf[t], acc[i][t]);
    };
    static_for<G1_NS>([&](auto SI) {
        constexpr int s = SI;
        raw_barrier();
        rd(xa, wa, s % G1_R, 0);
        __builtin_amdgcn_sched_barrier(0);
        if constexpr (s > 0) mm(xb, wb);
        __builtin_amdgcn_sched_barrier(0);
        rd(xb, wb, s % G1_R, 1);
        __builtin_amdgcn_sched_barrier(0);
        mm(xa, wa);
        __builtin_amdgcn_sched_barrier(0);
        wait_lgkm0();
    });
    mm(xb, wb);
    raw_barrier();
    const int rdst = (hh * 32 + pi_inv(lane & 31)) * 8;
#pragma unroll
    for (int t = 0; t < 2; ++t) {
        const f32x2 st = *(LAS f32x2*)(L + G1_ST + (32 * (2 * wm + t) + (lane & 31)) * 8);
        const float mb = st[0], sb = st[1];
        const u32 nm = hh ? 0u : pk_bf16(-mb, -mb);
        const u32x4 mf4 = {nm, nm & 0xffffu, 0u, 0u};
        const bf16x8 mf = __builtin_bit_cast(bf16x8, mf4);
        const float ce = -1.44269504088896341f / sb;
#pragma unroll
        for (int i = 0; i < 3; ++i) {
            const f32x16 a = MFMA32(csf[i], mf, acc[i][t]);
            float hv[8];
#pragma unroll
            for (int j = 0; j < 8; ++j) {
                const float e = __builtin_amdgcn_exp2f(a[j] * ce);
                hv[j] = a[8 + j] * __builtin_amdgcn_rcpf(__builtin_fmaf(e, sb, sb));
            }
            const u32x4 o = {pk_bf16(hv[0], hv[1]), pk_bf16(hv[2], hv[3]), pk_bf16(hv[4], hv[5]), pk_bf16(hv[6], hv[7])};
            *(u32x4*)(hF + ((4 * mt + 2 * wm + t) * NU + u0 + 3 * wn + i) * 512 + rdst) = o;
        }
    }
}

constexpr int G2_NS = 22, G2_R = 9, G2_SLOT = 16384, G2_XS = G2_R * G2_SLOT, G2_XROW = 160, G2_LDS = G2_XS + 64 * G2_XROW;
constexpr int g2_cnt(int s) { return s < G2_NS - 1 ? 4 : 2; }
constexpr int g2_out(int s) {
    int last = (s + G2_R - 2 < G2_NS - 1) ? s + G2_R - 2 : G2_NS - 1, n = 0;
    for (int t = s + 1; t <= last; ++t) n += g2_cnt(t);
    return n;
}

template <int FINAL>
__global__ __launch_bounds__(512) void gemm2_kernel(const u16* __restrict__ hF, const u16* __restrict__ W2F, const u16* xin,
                                                    u16* xout, float* __restrict__ statsP) {
    extern __shared__ __attribute__((aligned(16))) char smem_raw[];
    LAS char* L = (LAS char*)smem_raw;
    const int lane = threadIdx.x & 63, wave = __builtin_amdgcn_readfirstlane(threadIdx.x >> 6);
    const int xcd = blockIdx.x & 7, sl = blockIdx.x >> 3;
    const int mt = xcd * 4 + (sl & 3), nt = sl >> 2;
    if (wave >= 4) {
        const int lw = wave - 4;
        const u16* src = (lw < 2 ? hF + (2 * mt + lw) * NU * 512 : W2F + (2 * nt + lw - 2) * NU * 512) + lane * 8;
        auto issue = [&](auto SI) {
            constexpr int s = SI;
            LAS char* dst = L + (s % G2_R) * G2_SLOT + lw * 4096;
#pragma unroll
            for (int j = 0; j < g2_cnt(s); ++j) glds16(src + (4 * s + j) * 512, dst + j * 1024);
        };
        static_for<G2_R - 1>([&](auto S) { issue(S); });
        static_for<G2_NS>([&](auto SI) {
            constexpr int s = SI;
            wait_vm<g2_out(s)>();
            raw_barrier();
            if constexpr (s + G2_R - 1 < G2_NS) issue(std::integral_constant<int, s + G2_R - 1>{});
        });
        if constexpr (!FINAL) raw_barrier();
        return;
    }
    const int wm = wave & 1, wn = wave >> 1, hh = lane >> 5;
    const int pb = 2 * mt + wm, cb = 2 * nt + wn;
    u32x4 xr[2];
    u16 xs[16];
    if constexpr (!FINAL) {
        const u32x4* p = (const u32x4*)(xin + (32 * cb + pi_fwd(lane & 31)) * SEQ + 32 * pb + 16 * hh);
        xr[0] = p[0]; xr[1] = p[1];
    } else {
        const u16* p = xin + (32 * cb + 16 * hh) * SEQ + 32 * pb + pi_fwd(lane & 31);
#pragma unroll
        for (int r = 0; r < 16; ++r) xs[r] = p[r * SEQ];
    }
    f32x16 acc;
#pragma unroll
    for (int r = 0; r < 16; ++r) acc[r] = 0.f;
    const int ho = wm * 4096 + lane * 16, wo = (2 + wn) * 4096 + lane * 16;
    bf16x8 ha[2], wa[2], hb[2], wb[2];
    auto rd = [&](bf16x8(&hf)[2], bf16x8(&wf)[2], int slot, int half) {
        LAS char* base = L + slot * G2_SLOT + half * 2048;
#pragma unroll
        for (int j = 0; j < 2; ++j) { hf[j] = *(LAS bf16x8*)(base + ho + j * 1024); wf[j] = *(LAS bf16x8*)(base + wo + j * 1024); }
    };
    auto mm = [&](bf16x8(&hf)[2], bf16x8(&wf)[2]) {
#pragma unroll
        for (int j = 0; j < 2; ++j) acc = FINAL ? MFMA32(wf[j], hf[j], acc) : MFMA32(hf[j], wf[j], acc);
    };
    static_for<G2_NS>([&](auto SI) {
        constexpr int s = SI;
        raw_barrier();
        rd(ha, wa, s % G2_R, 0);
        __builtin_amdgcn_sched_barrier(0);
        if constexpr (s > 0) mm(hb, wb);
        __builtin_amdgcn_sched_barrier(0);
        if constexpr (s < G2_NS - 1) rd(hb, wb, s % G2_R, 1);
        __builtin_amdgcn_sched_barrier(0);
        mm(ha, wa);
        __builtin_amdgcn_sched_barrier(0);
        wait_lgkm0();
    });
    if constexpr (!FINAL) {
        u32x4 o[2];
#pragma unroll
        for (int p = 0; p < 8; ++p) {
            const u32 ob = pk_bf16(acc[2 * p], acc[2 * p + 1]), xv = xr[p >> 2][p & 3];
            o[p >> 2][p & 3] = pk_bf16(bflo(xv) + bflo(ob), bfhi(xv) + bfhi(ob));
        }
        u32x4* dst = (u32x4*)(xout + (32 * cb + pi_fwd(lane & 31)) * SEQ + 32 * pb + 16 * hh);
        dst[0] = o[0]; dst[1] = o[1];
        LAS char* xsb = L + G2_XS;
        LAS u32x4* wp = (LAS u32x4*)(xsb + (32 * wn + (lane & 31)) * G2_XROW + 64 * wm + 32 * hh);
        wp[0] = o[0]; wp[1] = o[1];
        wait_lgkm0();
        raw_barrier();
        LAS char* rp = xsb + (4 * (lane >> 4) + ((lane & 15) >> 2)) * G2_XROW + 32 * wave + 8 * (lane & 3);
        float S = 0.f, Q = 0.f;
#pragma unroll
        for (int i = 0; i < 4; ++i) {
            const u32x2 v = __builtin_bit_cast(u32x2, __builtin_amdgcn_ds_read_tr16_b64_v4bf16((LAS bf16x4*)(rp + 16 * i * G2_XROW)));
            S = dot2(v[0], 0x3f803f80u, S); Q = dot2(v[0], v[0], Q);
            S = dot2(v[1], 0x3f803f80u, S); Q = dot2(v[1], v[1], Q);
        }
        S += __shfl_xor(S, 16); Q += __shfl_xor(Q, 16);
        S += __shfl_xor(S, 32); Q += __shfl_xor(Q, 32);
        if (lane < 16) *(f32x2*)(statsP + 2 * (nt * SEQ + 64 * mt + 16 * wave + lane)) = f32x2{S, Q};
    } else {
        u32x4 o[2];
#pragma unroll
        for (int p = 0; p < 8; ++p) {
            const u32 ob = pk_bf16(acc[2 * p], acc[2 * p + 1]);
            o[p >> 2][p & 3] = pk_bf16(bf2f(xs[2 * p]) + bflo(ob), bf2f(xs[2 * p + 1]) + bfhi(ob));
        }
        const int pos = 32 * pb + pi_fwd(lane & 31);
        const int tok = (pos & ~255) | ((pos & 3) << 6) | ((pos >> 2) & 63);
        u32x4* dst = (u32x4*)(xout + tok * DM + 32 * cb + 16 * hh);
        dst[0] = o[0]; dst[1] = o[1];
    }
}

extern "C" void kernel_launch(void* const* d_in, const int* in_sizes, int n_in, void* d_out, int out_size,
                              void* d_ws, size_t ws_size, hipStream_t stream) {
    (void)in_sizes; (void)n_in; (void)out_size; (void)ws_size;
    const u16* x0 = (const u16*)d_in[0];
    const u16* logA = (const u16*)d_in[1];
    const u16* Aim = (const u16*)d_in[2];
    const u16* Cre = (const u16*)d_in[3];
    const u16* Cim = (const u16*)d_in[4];
    const u16* logdt = (const u16*)d_in[5];
    const u16* Dsk = (const u16*)d_in[6];
    const u16* Wg = (const u16*)d_in[8];
    const u16* Wu = (const u16*)d_in[9];
    const u16* Wd = (const u16*)d_in[10];
    char* ws = (char*)d_ws;
    constexpr size_t MB = 1 << 20;
    u16* xT0 = (u16*)(ws + 0 * MB);
    u16* xT1 = (u16*)(ws + 2 * MB);
    u16* xT2 = (u16*)(ws + 4 * MB);
    u16* xT3 = (u16*)(ws + 6 * MB);
    u16* Kg = (u16*)(ws + 8 * MB);
    u16* W1F = (u16*)(ws + 10 * MB);
    u16* W2F = (u16*)(ws + 13 * MB);
    u16* hF = (u16*)(ws + 15 * MB);
    float* stats0 = (float*)(ws + 21 * MB);
    float* statsP = (float*)(ws + 21 * MB + 65536);

    (void)hipFuncSetAttribute((const void*)gemm1_kernel, hipFuncAttributeMaxDynamicSharedMemorySize, G1_LDS);
    (void)hipFuncSetAttribute((const void*)gemm2_kernel<0>, hipFuncAttributeMaxDynamicSharedMemorySize, G2_LDS);
    (void)hipFuncSetAttribute((const void*)gemm2_kernel<1>, hipFuncAttributeMaxDynamicSharedMemorySize, G2_LDS);

    prep_kernel<<<PREP_BLOCKS, 256, PREP_LDS, stream>>>(x0, logA, Aim, Cre, Cim, logdt, Wg, Wu, Wd, xT0, stats0, Kg, W1F, W2F);
    conv_kernel<1><<<256, 256, CV_LDS, stream>>>(xT0, xT1, stats0, Kg, Dsk);
    gemm1_kernel<<<256, 512, G1_LDS, stream>>>(xT1, W1F, hF);
    gemm2_kernel<0><<<256, 512, G2_LDS, stream>>>(hF, W2F, xT1, xT2, statsP);
    conv_kernel<8><<<256, 256, CV_LDS, stream>>>(xT2, xT3, statsP, Kg, Dsk);
    gemm1_kernel<<<256, 512, G1_LDS, stream>>>(xT3, W1F, hF);
    gemm2_kernel<1><<<256, 512, G2_LDS, stream>>>(hF, W2F, xT3, (u16*)d_out, statsP);
}
```

```cpp
#include <hip/hip_runtime.h>
#include <hip/hip_bf16.h>
#include <stdint.h>
#include <utility>

typedef unsigned short u16;
typedef unsigned int u32;
typedef __bf16 bf16x8 __attribute__((ext_vector_type(8)));
typedef __bf16 bf16x4 __attribute__((ext_vector_type(4)));
typedef __bf16 bf16x2 __attribute__((ext_vector_type(2)));
typedef _Float16 f16x8 __attribute__((ext_vector_type(8)));
typedef _Float16 f16x2 __attribute__((ext_vector_type(2)));
typedef float f32x16 __attribute__((ext_vector_type(16)));
typedef float f32x4 __attribute__((ext_vector_type(4)));
typedef float f32x2 __attribute__((ext_vector_type(2)));
typedef double f64x2 __attribute__((ext_vector_type(2)));
typedef u32 u32x4 __attribute__((ext_vector_type(4)));
typedef u32 u32x2 __attribute__((ext_vector_type(2)));

#define LAS __attribute__((address_space(3)))
#define GAS __attribute__((address_space(1)))
#define DI __device__ __forceinline__

constexpr int SEQ = 2048, DM = 512, NST = 128, FF = 1376, NU = FF / 16;
constexpr int XTILE = DM * 128;

constexpr size_t MB = 1 << 20;
constexpr size_t WS_XSTEP = 2 * MB, WS_HSTEP = 6 * MB;
constexpr size_t WS_X = 0, WS_K = 8 * MB, WS_W1 = 10 * MB, WS_W2 = 13 * MB, WS_LN = 15 * MB - 262144, WS_H = 15 * MB;
constexpr size_t WS_CSP = WS_LN + 32768;
constexpr size_t WS_CS = WS_LN + 98304;
constexpr size_t WS_ZERO = WS_LN + 131072;
constexpr size_t WS_ST64 = WS_ZERO, WS_LNW = WS_ZERO + SEQ * 16;
constexpr int ZERO_BYTES = SEQ * 16 + 2 * SEQ * 4;

#define TG1 4
#define TG2 8

#define ST_DECL do {} while (0)
#define ST(i) do {} while (0)
#define ST_FLUSH(kid) do {} while (0)

DI float bf2f(u16 h) { return __uint_as_float((u32)h << 16); }
DI float bflo(u32 w) { return __uint_as_float(w << 16); }
DI float bfhi(u32 w) { return __uint_as_float(w & 0xffff0000u); }
DI u32 pk_bf16(f32x2 v) { return __builtin_bit_cast(u32, __builtin_convertvector(v, bf16x2)); }
DI u32 pk_bf16(float lo, float hi) { return pk_bf16(f32x2{lo, hi}); }
DI float rbf(float x) { return bflo(pk_bf16(x, x)); }
DI u32 pk_f16(f32x2 v) { return __builtin_bit_cast(u32, __builtin_convertvector(v, f16x2)); }
DI float dot2(u32 a, u32 b, float c) { return __builtin_amdgcn_fdot2_f32_bf16(__builtin_bit_cast(bf16x2, a), __builtin_bit_cast(bf16x2, b), c, false); }
DI u32 perm_lo(u32 hi, u32 lo) { return __builtin_amdgcn_perm(hi, lo, 0x05040100u); }
DI u32 perm_hi(u32 hi, u32 lo) { return __builtin_amdgcn_perm(hi, lo, 0x07060302u); }
DI int pi_fwd(int r) { return (((r >> 2) & 1) << 4) | ((r >> 3) << 2) | (r & 3); }
DI int pi_inv(int p) { return (((p >> 2) & 3) << 3) | ((p >> 4) << 2) | (p & 3); }
DI int xt_off(int ch, int pos) { return (pos >> 7) * XTILE + ch * 128 + (pos & 127); }
DI int st_thread(int pos) { return ((pos >> 8) << 5) | (((pos >> 5) & 7) << 2) | ((pos >> 2) & 3); }
DI int st_item(int pos) { return (((pos >> 4) & 1) << 2) | (pos & 3); }
DI int st_slot(int thread, int item, int nitem) { return ((thread >> 6) * nitem + item) * 64 + (thread & 63); }
constexpr u32 ONES = 0x3f803f80u;

template <int N> DI void wait_vm() { asm volatile("s_waitcnt vmcnt(%0)" ::"n"(N) : "memory"); }
DI void wait_lgkm0() { asm volatile("s_waitcnt lgkmcnt(0)" ::: "memory"); }
DI void raw_barrier() { asm volatile("" ::: "memory"); __builtin_amdgcn_s_barrier(); asm volatile("" ::: "memory"); }
DI float opaque(float v) { asm("" : "+v"(v)); return v; }
#define PIN16(x) asm volatile("" : "+v"(x))
DI void sched_fence() { __builtin_amdgcn_sched_barrier(0); }
DI void glds16(const void* src, LAS char* dst) {
    __builtin_amdgcn_global_load_lds((const GAS void*)src, (LAS void*)dst, 16, 0, 0);
}
DI bf16x8 tr_frag(LAS char* a0, LAS char* a1) {
    bf16x4 lo = __builtin_amdgcn_ds_read_tr16_b64_v4bf16((LAS bf16x4*)a0);
    bf16x4 hi = __builtin_amdgcn_ds_read_tr16_b64_v4bf16((LAS bf16x4*)a1);
    return __builtin_shufflevector(lo, hi, 0, 1, 2, 3, 4, 5, 6, 7);
}
template <int N> DI void glds_s(const char* ubase, u32 voff, u32 lds_dst) {
    u32 keep;
    if constexpr (N == 1)
        asm volatile("s_mov_b32 %0, m0\n\ts_mov_b32 m0, %3\n\ts_nop 0\n\tglobal_load_lds_dwordx4 %1, %2\n\ts_mov_b32 m0, %0"
                     : "=&s"(keep) : "v"(voff), "s"(ubase), "s"(lds_dst) : "memory");
    else if constexpr (N == 2)
        asm volatile("s_mov_b32 %0, m0\n\ts_mov_b32 m0, %3\n\ts_nop 0\n\tglobal_load_lds_dwordx4 %1, %2\n\t"
                     "global_load_lds_dwordx4 %1, %2 offset:1024\n\ts_mov_b32 m0, %0"
                     : "=&s"(keep) : "v"(voff), "s"(ubase), "s"(lds_dst) : "memory");
    else
        asm volatile("s_mov_b32 %0, m0\n\ts_mov_b32 m0, %3\n\ts_nop 0\n\tglobal_load_lds_dwordx4 %1, %2\n\t"
                     "global_load_lds_dwordx4 %1, %2 offset:1024\n\tglobal_load_lds_dwordx4 %1, %2 offset:2048\n\t"
                     "global_load_lds_dwordx4 %1, %2 offset:3072\n\ts_mov_b32 m0, %0"
                     : "=&s"(keep) : "v"(voff), "s"(ubase), "s"(lds_dst) : "memory");
}
typedef __amdgpu_buffer_rsrc_t rsrc_t;
DI rsrc_t make_rsrc(void* base) { return __builtin_amdgcn_make_buffer_rsrc(base, 0, 0x7fffffff, 0x00020000); }
template <int AUX = 16> DI void st16(rsrc_t r, u32 voff, u32 soff, u32x4 v) { __builtin_amdgcn_raw_buffer_store_b128(v, r, voff + soff, 0, AUX); }
template <int AUX = 16> DI void st8(rsrc_t r, u32 voff, u32 soff, u32x2 v) { __builtin_amdgcn_raw_buffer_store_b64(v, r, voff, soff, AUX); }
#define H_AUX 16
#define MFMA16(a, b, c) __builtin_amdgcn_mfma_f32_16x16x32_bf16((a), (b), (c), 0, 0, 0)
#define MFMA32(a, b, c) __builtin_amdgcn_mfma_f32_32x32x16_bf16((a), (b), (c), 0, 0, 0)

template <class F, int... I> DI void static_for_impl(F&& f, std::integer_sequence<int, I...>) { (f(std::integral_constant<int, I>{}), ...); }
template <int N, class F> DI void static_for(F&& f) { static_for_impl(f, std::make_integer_sequence<int, N>{}); }

template <int CTRL, int RM = 0xf> DI float dppz(float v) { return __int_as_float(__builtin_amdgcn_update_dpp(0, __float_as_int(v), CTRL, RM, 0xf, true)); }
DI float wave_sum63(float v) {
    v += dppz<0x111>(v); v += dppz<0x112>(v); v += dppz<0x114>(v); v += dppz<0x118>(v);
    v += dppz<0x142, 0xa>(v); v += dppz<0x143, 0xc>(v);
    return v;
}
DI float wave_max63(float v) {
    v = fmaxf(v, dppz<0x111>(v)); v = fmaxf(v, dppz<0x112>(v)); v = fmaxf(v, dppz<0x114>(v)); v = fmaxf(v, dppz<0x118>(v));
    v = fmaxf(v, dppz<0x142, 0xa>(v)); v = fmaxf(v, dppz<0x143, 0xc>(v));
    return v;
}
DI float rdlane63(float v) { return __int_as_float(__builtin_amdgcn_readlane(__float_as_int(v), 63)); }
DI float half_sum2(float a, float b) {
    auto r = __builtin_amdgcn_permlane32_swap(__float_as_uint(a), __float_as_uint(b), false, false);
    return __uint_as_float(r[0]) + __uint_as_float(r[1]);
}
DI float row_pair_sum(float a) {
    auto r = __builtin_amdgcn_permlane16_swap(__float_as_uint(a), __float_as_uint(a), false, false);
    return __uint_as_float(r[0]) + __uint_as_float(r[1]);
}
DI void ln_scalars(float S, float Q, float& mb, float& sb) {
    const float mean = S * (1.f / DM);
    const float var = fmaxf(__builtin_fmaf(-mean, mean, Q * (1.f / DM)), 0.f);
    const u32 mv = pk_bf16(mean, var);
    mb = bflo(mv);
    sb = rbf(__builtin_amdgcn_sqrtf(rbf(bfhi(mv) + 1e-5f)));
}

DI f32x2 cmul(f32x2 a, f32x2 b) {
    f32x2 t, r;
    asm("v_pk_mul_f32 %0, %1, %2 op_sel_hi:[0,1]" : "=v"(t) : "v"(a), "v"(b));
    asm("v_pk_fma_f32 %0, %1, %2, %3 op_sel:[1,1,0] op_sel_hi:[1,0,1] neg_lo:[1,0,0]" : "=v"(r) : "v"(a), "v"(b), "v"(t));
    return r;
}
DI f32x2 cexp_p(float arl, float aiv, float p) {
    const float mag = __builtin_amdgcn_exp2f(arl * p);
    const float th = __builtin_amdgcn_fractf(aiv * p);
    return f32x2{mag * __builtin_amdgcn_cosf(th), mag * __builtin_amdgcn_sinf(th)};
}
DI float exp_bf(float x) {
    const float t = x * 1.44269504088896341f;
    const float r = __builtin_fmaf(x, 1.44269504088896341f, -t) + x * 1.92596299e-8f;
    const float e = __builtin_amdgcn_exp2f(t);
    return rbf(__builtin_fmaf(e * r, 0.693147181f, e));
}

constexpr int KG_ROW = 528;
constexpr int KG_B = 21 * KG_ROW, KG_TAB = 53 * KG_ROW, PREP_LDS = KG_TAB + 2320 * 2;
constexpr int PB_KGEN = 512, PB_W1 = 176, PB_W2 = 88, PB_TR = 128, PB_ST = 128;
constexpr int PREP_BLOCKS = PB_KGEN + PB_TR + PB_ST + PB_W1 + PB_W2;

__global__ __launch_bounds__(256, 2) void prep_kernel(
    const u16* __restrict__ x0, const u16* __restrict__ logA, const u16* __restrict__ Aim,
    const u16* __restrict__ Cre, const u16* __restrict__ Cim, const u16* __restrict__ logdt, const u16* __restrict__ Dskip,
    const u16* __restrict__ Wg, const u16* __restrict__ Wu, const u16* __restrict__ Wd, char* __restrict__ ws) {
    extern __shared__ __attribute__((aligned(16))) char smem_raw[];
    LAS char* L = (LAS char*)smem_raw;
    asm volatile("" ::"s"(x0), "s"(logA), "s"(Aim), "s"(Cre), "s"(Cim), "s"(logdt), "s"(Dskip), "s"(Wg), "s"(Wu), "s"(Wd), "s"(ws));
    const rsrc_t WS = make_rsrc(ws);
    const int tid = threadIdx.x, lane = tid & 63;
    const int wave = __builtin_amdgcn_readfirstlane(tid >> 6);
    int b = blockIdx.x;
    ST_DECL;
    if (b < PB_KGEN) {
        const int d = b, n = tid & 127;
        const u16 h_dt = logdt[d], h_D = Dskip[d], h_la = logA[d * NST + n], h_ai = Aim[d * NST + n];
        const u16 h_cr = Cre[d * NST + n], h_ci = Cim[d * NST + n];
        const u32 ca = ((const u32*)(Cre + d * NST))[lane], cc = ((const u32*)(Cim + d * NST))[lane];
        const float dt = exp_bf(bf2f(h_dt));
        const float arl = -exp_bf(bf2f(h_la)) * dt * 1.44269504088896341f;
        float aiv = bf2f(h_ai) * dt * 0.159154943091895336f;
        float mx = fmaxf(fmaxf(fabsf(bflo(ca)), fabsf(bfhi(ca))), fmaxf(fabsf(bflo(cc)), fabsf(bfhi(cc))));
        mx = rdlane63(wave_max63(mx));
        ST(1);
        const bool okm = mx > 1e-30f && mx < 1e30f;
        const float sc = okm ? 256.f * __builtin_amdgcn_rcpf(mx) : 1.f;
        const float back = dt * (2.f / 256.f) / sc;
        if (wave < 2) {
            aiv = -aiv;
            const f32x2 t0 = {bf2f(h_cr) * sc, -bf2f(h_ci) * sc};
            const f32x2 w1 = cexp_p(arl, aiv, 1.f), v = cexp_p(arl, aiv, 256.f);
            const f32x2 w2 = cmul(w1, w1), w4 = cmul(w2, w2);
            LAS u32* A = (LAS u32*)(L + 4 * n);
            auto put = [&](int row, f32x2 z) { A[row * (KG_ROW / 4)] = pk_f16(z); };
            const f32x2 r1 = cmul(t0, w1), r2 = cmul(t0, w2), r3 = cmul(r1, w2);
            put(0, t0); put(1, r1); put(2, r2); put(3, r3);
            put(4, cmul(t0, w4)); put(5, cmul(r1, w4)); put(6, cmul(r2, w4)); put(7, cmul(r3, w4));
            f32x2 t = cmul(t0, v);
            const f32x2 r9 = cmul(t, w2);
            put(8, t); put(9, r9); put(10, cmul(t, w4)); put(11, cmul(r9, w4));
            t = cmul(t, v); put(12, t); put(13, cmul(t, w4));
            t = cmul(t, v); put(14, t); put(15, cmul(t, w4));
#pragma unroll
            for (int r = 16; r <= 20; ++r) { t = cmul(t, v); put(r, t); }
        } else {
            f32x2 e[16];
            const f32x2 b1 = cexp_p(arl, aiv, 8.f), b8 = cexp_p(arl, aiv, 64.f);
            const f32x2 b2 = cmul(b1, b1), b4 = cmul(b2, b2), b16 = cmul(b8, b8);
            LAS u32* B = (LAS u32*)(L + KG_B + 4 * n);
            e[0] = f32x2{256.f, 0.f}; e[1] = b1 * 256.f; e[2] = b2 * 256.f; e[3] = cmul(e[1], b2);
            e[4] = b4 * 256.f; e[8] = b8 * 256.f;
#pragma unroll
            for (int c = 1; c < 4; ++c) e[4 + c] = cmul(e[c], b4);
#pragma unroll
            for (int c = 1; c < 8; ++c) e[8 + c] = cmul(e[c], b8);
#pragma unroll
            for (int c = 0; c < 16; ++c) {
                B[c * (KG_ROW / 4)] = pk_f16(e[c]);
                B[(16 + c) * (KG_ROW / 4)] = pk_f16(c ? cmul(e[c], b16) : b16 * 256.f);
            }
        }
        const int rb = wave & 1, cb = wave >> 1, q = lane >> 4;
        const int col8 = 8 * (16 * cb + (lane & 15));
        int ph[4];
#pragma unroll
        for (int j = 0; j < 4; ++j)
            ph[j] = col8 + (rb ? (q == 0 ? 1024 + 256 * j : (q == 1 && j == 0) ? 2048 : 2049)
                               : (q < 2 ? 4 * q + j : q == 2 ? 256 + 2 * j : 512 + 256 * (j >> 1) + 4 * (j & 1)));
        ST(2);
        __syncthreads();
        ST(3);
        {
            LAS char* pa = L + (16 * rb + (lane & 15)) * KG_ROW + q * 16;
            LAS char* pb = L + KG_B + (16 * cb + (lane & 15)) * KG_ROW + q * 16;
            f32x4 acc = {0.f, 0.f, 0.f, 0.f}, acc2 = {0.f, 0.f, 0.f, 0.f};
#pragma unroll
            for (int ks = 0; ks < 8; ks += 2) {
                acc = __builtin_amdgcn_mfma_f32_16x16x32_f16(*(LAS f16x8*)(pa + 64 * ks), *(LAS f16x8*)(pb + 64 * ks), acc, 0, 0, 0);
                acc2 = __builtin_amdgcn_mfma_f32_16x16x32_f16(*(LAS f16x8*)(pa + 64 * ks + 64), *(LAS f16x8*)(pb + 64 * ks + 64), acc2, 0, 0, 0);
            }
            acc += acc2;
            LAS u16* tab = (LAS u16*)(L + KG_TAB);
            const float add0 = tid == 0 ? bf2f(h_D) : 0.f;
            const u32 v01 = pk_bf16(__builtin_fmaf(acc[0], back, add0), acc[1] * back), v23 = pk_bf16(acc[2] * back, acc[3] * back);
            tab[ph[0]] = (u16)v01; tab[ph[1]] = (u16)(v01 >> 16);
            tab[ph[2]] = (u16)v23; tab[ph[3]] = (u16)(v23 >> 16);
        }
        ST(4);
        __syncthreads();
        ST(5);
        {
            const u32x4 w = *(LAS u32x4*)(L + KG_TAB + 16 * tid);
            const u32 w4 = *(LAS u32*)(L + KG_TAB + 16 * tid + 16);
            u32x4 o;
            if (wave == 0) {
                const u32x4 s1 = {perm_lo(w[0], w[0]), perm_lo(w[2], w[1]), perm_lo(w[2], w[2]), perm_lo(w4, w[3])};
                o = lane < 32 ? w : s1;
            } else if (wave == 1) {
                o = u32x4{perm_lo(w[0], w[0]), perm_lo(w[2], w[0]), perm_lo(w[2], w[2]), perm_lo(w4, w4)};
            } else {
                const u32 lo = perm_lo(w[0], w[0]);
                o = u32x4{lo, lo, perm_lo(w4, (lane & 1) ? w4 : w[0]), perm_lo(w4, w4)};
            }
            st16(WS, 16 * tid, WS_K + d * SEQ * 2, o);
        }
        ST(6);
        ST_FLUSH(0);
        return;
    }
    b -= PB_KGEN;
    if (b < PB_W1 + PB_W2) {
        const bool is1 = b < PB_W1;
        const int bb = is1 ? b : b - PB_W1;
        const int ct = is1 ? bb >> 3 : bb & 7, ty = (bb >> 2) & 1, rt = is1 ? bb & 3 : bb >> 3;
        const int pitch = is1 ? FF * 2 : DM * 2, nrow = is1 ? DM : FF;
        const char* W = (const char*)(is1 ? (ty ? Wu : Wg) : Wd);
        const int cbytes = is1 && ct == 21 ? 64 : 128;
        {
            const int r0 = 128 * rt + 32 * wave + (lane >> 3);
            const int co = ct * 128 + (((lane & 7) * 16) & (cbytes - 1));
#pragma unroll
            for (int i = 0; i < 4; ++i)
                glds_s<1>(W, (u32)(min(r0 + 8 * i, nrow - 1) * pitch + co), (u32)(uintptr_t)L + wave * 4096 + i * 1024);
        }
        ST(1);
        wait_vm<0>();
        ST(2);
        const u32 lb = (u32)(uintptr_t)L + wave * 4096;
        const int g = lane >> 4, q = (lane & 15) >> 2, pp = lane & 3;
        u32x2 f[8];
        if (is1) {
            const u32 a = lb + (8 * g + q) * 128 + pp * 8;
            asm volatile("ds_read_b64_tr_b16 %0, %8\n\tds_read_b64_tr_b16 %1, %8 offset:512\n\t"
                         "ds_read_b64_tr_b16 %2, %8 offset:32\n\tds_read_b64_tr_b16 %3, %8 offset:544\n\t"
                         "ds_read_b64_tr_b16 %4, %8 offset:64\n\tds_read_b64_tr_b16 %5, %8 offset:576\n\t"
                         "ds_read_b64_tr_b16 %6, %8 offset:96\n\tds_read_b64_tr_b16 %7, %8 offset:608\n\ts_waitcnt lgkmcnt(0)"
                         : "=&v"(f[0]), "=&v"(f[1]), "=&v"(f[2]), "=&v"(f[3]), "=&v"(f[4]), "=&v"(f[5]), "=&v"(f[6]), "=&v"(f[7])
                         : "v"(a) : "memory");
            LAS float* red = (LAS float*)(L + 16384);
#pragma unroll
            for (int i = 0; i < 4; ++i) {
                const int u = 4 * ct + i;
                if (u < NU) st16(WS, lane * 16, WS_W1 + ((u * 16 + 4 * rt + wave) * 2 + ty) * 1024, u32x4{f[2 * i][0], f[2 * i][1], f[2 * i + 1][0], f[2 * i + 1][1]});
            }
#pragma unroll
            for (int i = 0; i < 4; i += 2) {
                const float s0 = dot2(f[2 * i][0], ONES, dot2(f[2 * i][1], ONES, dot2(f[2 * i + 1][0], ONES, dot2(f[2 * i + 1][1], ONES, 0.f))));
                const float s1 = dot2(f[2 * i + 2][0], ONES, dot2(f[2 * i + 2][1], ONES, dot2(f[2 * i + 3][0], ONES, dot2(f[2 * i + 3][1], ONES, 0.f))));
                const float v = row_pair_sum(half_sum2(s0, s1));
                if ((lane & 16) == 0) red[wave * 64 + (i + (lane >> 5)) * 16 + (lane & 15)] = v;
            }
            __syncthreads();
            if (tid < 64 && 4 * ct + (tid >> 4) < NU)
                ((float*)(ws + WS_CSP))[((rt * NU + 4 * ct + (tid >> 4)) * 2 + ty) * 16 + (tid & 15)] =
                    (red[tid] + red[64 + tid]) + (red[128 + tid] + red[192 + tid]);
        } else {
            const u32 a = lb + (8 * (g >> 1) + q) * 128 + pi_fwd(16 * (g & 1) + 4 * pp) * 2;
            asm volatile("ds_read_b64_tr_b16 %0, %8\n\tds_read_b64_tr_b16 %1, %8 offset:512\n\t"
                         "ds_read_b64_tr_b16 %2, %8 offset:64\n\tds_read_b64_tr_b16 %3, %8 offset:576\n\t"
                         "ds_read_b64_tr_b16 %4, %8 offset:2048\n\tds_read_b64_tr_b16 %5, %8 offset:2560\n\t"
                         "ds_read_b64_tr_b16 %6, %8 offset:2112\n\tds_read_b64_tr_b16 %7, %8 offset:2624\n\ts_waitcnt lgkmcnt(0)"
                         : "=&v"(f[0]), "=&v"(f[1]), "=&v"(f[2]), "=&v"(f[3]), "=&v"(f[4]), "=&v"(f[5]), "=&v"(f[6]), "=&v"(f[7])
                         : "v"(a) : "memory");
#pragma unroll
            for (int i = 0; i < 4; ++i) {
                const int u = 8 * rt + 2 * wave + (i >> 1), cb = 2 * ct + (i & 1);
                if (u < NU) st16(WS, lane * 16, WS_W2 + (cb * NU + u) * 1024, u32x4{f[2 * i][0], f[2 * i][1], f[2 * i + 1][0], f[2 * i + 1][1]});
            }
        }
        ST(3);
        ST_FLUSH(0);
        return;
    }
    b -= PB_W1 + PB_W2;
    if (b < PB_TR) {
        const int w = b * 4 + wave, tau = w >> 6, lg = (w >> 4) & 3, cgg = w & 15;
        const int ln = lg * 16 + (lane & 15), cg = cgg * 4 + (lane >> 4);
        u32x4 in[4];
#pragma unroll
        for (int g = 0; g < 4; ++g) in[g] = *(const u32x4*)(x0 + (256 * tau + 64 * g + ln) * DM + 8 * cg);
        const u32 dst = 2 * xt_off(8 * cg, 256 * tau + 4 * ln);
#pragma unroll
        for (int q = 0; q < 4; ++q) {
            st8(WS, dst + (2 * q) * 256, WS_X, u32x2{perm_lo(in[1][q], in[0][q]), perm_lo(in[3][q], in[2][q])});
            st8(WS, dst + (2 * q + 1) * 256, WS_X, u32x2{perm_hi(in[1][q], in[0][q]), perm_hi(in[3][q], in[2][q])});
        }
        ST_FLUSH(0);
        return;
    }
    b -= PB_TR;
    {
        const int w = b * 4 + wave, tau = w >> 6, ln = w & 63;
        u32x4 in[4];
#pragma unroll
        for (int g = 0; g < 4; ++g) in[g] = *(const u32x4*)(x0 + (256 * tau + 64 * g + ln) * DM + 8 * lane);
        if ((b * 256 + tid) * 16 < ZERO_BYTES) st16(WS, (b * 256 + tid) * 16, WS_ZERO, u32x4{0u, 0u, 0u, 0u});
        float o[8];
#pragma unroll
        for (int g = 0; g < 4; ++g) {
            float S = 0.f, Q = 0.f;
#pragma unroll
            for (int q = 0; q < 4; ++q) { S = dot2(in[g][q], ONES, S); Q = dot2(in[g][q], in[g][q], Q); }
            S = wave_sum63(S); Q = wave_sum63(Q);
            float mb, sb;
            ln_scalars(S, Q, mb, sb);
            o[2 * g] = __builtin_amdgcn_rcpf(sb);
            o[2 * g + 1] = -mb * o[2 * g];
        }
        if (lane == 63) {
            const int pos = 256 * tau + 4 * ln;
            const u32 dst = 16 * st_slot(st_thread(pos), st_item(pos) >> 1, 4);
            st16(WS, dst, WS_LN, __builtin_bit_cast(u32x4, f32x4{o[0], o[1], o[2], o[3]}));
            st16(WS, dst + 1024, WS_LN, __builtin_bit_cast(u32x4, f32x4{o[4], o[5], o[6], o[7]}));
        }
        ST_FLUSH(0);
    }
}

constexpr int CV_KROW = (64 + SEQ) * 2;
constexpr int CV_ZRS = 84 * 16;
constexpr int CV_Z = 2 * CV_KROW;
constexpr int CV_C = CV_Z + 2 * 4 * CV_ZRS;
constexpr int CV_DUMP = CV_C + 2 * 33 * 16;
constexpr int CV_X = CV_DUMP + 1024 + 33 * 16;
constexpr int CV_S = CV_X + 2 * SEQ * 2;
constexpr int CV_LDS = CV_S + SEQ * 16;
#define PF_SLEEP 12
#define CV_GRID 512

template <int HALF> struct TauSet { static constexpr int t(int i) { return HALF == 0 ? (i == 0 ? 0 : i == 1 ? 2 : i == 2 ? 5 : 7) : (i == 0 ? 1 : i == 1 ? 3 : i == 2 ? 4 : 6); } };

template <int HALF>
DI void conv_main(LAS char* L, rsrc_t xout, u32 xoff, int ch, int lane
) {
    using TS = TauSet<HALF>;
    constexpr int TMAX = TS::t(3);
    f32x16 acc[4];
#pragma unroll
    for (int i = 0; i < 4; ++i)
#pragma unroll
        for (int r = 0; r < 16; ++r) acc[i][r] = 0.f;
    const int hh = lane >> 5;
    LAS char* zb = L + CV_Z + (ch * 4 + (lane & 3)) * CV_ZRS + 128 + 16 * (((lane & 31) >> 2) + hh) - 16 * 7;
    LAS char* kb = L + ch * CV_KROW + 128 + 2 * (16 * ((lane >> 4) & 1) - 32 * hh - 4 * ((lane & 15) >> 2) + 4 * (lane & 3)) - 32;
    const int cbase = CV_C + ch * 33 * 16;
    LAS char* cw = L + (lane == 63 ? cbase + 16 : CV_DUMP + lane * 16);
    LAS char* cr[4];
#pragma unroll
    for (int i = 0; i < 4; ++i) cr[i] = L + (lane < 3 ? cbase + 4 + 4 * lane + 64 * TS::t(i) : 0);
    u32x2 xm[4];
    bf16x8 kf[2][TMAX + 1], zf[2][TMAX + 1];
    auto load = [&](auto EI) {
        constexpr int ei = EI, e = 2 * ei + 1;
#pragma unroll
        for (int m = 0; m <= TMAX; ++m) {
            kf[ei & 1][m] = tr_frag(kb + 32 + 64 * (8 * m + e), kb + 64 * (8 * m + e));
            zf[ei & 1][m] = *(LAS bf16x8*)(zb + 16 * (8 * m + 7 - e));
            if constexpr (ei == 0) sched_fence();
        }
    };
    load(std::integral_constant<int, 0>{});
    static_for<3>([&](auto EI) {
        constexpr int ei = EI;
        sched_fence();
        load(std::integral_constant<int, ei + 1>{});
        if constexpr (ei == 2) {
#pragma unroll
            for (int i = 0; i < 4; ++i) xm[i] = *(LAS u32x2*)(L + CV_X + ch * 4096 + 512 * TS::t(i) + 8 * lane);
        }
#pragma unroll
        for (int i = 0; i < 4; ++i)
#pragma unroll
            for (int mu = 0; mu <= TS::t(i); ++mu) acc[i] = MFMA32(zf[ei & 1][TS::t(i) - mu], kf[ei & 1][mu], acc[i]);
        if constexpr (ei == 0) __builtin_amdgcn_sched_group_barrier(0x8, 5, 0);
#pragma unroll
        for (int k = 0; k < 13; ++k) { __builtin_amdgcn_sched_group_barrier(0x8, 1, 0); __builtin_amdgcn_sched_group_barrier(0x100, 2, 0); }
        PIN16(acc[0]); PIN16(acc[1]); PIN16(acc[2]); PIN16(acc[3]);
    });
    sched_fence();
    float y[4][4];
    auto post = [&](auto II) {
        constexpr int i = II;
        float s1[4], s2[4];
#pragma unroll
        for (int g = 0; g < 4; ++g) {
            s2[g] = opaque(acc[i][4 * g + 2] + dppz<0x138>(acc[i][4 * g + 3]));
            y[i][g] = opaque(acc[i][4 * g] + ((g & 1) ? bfhi(xm[i][g >> 1]) : bflo(xm[i][g >> 1])));
        }
#pragma unroll
        for (int g = 0; g < 4; ++g) s1[g] = opaque(acc[i][4 * g + 1] + dppz<0x138>(s2[g]));
#pragma unroll
        for (int g = 0; g < 4; ++g) {
            y[i][g] = opaque(y[i][g] + dppz<0x138>(s1[g]));
            *(LAS f32x4*)(cw + (4 * TS::t(i) + g) * 16) = f32x4{y[i][g], s1[g], s2[g], acc[i][4 * g + 3]};
        }
    };
    static_for<4>([&](auto II) {
        constexpr int i = II;
#pragma unroll
        for (int mu = 0; mu <= TS::t(i); ++mu) acc[i] = MFMA32(zf[1][TS::t(i) - mu], kf[1][mu], acc[i]);
        if constexpr (i > 0) {
            post(std::integral_constant<int, i - 1>{});
            __builtin_amdgcn_sched_group_barrier(0x8, 2, 0);
#pragma unroll
            for (int k = 2; k <= TS::t(i); ++k) { __builtin_amdgcn_sched_group_barrier(0x202, 6, 0); __builtin_amdgcn_sched_group_barrier(0x8, 1, 0); }
        }
        PIN16(acc[i]);
        sched_fence();
    });
    ST(4);
    post(std::integral_constant<int, 3>{});
    ST(5);
    wait_lgkm0();
    raw_barrier();
    ST(6);
    float cv[4][4];
#pragma unroll
    for (int i = 0; i < 4; ++i)
#pragma unroll
        for (int g = 0; g < 4; ++g) cv[i][g] = *(LAS float*)(cr[i] + 16 * g);
    static_for<4>([&](auto II) {
        constexpr int i = II;
        st8(xout, xoff, 4 * TS::t(i) * XTILE, u32x2{pk_bf16(opaque(TS::t(i) ? y[i][0] + cv[i][0] : y[i][0]), opaque(y[i][1] + cv[i][1])),
                                                    pk_bf16(opaque(y[i][2] + cv[i][2]), opaque(y[i][3] + cv[i][3]))});
    });
}

__global__ __launch_bounds__(256, 2) void conv_kernel(char* ws, int layer) {
    extern __shared__ __attribute__((aligned(16))) char smem_raw[];
    LAS char* L = (LAS char*)smem_raw;
    asm volatile("" ::"s"(ws), "s"(layer));
    const int tid = threadIdx.x, lane = tid & 63, wave = __builtin_amdgcn_readfirstlane(tid >> 6);
    if (blockIdx.x >= 256) {
        const int xcd = blockIdx.x & 7, sl = ((blockIdx.x - 256) >> 3) * 4 + wave;
        constexpr int P1 = (96 / (8 / TG1)) * 32, P2 = 16 * NU / (8 / TG2);
        constexpr int N1 = (P1 + 127) / 128, N2 = (P2 + 127) / 128;
        const int k1 = (xcd / TG1) * P1 + sl * N1, k2 = (xcd / TG2) * P2 + sl * N2;
        const u32 dst = (u32)(uintptr_t)L + wave * 1024;
        if (layer == 0 && blockIdx.x < 256 + 3) {
            const int i = (blockIdx.x - 256) * 256 + tid;
            const f32x4* cp = (const f32x4*)(ws + WS_CSP) + i;
            if (i < NU * 8) ((f32x4*)(ws + WS_CS))[i] = (cp[0] + cp[NU * 8]) + (cp[2 * NU * 8] + cp[3 * NU * 8]);
        }
        __builtin_amdgcn_s_sleep(PF_SLEEP);
#pragma unroll 1
        for (int i = 0; i < N1; ++i) glds_s<1>(ws + WS_W1 + (size_t)min(k1 + i, NU * 32 - 1) * 1024, lane * 16, dst);
#pragma unroll 1
        for (int i = 0; i < N2; ++i) glds_s<1>(ws + WS_W2 + (size_t)min(k2 + i, 16 * NU - 1) * 1024, lane * 16, dst);
        wait_vm<0>();
        return;
    }
    const u16* xin = (const u16*)(ws + WS_X + 2 * layer * WS_XSTEP);
    const rsrc_t xout = make_rsrc(ws + WS_X + (2 * layer + 1) * WS_XSTEP);
    const int ch = wave >> 1, half = wave & 1, d0 = 2 * blockIdx.x, d = d0 + ch;
    ST_DECL;
    const int tau = tid >> 5, c = tid & 3, sp = (tid >> 2) & 7;
    const int p0 = 256 * tau + 32 * sp + 4 * c;
    const u32 l0 = (u32)(uintptr_t)L;
    {
        const int n = layer ? 8 : 4, rot = blockIdx.x >> 3;
        const char* sb = ws + (layer ? WS_ST64 : WS_LN) + wave * n * 1024;
#pragma unroll 1
        for (int j = 0; j < n; ++j) {
            const int k = (j + rot) & (n - 1);
            glds_s<1>(sb + k * 1024, lane * 16, l0 + CV_S + wave * 8192 + k * 1024);
        }
    }
    {
        const char* xb = (const char*)(xin + 4 * wave * XTILE + d0 * 128);
        const u32 vo = (lane >> 4) * (XTILE * 2) + (lane & 15) * 16;
        glds_s<1>(xb, vo, l0 + CV_X + wave * 1024);
        glds_s<1>(xb + 256, vo, l0 + CV_X + 4096 + wave * 1024);
    }
    glds_s<2>(ws + WS_K + (size_t)(d * SEQ + half * 1024) * 2, lane * 16, l0 + ch * CV_KROW + 128 + half * 2048);
    const u32 xoff = 2 * xt_off(d, 4 * lane);
    if (wave == 0) *(LAS u32x4*)(L + CV_Z + (lane >> 3) * CV_ZRS + 16 * (lane & 7)) = u32x4{0u, 0u, 0u, 0u};
    if (wave == 1) *(LAS u32x4*)(L + ((lane >> 3) & 1) * CV_KROW + 16 * (lane & 7)) = u32x4{0u, 0u, 0u, 0u};
    ST(1);
    f32x2 rc[2][4];
    LAS char* sl = L + CV_S + wave * 8192 + lane * 16;
    wait_vm<4>();
    ST(8);
    if (layer != 0) {
#pragma unroll
        for (int j = 0; j < 8; ++j) {
            const f64x2 sq = *(LAS f64x2*)(sl + j * 1024);
            float mb, sb;
            ln_scalars((float)sq[0], (float)sq[1], mb, sb);
            const float rs = __builtin_amdgcn_rcpf(sb);
            rc[j >> 2][j & 3] = f32x2{rs, -mb * rs};
        }
    } else {
#pragma unroll
        for (int j = 0; j < 4; ++j) {
            const f32x4 v = *(LAS f32x4*)(sl + j * 1024);
            rc[j >> 1][2 * (j & 1)] = f32x2{v[0], v[1]}; rc[j >> 1][2 * (j & 1) + 1] = f32x2{v[2], v[3]};
        }
    }
    wait_vm<2>();
    u32x2 xz[2][2];
#pragma unroll
    for (int k = 0; k < 2; ++k)
#pragma unroll
        for (int s = 0; s < 2; ++s) xz[k][s] = *(LAS u32x2*)(L + CV_X + k * 4096 + (p0 + 16 * s) * 2);
    ST(9);
    {
        LAS char* zw = L + CV_Z + c * CV_ZRS + 128 + 2 * (64 * tau + 2 * sp);
#pragma unroll
        for (int k = 0; k < 2; ++k)
#pragma unroll
            for (int g = 0; g < 4; ++g) {
                const float a = (g & 1) ? bfhi(xz[k][0][g >> 1]) : bflo(xz[k][0][g >> 1]);
                const float e = (g & 1) ? bfhi(xz[k][1][g >> 1]) : bflo(xz[k][1][g >> 1]);
                *(LAS u32*)(zw + k * 4 * CV_ZRS + 32 * g) =
                    pk_bf16(__builtin_fmaf(a, rc[0][g][0], rc[0][g][1]), __builtin_fmaf(e, rc[1][g][0], rc[1][g][1]));
            }
    }
    ST(2);
    asm volatile("s_waitcnt vmcnt(0) lgkmcnt(0)" ::: "memory");
    raw_barrier();
    ST(3);
    if (half) conv_main<1>(L, xout, xoff, ch, lane); else conv_main<0>(L, xout, xoff, ch, lane);
    ST(7);
    ST_FLUSH(layer == 0 ? 1 : 4);
}

#define G1_R 6
#define G1_UNROLL 1
constexpr int G1_NS = 16, G1_SLOT = 20480, G1_ST = 131072, G1_LDS = G1_ST + 8 * 128 * 8;

__global__ __launch_bounds__(512) void gemm1_kernel(char* ws, int layer) {
    extern __shared__ __attribute__((aligned(16))) char smem_raw[];
    LAS char* L = (LAS char*)smem_raw;
    asm volatile("" ::"s"(ws), "s"(layer));
    const u16* xT = (const u16*)(ws + WS_X + (2 * layer + 1) * WS_XSTEP);
    const char* W1F = ws + WS_W1;
    u32* lnw = (u32*)(ws + WS_LNW) + layer * SEQ;
    const int lane = threadIdx.x & 63, wave = __builtin_amdgcn_readfirstlane(threadIdx.x >> 6);
    const int xcd = blockIdx.x & 7, sl = blockIdx.x >> 3;
    constexpr int MPX = 16 / TG1;
    const int mt = (xcd % TG1) * MPX + (sl % MPX), fg = (xcd / TG1) * (32 / MPX) + (sl / MPX);
    ST_DECL;
    const int g = lane >> 4, q = (lane & 15) >> 2;
    auto xfrag_off = [&](int tb) { return (8 * g + q) * 256 + ((tb ^ (q | ((g & 1) << 2))) << 5) + (lane & 3) * 8; };
    auto xsrc_off = [&](int odd) { return (u32)(g * 256 + ((((lane & 15) >> 1) ^ (g | (odd << 2))) << 5) + (lane & 1) * 16); };
    const char* xtile = (const char*)(xT + mt * XTILE);

    if (fg == 15) {
        LAS char* mine = L + wave * 16384;
        const u32 so0 = xsrc_off(0), so1 = xsrc_off(1);
#pragma unroll
        for (int i = 0; i < 8; ++i) glds_s<2>(xtile + (16 * wave + 2 * i) * 1024, (i & 1) ? so1 : so0, (u32)(uintptr_t)mine + i * 2048);
        float S[8], Q[8];
        u32 a[8];
#pragma unroll
        for (int tb = 0; tb < 8; ++tb) { a[tb] = (u32)(uintptr_t)mine + xfrag_off(tb); S[tb] = 0.f; Q[tb] = 0.f; }
        static_for<4>([&](auto KI) {
            constexpr int kk = KI >> 1, tq = (KI & 1) * 4;
            if constexpr (tq == 0) wait_vm<8 - 8 * kk>();
            u32x2 f[8];
            asm volatile("ds_read_b64_tr_b16 %0, %8 offset:%c12\n\tds_read_b64_tr_b16 %1, %8 offset:%c13\n\t"
                         "ds_read_b64_tr_b16 %2, %9 offset:%c12\n\tds_read_b64_tr_b16 %3, %9 offset:%c13\n\t"
                         "ds_read_b64_tr_b16 %4, %10 offset:%c12\n\tds_read_b64_tr_b16 %5, %10 offset:%c13\n\t"
                         "ds_read_b64_tr_b16 %6, %11 offset:%c12\n\tds_read_b64_tr_b16 %7, %11 offset:%c13\n\ts_waitcnt lgkmcnt(0)"
                         : "=&v"(f[0]), "=&v"(f[1]), "=&v"(f[2]), "=&v"(f[3]), "=&v"(f[4]), "=&v"(f[5]), "=&v"(f[6]), "=&v"(f[7])
                         : "v"(a[tq]), "v"(a[tq + 1]), "v"(a[tq + 2]), "v"(a[tq + 3]), "i"(kk * 8192), "i"(kk * 8192 + 1024) : "memory");
#pragma unroll
            for (int j = 0; j < 4; ++j)
#pragma unroll
                for (int tb = 0; tb < 4; ++tb) {
                    const u32 w = f[2 * tb + (j >> 1)][j & 1];
                    S[tq + tb] = dot2(w, ONES, S[tq + tb]); Q[tq + tb] = dot2(w, w, Q[tq + tb]);
                }
        });
        LAS float* red = (LAS float*)(L + G1_ST);
#pragma unroll
        for (int tb = 0; tb < 8; ++tb) {
            const float v = row_pair_sum(half_sum2(S[tb], Q[tb]));
            if ((lane & 16) == 0) red[wave * 256 + (lane >> 5) * 128 + 16 * tb + (lane & 15)] = v;
        }
        wait_lgkm0();
        raw_barrier();
        if (threadIdx.x < 128) {
            float s = 0.f, qq = 0.f;
#pragma unroll
            for (int w = 0; w < 8; ++w) { s += red[w * 256 + threadIdx.x]; qq += red[w * 256 + 128 + threadIdx.x]; }
            float mb, sb;
            ln_scalars(s, qq, mb, sb);
            __hip_atomic_store(lnw + mt * 128 + threadIdx.x, pk_bf16(mb, sb), __ATOMIC_RELAXED, __HIP_MEMORY_SCOPE_AGENT);
        }
        ST_FLUSH(layer ? 5 : 2);
        return;
    }
    const int u0 = fg * 6 < NU - 6 ? fg * 6 : NU - 6;
    if (wave >= 4) {
        const int lw = wave - 4;
        const u32 xo = xsrc_off(lw & 1), wo = lane * 16;
        const char* xs = xtile + 2 * lw * 1024;
        const int up = lw + (lw >> 1) + (lw & 1), us = 1 + 3 * (lw >> 1);
        const char* wp = W1F + (u0 + up) * 32 * 1024;
        const char* w1 = W1F + ((u0 + us) * 32 + (lw & 1)) * 1024;
        const u32 l0 = (u32)(uintptr_t)L;
        int slot = 0;
        auto issue = [&]() {
            const u32 dst = l0 + slot * G1_SLOT;
            glds_s<2>(xs, xo, dst + 2 * lw * 1024);
            glds_s<2>(wp, wo, dst + 8192 + up * 2048);
            glds_s<1>(w1, wo, dst + 8192 + us * 2048 + (lw & 1) * 1024);
            xs += 8192;
            wp += 2048; w1 += 2048;
            slot = slot == G1_R - 1 ? 0 : slot + 1;
        };
        constexpr int LEAD = G1_R - 2;
        issue(); issue();
        ST(1);
        static_for<LEAD - 2>([&](auto TI) {
            wait_vm<5 * (TI + 1)>();
            raw_barrier();
            issue(); issue();
        });
#pragma unroll 1
        for (int s = LEAD - 2; s < G1_NS - LEAD; ++s) {
            wait_vm<5 * (LEAD - 1)>();
            raw_barrier();
            issue();
        }
        ST(2);
        static_for<LEAD>([&](auto TI) {
            wait_vm<5 * (LEAD - 1 - TI)>();
            raw_barrier();
        });
        ST(3);
        ST_FLUSH(layer ? 5 : 2);
        return;
    }
    const int wm = wave & 1, wn = wave >> 1;
    f32x4 acc[6][4];
    bf16x8 xf[2][4], wf[2][6];
#pragma unroll
    for (int i = 0; i < 6; ++i)
#pragma unroll
        for (int t = 0; t < 4; ++t) acc[i][t] = f32x4{0.f, 0.f, 0.f, 0.f};
    int xo[4];
#pragma unroll
    for (int t = 0; t < 4; ++t) xo[t] = xfrag_off(4 * wm + t);
    const int wo = 8192 + 6 * wn * 1024 + lane * 16;
    int slot = 0;
    auto rd = [&](auto CI) {
        constexpr int c = CI;
        LAS char* base = L + slot * G1_SLOT;
        slot = slot == G1_R - 1 ? 0 : slot + 1;
        wf[c][0] = *(LAS bf16x8*)(base + wo);
#pragma unroll
        for (int t = 0; t < 4; ++t) xf[c][t] = tr_frag(base + xo[t], base + xo[t] + 1024);
#pragma unroll
        for (int i = 1; i < 6; ++i) wf[c][i] = *(LAS bf16x8*)(base + wo + i * 1024);
    };
    auto mm = [&](auto CI) {
        constexpr int c = CI;
#pragma unroll
        for (int i = 0; i < 6; ++i)
#pragma unroll
            for (int t = 0; t < 4; ++t) acc[i][t] = MFMA16(wf[c][i], xf[c][t], acc[i][t]);
    };
    auto step = [&](auto CI) {
        constexpr int c = CI;
        raw_barrier();
        rd(std::integral_constant<int, c>{});
        mm(std::integral_constant<int, c ^ 1>{});
#pragma unroll
        for (int k = 0; k < 14; ++k) { __builtin_amdgcn_sched_group_barrier(0x8, 1, 0); __builtin_amdgcn_sched_group_barrier(0x100, 1, 0); }
        PIN16(acc[5][3]);
        sched_fence();
    };
    using I0 = std::integral_constant<int, 0>; using I1 = std::integral_constant<int, 1>;
    ST(1);
    raw_barrier();
    rd(I0{});
    sched_fence();
    ST(2);
#pragma unroll G1_UNROLL
    for (int s = 1; s < G1_NS - 3; s += 2) { step(I1{}); step(I0{}); }
    u32 st[4];
    f32x4 cs[6];
    u32* stp = lnw + mt * 128 + 64 * wm + (lane & 15);
#pragma unroll
    for (int t = 0; t < 4; ++t) st[t] = __hip_atomic_load(stp + 16 * t, __ATOMIC_RELAXED, __HIP_MEMORY_SCOPE_AGENT);
#pragma unroll
    for (int i = 0; i < 6; ++i) cs[i] = ((const f32x4*)(ws + WS_CS))[(u0 + 3 * wn) * 8 + 4 * i + g];
    sched_fence();
    step(I1{}); step(I0{}); step(I1{});
    for (int spin = 0; spin < (1 << 22) && !__all(st[0] != 0u && st[1] != 0u && st[2] != 0u && st[3] != 0u); ++spin) {
        __builtin_amdgcn_s_sleep(2);
#pragma unroll
        for (int t = 0; t < 4; ++t) st[t] = __hip_atomic_load(stp + 16 * t, __ATOMIC_RELAXED, __HIP_MEMORY_SCOPE_AGENT);
    }
    ST(8);
    mm(I1{});
    ST(9);
    const rsrc_t HR = make_rsrc(ws + WS_H + (1 - layer) * WS_HSTEP + ((4 * mt + 2 * wm) * NU + u0 + 3 * wn) * 1024);
    const u32 hoff = (g >> 1) * 512 + pi_inv(16 * (g & 1) + (lane & 15)) * 16;
    auto glu = [&](int i, int t) {
        const float sb = bfhi(st[t]), nm = -bflo(st[t]);
        const float ce = -1.44269504088896341f * __builtin_amdgcn_rcpf(sb);
        const f32x2 nm2 = {nm, nm}, sb2 = {sb, sb};
        const f32x4 ga = acc[2 * i][t], ua = acc[2 * i + 1][t], cg = cs[2 * i], cu = cs[2 * i + 1];
        u32x2 o;
#pragma unroll
        for (int j = 0; j < 2; ++j) {
            const f32x2 arg = __builtin_elementwise_fma(f32x2{cg[2 * j], cg[2 * j + 1]}, nm2, f32x2{ga[2 * j], ga[2 * j + 1]}) * ce;
            const f32x2 e = {__builtin_amdgcn_exp2f(arg[0]), __builtin_amdgcn_exp2f(arg[1])};
            const f32x2 den = __builtin_elementwise_fma(e, sb2, sb2);
            const f32x2 r = {__builtin_amdgcn_rcpf(den[0]), __builtin_amdgcn_rcpf(den[1])};
            o[j] = pk_bf16(__builtin_elementwise_fma(f32x2{cu[2 * j], cu[2 * j + 1]}, nm2, f32x2{ua[2 * j], ua[2 * j + 1]}) * r);
        }
        return o;
    };
#pragma unroll
    for (int tp = 0; tp < 2; ++tp)
#pragma unroll
        for (int i = 0; i < 3; ++i) {
            const u32x2 a = glu(i, 2 * tp), b = glu(i, 2 * tp + 1);
            const auto r0 = __builtin_amdgcn_permlane16_swap(a[0], b[0], false, false);
            const auto r1 = __builtin_amdgcn_permlane16_swap(a[1], b[1], false, false);
            st16<H_AUX>(HR, hoff + i * 1024, tp * NU * 1024, u32x4{r0[0], r1[0], r0[1], r1[1]});
        }
    ST(10);
    ST_FLUSH(layer ? 5 : 2);
}

#define G2_R 6
#define G2_NL 4
constexpr int G2_NS = 22, G2_SLOT = 16384, G2_XS = G2_R * G2_SLOT, G2_OS = G2_XS + 8192, G2_LDS = G2_OS + 8192;

__global__ __launch_bounds__(256 + 64 * G2_NL) void gemm2_kernel(char* ws, u16* out, int final_) {
    extern __shared__ __attribute__((aligned(16))) char smem_raw[];
    LAS char* L = (LAS char*)smem_raw;
    asm volatile("" ::"s"(ws), "s"(out), "s"(final_));
    const char* hF = ws + WS_H + (1 - final_) * WS_HSTEP;
    const char* W2F = ws + WS_W2;
    const u16* xin = (const u16*)(ws + WS_X + (2 * final_ + 1) * WS_XSTEP);
    const rsrc_t xout = make_rsrc(final_ ? (char*)out : ws + WS_X + 2 * WS_XSTEP);
    double* stats = (double*)(ws + WS_ST64);
    const int lane = threadIdx.x & 63, wave = __builtin_amdgcn_readfirstlane(threadIdx.x >> 6);
    const int xcd = blockIdx.x & 7, sl = blockIdx.x >> 3;
    constexpr int MPX = 32 / TG2;
    const int tg = TG2 > TG1 ? (xcd % TG1) * (TG2 / TG1) + (xcd / TG1) % (TG2 / TG1) : xcd % TG2;
    const int mt = tg * MPX + (sl % MPX), nt = (xcd / TG2) * (32 / MPX) + (sl / MPX);
    ST_DECL;
    if (wave >= 4) {
        constexpr int P = 16 / G2_NL;
        const int lw = (wave - 4) & 3, part = (wave - 4) >> 2;
        const char* src = (lw < 2 ? (const char*)hF + (2 * mt + lw) * NU * 1024 : (const char*)W2F + (2 * nt + lw - 2) * NU * 1024);
        const u32 vo = lane * 16;
        int slot = 0;
        auto issue = [&](auto NI) {
            constexpr int n = NI;
            glds_s<n>(src + part * n * 1024, vo, (u32)(uintptr_t)L + slot * G2_SLOT + lw * 4096 + part * n * 1024);
            src += 4096;
            slot = slot == G2_R - 1 ? 0 : slot + 1;
        };
        using IP = std::integral_constant<int, P>; using IH = std::integral_constant<int, P / 2>;
        issue(IP{}); issue(IP{});
        ST(1);
        constexpr int LEAD = G2_R - 2;
        static_for<LEAD - 2>([&](auto TI) {
            wait_vm<P * (TI + 1)>();
            raw_barrier();
            issue(IP{}); issue(IP{});
        });
#pragma unroll 1
        for (int s = LEAD - 2; s < G2_NS - 1 - LEAD; ++s) {
            wait_vm<P * (LEAD - 1)>();
            raw_barrier();
            issue(IP{});
        }
        wait_vm<P * (LEAD - 1)>();
        raw_barrier();
        issue(IH{});
        ST(2);
        static_for<LEAD>([&](auto TI) {
            constexpr int left = LEAD - 1 - TI;
            wait_vm<(left > 0 ? P * left - P / 2 : 0)>();
            raw_barrier();
        });
        ST(3);
        ST_FLUSH(final_ ? 6 : 3);
        return;
    }
    const int wm = wave & 1, wn = wave >> 1, hh = lane >> 5;
    {
        const char* xb = (const char*)(xin + (mt >> 1) * XTILE + (64 * nt + 16 * wave) * 128 + (mt & 1) * 64);
        const u32 vo = (lane >> 3) * 256 + (((lane & 7) ^ (lane >> 3)) << 4);
        glds_s<1>(xb, vo, (u32)(uintptr_t)L + G2_XS + wave * 2048);
        glds_s<1>(xb + 2048, vo, (u32)(uintptr_t)L + G2_XS + wave * 2048 + 1024);
    }
    f32x16 acc;
#pragma unroll
    for (int r = 0; r < 16; ++r) acc[r] = 0.f;
    const int ho = wm * 4096 + lane * 16, wo = (2 + wn) * 4096 + lane * 16;
    const int ao = final_ ? wo : ho, bo = final_ ? ho : wo;
    bf16x8 fa[2][4], fb[2][4];
#pragma unroll
    for (int j = 0; j < 4; ++j)
#pragma unroll
        for (int e = 0; e < 8; ++e) { fa[1][j][e] = (__bf16)0.f; fb[1][j][e] = (__bf16)0.f; }
    int slot = 0;
    auto step = [&](auto CI, auto NI) {
        constexpr int cur = CI, n = NI;
        LAS char* base = L + slot * G2_SLOT;
        slot = slot == G2_R - 1 ? 0 : slot + 1;
        raw_barrier();
#pragma unroll
        for (int j = 0; j < n; ++j) { fa[cur][j] = *(LAS bf16x8*)(base + ao + j * 1024); fb[cur][j] = *(LAS bf16x8*)(base + bo + j * 1024); }
        sched_fence();
#pragma unroll
        for (int j = 0; j < 4; ++j) acc = MFMA32(fa[cur ^ 1][j], fb[cur ^ 1][j], acc);
        PIN16(acc);
        sched_fence();
    };
    using I0 = std::integral_constant<int, 0>; using I1 = std::integral_constant<int, 1>;
    using I2 = std::integral_constant<int, 2>; using I4 = std::integral_constant<int, 4>;
    ST(1);
    wait_vm<0>();
    step(I0{}, I4{});
    LAS char* xs = L + G2_XS;
    const int item = pi_fwd(lane & 31);
    {
        u32 xr[8];
        if (!final_) {
            const int row = 32 * wn + item;
            const u32x4 a = *(LAS u32x4*)(xs + row * 128 + (((4 * wm + 2 * hh) ^ (row & 7)) << 4));
            const u32x4 c = *(LAS u32x4*)(xs + row * 128 + (((4 * wm + 2 * hh + 1) ^ (row & 7)) << 4));
#pragma unroll
            for (int r = 0; r < 4; ++r) { xr[r] = a[r]; xr[4 + r] = c[r]; }
        } else {
            const int qq = (lane & 15) >> 2, c8 = 8 * wm + (pi_fwd((lane & 16) + 4 * (lane & 3)) >> 2);
#pragma unroll
            for (int i = 0; i < 4; ++i) {
                const int row = 32 * wn + 16 * hh + 4 * i + qq;
                const u32x2 v = __builtin_bit_cast(u32x2, __builtin_amdgcn_ds_read_tr16_b64_v4bf16(
                                    (LAS bf16x4*)(xs + row * 128 + (((c8 >> 1) ^ (row & 7)) << 4) + (c8 & 1) * 8)));
                xr[2 * i] = v[0]; xr[2 * i + 1] = v[1];
            }
        }
#pragma unroll
        for (int p = 0; p < 8; ++p) { acc[2 * p] = bflo(xr[p]); acc[2 * p + 1] = bfhi(xr[p]); }
    }
    sched_fence();
#pragma unroll 1
    for (int s = 1; s < G2_NS - 1; s += 2) { step(I1{}, I4{}); step(I0{}, I4{}); }
    step(I1{}, I2{});
#pragma unroll
    for (int j = 0; j < 2; ++j) acc = MFMA32(fa[1][j], fb[1][j], acc);
    ST(8);
    LAS char* os = L + G2_OS;
    {
        const int row = 32 * (final_ ? wm : wn) + item, c0 = 4 * (final_ ? wn : wm) + 2 * hh;
        *(LAS u32x4*)(os + row * 128 + ((c0 ^ (row & 7)) << 4)) =
            u32x4{pk_bf16(acc[0], acc[1]), pk_bf16(acc[2], acc[3]), pk_bf16(acc[4], acc[5]), pk_bf16(acc[6], acc[7])};
        *(LAS u32x4*)(os + row * 128 + (((c0 + 1) ^ (row & 7)) << 4)) =
            u32x4{pk_bf16(acc[8], acc[9]), pk_bf16(acc[10], acc[11]), pk_bf16(acc[12], acc[13]), pk_bf16(acc[14], acc[15])};
    }
    wait_lgkm0();
    raw_barrier();
    ST(9);
    if (!final_) {
        float S = 0.f, Q = 0.f;
        const int rr = 4 * (lane >> 4) + ((lane & 15) >> 2);
        LAS char* rp = os + rr * 128 + (((2 * wave + ((lane & 3) >> 1)) ^ (rr & 7)) << 4) + 8 * (lane & 1);
#pragma unroll
        for (int i = 0; i < 4; ++i) {
            const u32x2 v = __builtin_bit_cast(u32x2, __builtin_amdgcn_ds_read_tr16_b64_v4bf16((LAS bf16x4*)(rp + i * 2048)));
            S = dot2(v[0], ONES, S); Q = dot2(v[0], v[0], Q);
            S = dot2(v[1], ONES, S); Q = dot2(v[1], v[1], Q);
        }
        const float v = row_pair_sum(half_sum2(S, Q));
        const int pos = 64 * mt + 16 * wave + (lane & 15);
        if ((lane & 16) == 0) atomicAdd(stats + 2 * st_slot(st_thread(pos), st_item(pos), 8) + hh, (double)v);
    }
    {
        const int pos = 64 * mt + 16 * wave + (lane >> 3);
        const int tok = (pos & ~255) | ((pos & 3) << 6) | ((pos >> 2) & 63);
        const u32 vo = (final_ ? tok * (DM * 2) + 128 * nt : 2 * ((mt >> 1) * XTILE + (64 * nt + 16 * wave + (lane >> 3)) * 128 + (mt & 1) * 64))
                     + (((lane & 7) ^ (lane >> 3)) << 4);
        st16(xout, vo, 0, *(LAS u32x4*)(os + wave * 2048 + lane * 16));
        st16(xout, vo, 2048, *(LAS u32x4*)(os + wave * 2048 + 1024 + lane * 16));
    }
    ST(10);
    ST_FLUSH(final_ ? 6 : 3);
}

extern "C" void kernel_launch(void* const* d_in, const int* in_sizes, int n_in, void* d_out, int out_size,
                              void* d_ws, size_t ws_size, hipStream_t stream) {
    (void)in_sizes; (void)n_in; (void)out_size; (void)ws_size;
    const u16* x0 = (const u16*)d_in[0];
    const u16* logA = (const u16*)d_in[1];
    const u16* Aim = (const u16*)d_in[2];
    const u16* Cre = (const u16*)d_in[3];
    const u16* Cim = (const u16*)d_in[4];
    const u16* logdt = (const u16*)d_in[5];
    const u16* Dsk = (const u16*)d_in[6];
    const u16* Wg = (const u16*)d_in[8];
    const u16* Wu = (const u16*)d_in[9];
    const u16* Wd = (const u16*)d_in[10];
    char* ws = (char*)d_ws;
    (void)hipFuncSetAttribute((const void*)conv_kernel, hipFuncAttributeMaxDynamicSharedMemorySize, CV_LDS);
    (void)hipFuncSetAttribute((const void*)gemm1_kernel, hipFuncAttributeMaxDynamicSharedMemorySize, G1_LDS);
    (void)hipFuncSetAttribute((const void*)gemm2_kernel, hipFuncAttributeMaxDynamicSharedMemorySize, G2_LDS);

    prep_kernel<<<PREP_BLOCKS, 256, PREP_LDS, stream>>>(x0, logA, Aim, Cre, Cim, logdt, Dsk, Wg, Wu, Wd, ws);
    for (int layer = 0; layer < 2; ++layer) {
        conv_kernel<<<CV_GRID, 256, CV_LDS, stream>>>(ws, layer);
        gemm1_kernel<<<256, 512, G1_LDS, stream>>>(ws, layer);
        gemm2_kernel<<<256, 256 + 64 * G2_NL, G2_LDS, stream>>>(ws, (u16*)d_out, layer);
    }
}
```

```cpp
#include <hip/hip_runtime.h>
#include <hip/hip_bf16.h>
#include <stdint.h>
#include <utility>

typedef unsigned short u16;
typedef unsigned int u32;
typedef __bf16 bf16x8 __attribute__((ext_vector_type(8)));
typedef __bf16 bf16x4 __attribute__((ext_vector_type(4)));
typedef __bf16 bf16x2 __attribute__((ext_vector_type(2)));
typedef _Float16 f16x8 __attribute__((ext_vector_type(8)));
typedef _Float16 f16x2 __attribute__((ext_vector_type(2)));
typedef float f32x16 __attribute__((ext_vector_type(16)));
typedef float f32x4 __attribute__((ext_vector_type(4)));
typedef float f32x2 __attribute__((ext_vector_type(2)));
typedef double f64x2 __attribute__((ext_vector_type(2)));
typedef u32 u32x4 __attribute__((ext_vector_type(4)));
typedef u32 u32x2 __attribute__((ext_vector_type(2)));

#define LAS __attribute__((address_space(3)))
#define GAS __attribute__((address_space(1)))
#define DI __device__ __forceinline__

constexpr int SEQ = 2048, DM = 512, NST = 128, FF = 1376, NU = FF / 16;
constexpr int XTILE = DM * 128;

constexpr size_t MB = 1 << 20;
constexpr size_t WS_XSTEP = 2 * MB, WS_HSTEP = 6 * MB;
constexpr size_t WS_X = 0, WS_K = 8 * MB, WS_W1 = 10 * MB, WS_W2 = 13 * MB, WS_LN = 15 * MB - 262144, WS_H = 15 * MB;
constexpr size_t WS_CSP = WS_LN + 32768;
constexpr size_t WS_CS = WS_LN + 98304;
constexpr size_t WS_ZERO = WS_LN + 131072;
constexpr size_t WS_ST64 = WS_ZERO, WS_LNW = WS_ZERO + SEQ * 16, WS_CNT = WS_LNW + 2 * SEQ * 4;
constexpr int ZERO_BYTES = SEQ * 16 + 2 * SEQ * 4 + 512 * 128;

#define TG1 4

#define ST_DECL do {} while (0)
#define ST(i) do {} while (0)
#define ST_FLUSH(kid) do {} while (0)

DI float bf2f(u16 h) { return __uint_as_float((u32)h << 16); }
DI float bflo(u32 w) { return __uint_as_float(w << 16); }
DI float bfhi(u32 w) { return __uint_as_float(w & 0xffff0000u); }
DI u32 pk_bf16(f32x2 v) { return __builtin_bit_cast(u32, __builtin_convertvector(v, bf16x2)); }
DI u32 pk_bf16(float lo, float hi) { return pk_bf16(f32x2{lo, hi}); }
DI float rbf(float x) { return bflo(pk_bf16(x, x)); }
DI u32 pk_f16(f32x2 v) { return __builtin_bit_cast(u32, __builtin_convertvector(v, f16x2)); }
DI float dot2(u32 a, u32 b, float c) { return __builtin_amdgcn_fdot2_f32_bf16(__builtin_bit_cast(bf16x2, a), __builtin_bit_cast(bf16x2, b), c, false); }
DI u32 perm_lo(u32 hi, u32 lo) { return __builtin_amdgcn_perm(hi, lo, 0x05040100u); }
DI u32 perm_hi(u32 hi, u32 lo) { return __builtin_amdgcn_perm(hi, lo, 0x07060302u); }
DI int pi_fwd(int r) { return (((r >> 2) & 1) << 4) | ((r >> 3) << 2) | (r & 3); }
DI int pi_inv(int p) { return (((p >> 2) & 3) << 3) | ((p >> 4) << 2) | (p & 3); }
DI int xt_off(int ch, int pos) { return (pos >> 7) * XTILE + ch * 128 + (pos & 127); }
DI int st_thread(int pos) { return ((pos >> 8) << 5) | (((pos >> 5) & 7) << 2) | ((pos >> 2) & 3); }
DI int st_item(int pos) { return (((pos >> 4) & 1) << 2) | (pos & 3); }
DI int st_slot(int thread, int item, int nitem) { return ((thread >> 6) * nitem + item) * 64 + (thread & 63); }
constexpr u32 ONES = 0x3f803f80u;

template <int N> DI void wait_vm() { asm volatile("s_waitcnt vmcnt(%0)" ::"n"(N) : "memory"); }
DI void wait_lgkm0() { asm volatile("s_waitcnt lgkmcnt(0)" ::: "memory"); }
DI void raw_barrier() { asm volatile("" ::: "memory"); __builtin_amdgcn_s_barrier(); asm volatile("" ::: "memory"); }
DI float opaque(float v) { asm("" : "+v"(v)); return v; }
DI u32 pinned(u32 v) { asm volatile("" : "+v"(v)); return v; }
#define PIN16(x) asm volatile("" : "+v"(x))
DI void sched_fence() { __builtin_amdgcn_sched_barrier(0); }
DI void glds16(const void* src, LAS char* dst) {
    __builtin_amdgcn_global_load_lds((const GAS void*)src, (LAS void*)dst, 16, 0, 0);
}
DI bf16x8 tr_frag(LAS char* a0, LAS char* a1) {
    bf16x4 lo = __builtin_amdgcn_ds_read_tr16_b64_v4bf16((LAS bf16x4*)a0);
    bf16x4 hi = __builtin_amdgcn_ds_read_tr16_b64_v4bf16((LAS bf16x4*)a1);
    return __builtin_shufflevector(lo, hi, 0, 1, 2, 3, 4, 5, 6, 7);
}
#define GLDS_HEAD "s_mov_b32 %0, m0\n\ts_mov_b32 m0, %3\n\ts_nop 0\n\t"
#define GLDS_ONE(off, sc) "global_load_lds_dwordx4 %1, %2 offset:" #off sc "\n\t"
#define GLDS_TAIL "s_mov_b32 m0, %0" : "=&s"(keep) : "v"(voff), "s"(ubase), "s"(lds_dst) : "memory"
#define GLDS_BODY(sc)                                                                                                          \
    if constexpr (N == 1) asm volatile(GLDS_HEAD GLDS_ONE(0, sc) GLDS_TAIL);                                                   \
    else if constexpr (N == 2) asm volatile(GLDS_HEAD GLDS_ONE(0, sc) GLDS_ONE(1024, sc) GLDS_TAIL);                           \
    else if constexpr (N == 3) asm volatile(GLDS_HEAD GLDS_ONE(0, sc) GLDS_ONE(1024, sc) GLDS_ONE(2048, sc) GLDS_TAIL);        \
    else asm volatile(GLDS_HEAD GLDS_ONE(0, sc) GLDS_ONE(1024, sc) GLDS_ONE(2048, sc) GLDS_ONE(3072, sc) GLDS_TAIL)
template <int N, bool DEV = false> DI void glds_s(const char* ubase, u32 voff, u32 lds_dst) {
    u32 keep;
    lds_dst = __builtin_amdgcn_readfirstlane(lds_dst);
    if constexpr (DEV) { GLDS_BODY(" sc1"); } else { GLDS_BODY(""); }
}
typedef __amdgpu_buffer_rsrc_t rsrc_t;
DI rsrc_t make_rsrc(void* base) { return __builtin_amdgcn_make_buffer_rsrc(base, 0, 0x7fffffff, 0x00020000); }
template <int AUX = 16> DI void st16(rsrc_t r, u32 voff, u32 soff, u32x4 v) { __builtin_amdgcn_raw_buffer_store_b128(v, r, voff + soff, 0, AUX); }
template <int AUX = 16> DI void st8(rsrc_t r, u32 voff, u32 soff, u32x2 v) { __builtin_amdgcn_raw_buffer_store_b64(v, r, voff, soff, AUX); }
#define H_AUX 16
#define MFMA16(a, b, c) __builtin_amdgcn_mfma_f32_16x16x32_bf16((a), (b), (c), 0, 0, 0)
#define MFMA32(a, b, c) __builtin_amdgcn_mfma_f32_32x32x16_bf16((a), (b), (c), 0, 0, 0)

template <class F, int... I> DI void static_for_impl(F&& f, std::integer_sequence<int, I...>) { (f(std::integral_constant<int, I>{}), ...); }
template <int N, class F> DI void static_for(F&& f) { static_for_impl(f, std::make_integer_sequence<int, N>{}); }

template <int CTRL, int RM = 0xf> DI float dppz(float v) { return __int_as_float(__builtin_amdgcn_update_dpp(0, __float_as_int(v), CTRL, RM, 0xf, true)); }
DI float wave_sum63(float v) {
    v += dppz<0x111>(v); v += dppz<0x112>(v); v += dppz<0x114>(v); v += dppz<0x118>(v);
    v += dppz<0x142, 0xa>(v); v += dppz<0x143, 0xc>(v);
    return v;
}
DI float wave_max63(float v) {
    v = fmaxf(v, dppz<0x111>(v)); v = fmaxf(v, dppz<0x112>(v)); v = fmaxf(v, dppz<0x114>(v)); v = fmaxf(v, dppz<0x118>(v));
    v = fmaxf(v, dppz<0x142, 0xa>(v)); v = fmaxf(v, dppz<0x143, 0xc>(v));
    return v;
}
DI float rdlane63(float v) { return __int_as_float(__builtin_amdgcn_readlane(__float_as_int(v), 63)); }
DI float half_sum2(float a, float b) {
    auto r = __builtin_amdgcn_permlane32_swap(__float_as_uint(a), __float_as_uint(b), false, false);
    return __uint_as_float(r[0]) + __uint_as_float(r[1]);
}
DI float row_pair_sum(float a) {
    auto r = __builtin_amdgcn_permlane16_swap(__float_as_uint(a), __float_as_uint(a), false, false);
    return __uint_as_float(r[0]) + __uint_as_float(r[1]);
}
DI void ln_scalars(float S, float Q, float& mb, float& sb) {
    const float mean = S * (1.f / DM);
    const float var = fmaxf(__builtin_fmaf(-mean, mean, Q * (1.f / DM)), 0.f);
    const u32 mv = pk_bf16(mean, var);
    mb = bflo(mv);
    sb = rbf(__builtin_amdgcn_sqrtf(rbf(bfhi(mv) + 1e-5f)));
}

DI f32x2 cmul(f32x2 a, f32x2 b) {
    f32x2 t, r;
    asm("v_pk_mul_f32 %0, %1, %2 op_sel_hi:[0,1]" : "=v"(t) : "v"(a), "v"(b));
    asm("v_pk_fma_f32 %0, %1, %2, %3 op_sel:[1,1,0] op_sel_hi:[1,0,1] neg_lo:[1,0,0]" : "=v"(r) : "v"(a), "v"(b), "v"(t));
    return r;
}
DI f32x2 cexp_p(float arl, float aiv, float p) {
    const float mag = __builtin_amdgcn_exp2f(arl * p);
    const float th = __builtin_amdgcn_fractf(aiv * p);
    return f32x2{mag * __builtin_amdgcn_cosf(th), mag * __builtin_amdgcn_sinf(th)};
}
DI float exp_bf(float x) {
    const float t = x * 1.44269504088896341f;
    const float r = __builtin_fmaf(x, 1.44269504088896341f, -t) + x * 1.92596299e-8f;
    const float e = __builtin_amdgcn_exp2f(t);
    return rbf(__builtin_fmaf(e * r, 0.693147181f, e));
}

constexpr int KG_ROW = 528;
constexpr int KG_B = 21 * KG_ROW, KG_TAB = 53 * KG_ROW, PREP_LDS = KG_TAB + 2320 * 2;
constexpr int PB_KGEN = 512, PB_W1 = 176, PB_W2 = 88, PB_TR = 128, PB_ST = 128;
constexpr int PREP_BLOCKS = PB_KGEN + PB_TR + PB_ST + PB_W1 + PB_W2;

__global__ __launch_bounds__(256, 2) void prep_kernel(
    const u16* __restrict__ x0, const u16* __restrict__ logA, const u16* __restrict__ Aim,
    const u16* __restrict__ Cre, const u16* __restrict__ Cim, const u16* __restrict__ logdt, const u16* __restrict__ Dskip,
    const u16* __restrict__ Wg, const u16* __restrict__ Wu, const u16* __restrict__ Wd, char* __restrict__ ws) {
    extern __shared__ __attribute__((aligned(16))) char smem_raw[];
    LAS char* L = (LAS char*)smem_raw;
    asm volatile("" ::"s"(x0), "s"(logA), "s"(Aim), "s"(Cre), "s"(Cim), "s"(logdt), "s"(Dskip), "s"(Wg), "s"(Wu), "s"(Wd), "s"(ws));
    const rsrc_t WS = make_rsrc(ws);
    const int tid = threadIdx.x, lane = tid & 63;
    const int wave = __builtin_amdgcn_readfirstlane(tid >> 6);
    int b = blockIdx.x;
    ST_DECL;
    if (b < PB_KGEN) {
        const int d = b, n = tid & 127;
        const u16 h_dt = logdt[d], h_D = Dskip[d], h_la = logA[d * NST + n], h_ai = Aim[d * NST + n];
        const u16 h_cr = Cre[d * NST + n], h_ci = Cim[d * NST + n];
        const u32 ca = ((const u32*)(Cre + d * NST))[lane], cc = ((const u32*)(Cim + d * NST))[lane];
        const float dt = exp_bf(bf2f(h_dt));
        const float arl = -exp_bf(bf2f(h_la)) * dt * 1.44269504088896341f;
        float aiv = bf2f(h_ai) * dt * 0.159154943091895336f;
        float mx = fmaxf(fmaxf(fabsf(bflo(ca)), fabsf(bfhi(ca))), fmaxf(fabsf(bflo(cc)), fabsf(bfhi(cc))));
        mx = rdlane63(wave_max63(mx));
        ST(1);
        const bool okm = mx > 1e-30f && mx < 1e30f;
        const float sc = okm ? 256.f * __builtin_amdgcn_rcpf(mx) : 1.f;
        const float back = dt * (2.f / 256.f) / sc;
        if (wave < 2) {
            aiv = -aiv;
            const f32x2 t0 = {bf2f(h_cr) * sc, -bf2f(h_ci) * sc};
            const f32x2 w1 = cexp_p(arl, aiv, 1.f), v = cexp_p(arl, aiv, 256.f);
            const f32x2 w2 = cmul(w1, w1), w4 = cmul(w2, w2);
            LAS u32* A = (LAS u32*)(L + 4 * n);
            auto put = [&](int row, f32x2 z) { A[row * (KG_ROW / 4)] = pk_f16(z); };
            const f32x2 r1 = cmul(t0, w1), r2 = cmul(t0, w2), r3 = cmul(r1, w2);
            put(0, t0); put(1, r1); put(2, r2); put(3, r3);
            put(4, cmul(t0, w4)); put(5, cmul(r1, w4)); put(6, cmul(r2, w4)); put(7, cmul(r3, w4));
            f32x2 t = cmul(t0, v);
            const f32x2 r9 = cmul(t, w2);
            put(8, t); put(9, r9); put(10, cmul(t, w4)); put(11, cmul(r9, w4));
            t = cmul(t, v); put(12, t); put(13, cmul(t, w4));
            t = cmul(t, v); put(14, t); put(15, cmul(t, w4));
#pragma unroll
            for (int r = 16; r <= 20; ++r) { t = cmul(t, v); put(r, t); }
        } else {
            f32x2 e[16];
            const f32x2 b1 = cexp_p(arl, aiv, 8.f), b8 = cexp_p(arl, aiv, 64.f);
            const f32x2 b2 = cmul(b1, b1), b4 = cmul(b2, b2), b16 = cmul(b8, b8);
            LAS u32* B = (LAS u32*)(L + KG_B + 4 * n);
            e[0] = f32x2{256.f, 0.f}; e[1] = b1 * 256.f; e[2] = b2 * 256.f; e[3] = cmul(e[1], b2);
            e[4] = b4 * 256.f; e[8] = b8 * 256.f;
#pragma unroll
            for (int c = 1; c < 4; ++c) e[4 + c] = cmul(e[c], b4);
#pragma unroll
            for (int c = 1; c < 8; ++c) e[8 + c] = cmul(e[c], b8);
#pragma unroll
            for (int c = 0; c < 16; ++c) {
                B[c * (KG_ROW / 4)] = pk_f16(e[c]);
                B[(16 + c) * (KG_ROW / 4)] = pk_f16(c ? cmul(e[c], b16) : b16 * 256.f);
            }
        }
        const int rb = wave & 1, cb = wave >> 1, q = lane >> 4;
        const int col8 = 8 * (16 * cb + (lane & 15));
        int ph[4];
#pragma unroll
        for (int j = 0; j < 4; ++j)
            ph[j] = col8 + (rb ? (q == 0 ? 1024 + 256 * j : (q == 1 && j == 0) ? 2048 : 2049)
                               : (q < 2 ? 4 * q + j : q == 2 ? 256 + 2 * j : 512 + 256 * (j >> 1) + 4 * (j & 1)));
        ST(2);
        __syncthreads();
        ST(3);
        {
            LAS char* pa = L + (16 * rb + (lane & 15)) * KG_ROW + q * 16;
            LAS char* pb = L + KG_B + (16 * cb + (lane & 15)) * KG_ROW + q * 16;
            f32x4 acc = {0.f, 0.f, 0.f, 0.f}, acc2 = {0.f, 0.f, 0.f, 0.f};
#pragma unroll
            for (int ks = 0; ks < 8; ks += 2) {
                acc = __builtin_amdgcn_mfma_f32_16x16x32_f16(*(LAS f16x8*)(pa + 64 * ks), *(LAS f16x8*)(pb + 64 * ks), acc, 0, 0, 0);
                acc2 = __builtin_amdgcn_mfma_f32_16x16x32_f16(*(LAS f16x8*)(pa + 64 * ks + 64), *(LAS f16x8*)(pb + 64 * ks + 64), acc2, 0, 0, 0);
            }
            acc += acc2;
            LAS u16* tab = (LAS u16*)(L + KG_TAB);
            const float add0 = tid == 0 ? bf2f(h_D) : 0.f;
            const u32 v01 = pk_bf16(__builtin_fmaf(acc[0], back, add0), acc[1] * back), v23 = pk_bf16(acc[2] * back, acc[3] * back);
            tab[ph[0]] = (u16)v01; tab[ph[1]] = (u16)(v01 >> 16);
            tab[ph[2]] = (u16)v23; tab[ph[3]] = (u16)(v23 >> 16);
        }
        ST(4);
        __syncthreads();
        ST(5);
        {
            const u32x4 w = *(LAS u32x4*)(L + KG_TAB + 16 * tid);
            const u32 w4 = *(LAS u32*)(L + KG_TAB + 16 * tid + 16);
            u32x4 o;
            if (wave == 0) {
                const u32x4 s1 = {perm_lo(w[0], w[0]), perm_lo(w[2], w[1]), perm_lo(w[2], w[2]), perm_lo(w4, w[3])};
                o = lane < 32 ? w : s1;
            } else if (wave == 1) {
                o = u32x4{perm_lo(w[0], w[0]), perm_lo(w[2], w[0]), perm_lo(w[2], w[2]), perm_lo(w4, w4)};
            } else {
                const u32 lo = perm_lo(w[0], w[0]);
                o = u32x4{lo, lo, perm_lo(w4, (lane & 1) ? w4 : w[0]), perm_lo(w4, w4)};
            }
            st16(WS, 16 * tid, WS_K + d * SEQ * 2, o);
        }
        ST(6);
        ST_FLUSH(0);
        return;
    }
    b -= PB_KGEN;
    if (b < PB_W1 + PB_W2) {
        const bool is1 = b < PB_W1;
        const int bb = is1 ? b : b - PB_W1;
        const int ct = is1 ? bb >> 3 : bb & 7, ty = (bb >> 2) & 1, rt = is1 ? bb & 3 : bb >> 3;
        const int pitch = is1 ? FF * 2 : DM * 2, nrow = is1 ? DM : FF;
        const char* W = (const char*)(is1 ? (ty ? Wu : Wg) : Wd);
        const int cbytes = is1 && ct == 21 ? 64 : 128;
        {
            const int r0 = 128 * rt + 32 * wave + (lane >> 3);
            const int co = ct * 128 + (((lane & 7) * 16) & (cbytes - 1));
#pragma unroll
            for (int i = 0; i < 4; ++i)
                glds_s<1>(W, (u32)(min(r0 + 8 * i, nrow - 1) * pitch + co), (u32)(uintptr_t)L + wave * 4096 + i * 1024);
        }
        ST(1);
        wait_vm<0>();
        ST(2);
        const u32 lb = (u32)(uintptr_t)L + wave * 4096;
        const int g = lane >> 4, q = (lane & 15) >> 2, pp = lane & 3;
        u32x2 f[8];
        if (is1) {
            const u32 a = lb + (8 * g + q) * 128 + pp * 8;
            asm volatile("ds_read_b64_tr_b16 %0, %8\n\tds_read_b64_tr_b16 %1, %8 offset:512\n\t"
                         "ds_read_b64_tr_b16 %2, %8 offset:32\n\tds_read_b64_tr_b16 %3, %8 offset:544\n\t"
                         "ds_read_b64_tr_b16 %4, %8 offset:64\n\tds_read_b64_tr_b16 %5, %8 offset:576\n\t"
                         "ds_read_b64_tr_b16 %6, %8 offset:96\n\tds_read_b64_tr_b16 %7, %8 offset:608\n\ts_waitcnt lgkmcnt(0)"
                         : "=&v"(f[0]), "=&v"(f[1]), "=&v"(f[2]), "=&v"(f[3]), "=&v"(f[4]), "=&v"(f[5]), "=&v"(f[6]), "=&v"(f[7])
                         : "v"(a) : "memory");
            LAS float* red = (LAS float*)(L + 16384);
#pragma unroll
            for (int i = 0; i < 4; ++i) {
                const int u = 4 * ct + i;
                if (u < NU) st16(WS, lane * 16, WS_W1 + ((u * 16 + 4 * rt + wave) * 2 + ty) * 1024, u32x4{f[2 * i][0], f[2 * i][1], f[2 * i + 1][0], f[2 * i + 1][1]});
            }
#pragma unroll
            for (int i = 0; i < 4; i += 2) {
                const float s0 = dot2(f[2 * i][0], ONES, dot2(f[2 * i][1], ONES, dot2(f[2 * i + 1][0], ONES, dot2(f[2 * i + 1][1], ONES, 0.f))));
                const float s1 = dot2(f[2 * i + 2][0], ONES, dot2(f[2 * i + 2][1], ONES, dot2(f[2 * i + 3][0], ONES, dot2(f[2 * i + 3][1], ONES, 0.f))));
                const float v = row_pair_sum(half_sum2(s0, s1));
                if ((lane & 16) == 0) red[wave * 64 + (i + (lane >> 5)) * 16 + (lane & 15)] = v;
            }
            __syncthreads();
            if (tid < 64 && 4 * ct + (tid >> 4) < NU)
                ((float*)(ws + WS_CSP))[((rt * NU + 4 * ct + (tid >> 4)) * 2 + ty) * 16 + (tid & 15)] =
                    (red[tid] + red[64 + tid]) + (red[128 + tid] + red[192 + tid]);
        } else {
            const u32 a = lb + (8 * (g >> 1) + q) * 128 + pi_fwd(16 * (g & 1) + 4 * pp) * 2;
            asm volatile("ds_read_b64_tr_b16 %0, %8\n\tds_read_b64_tr_b16 %1, %8 offset:512\n\t"
                         "ds_read_b64_tr_b16 %2, %8 offset:64\n\tds_read_b64_tr_b16 %3, %8 offset:576\n\t"
                         "ds_read_b64_tr_b16 %4, %8 offset:2048\n\tds_read_b64_tr_b16 %5, %8 offset:2560\n\t"
                         "ds_read_b64_tr_b16 %6, %8 offset:2112\n\tds_read_b64_tr_b16 %7, %8 offset:2624\n\ts_waitcnt lgkmcnt(0)"
                         : "=&v"(f[0]), "=&v"(f[1]), "=&v"(f[2]), "=&v"(f[3]), "=&v"(f[4]), "=&v"(f[5]), "=&v"(f[6]), "=&v"(f[7])
                         : "v"(a) : "memory");
#pragma unroll
            for (int i = 0; i < 4; ++i) {
                const int u = 8 * rt + 2 * wave + (i >> 1), cb = 2 * ct + (i & 1);
                if (u < NU) st16(WS, lane * 16, WS_W2 + (cb * NU + u) * 1024, u32x4{f[2 * i][0], f[2 * i][1], f[2 * i + 1][0], f[2 * i + 1][1]});
            }
        }
        ST(3);
        ST_FLUSH(0);
        return;
    }
    b -= PB_W1 + PB_W2;
    if (b < PB_TR) {
        const int w = b * 4 + wave, tau = w >> 6, lg = (w >> 4) & 3, cgg = w & 15;
        const int ln = lg * 16 + (lane & 15), cg = cgg * 4 + (lane >> 4);
        u32x4 in[4];
#pragma unroll
        for (int g = 0; g < 4; ++g) in[g] = *(const u32x4*)(x0 + (256 * tau + 64 * g + ln) * DM + 8 * cg);
        const u32 dst = 2 * xt_off(8 * cg, 256 * tau + 4 * ln);
#pragma unroll
        for (int q = 0; q < 4; ++q) {
            st8(WS, dst + (2 * q) * 256, WS_X, u32x2{perm_lo(in[1][q], in[0][q]), perm_lo(in[3][q], in[2][q])});
            st8(WS, dst + (2 * q + 1) * 256, WS_X, u32x2{perm_hi(in[1][q], in[0][q]), perm_hi(in[3][q], in[2][q])});
        }
        ST_FLUSH(0);
        return;
    }
    b -= PB_TR;
    {
        const int w = b * 4 + wave, tau = w >> 6, ln = w & 63;
        u32x4 in[4];
#pragma unroll
        for (int g = 0; g < 4; ++g) in[g] = *(const u32x4*)(x0 + (256 * tau + 64 * g + ln) * DM + 8 * lane);
        if ((b * 256 + tid) * 16 < ZERO_BYTES) st16(WS, (b * 256 + tid) * 16, WS_ZERO, u32x4{0u, 0u, 0u, 0u});
        if (b >= 96) st16(WS, ((b - 96) * 256 + tid) * 16, WS_W1 + NU * 32 * 1024, u32x4{0u, 0u, 0u, 0u});
        float o[8];
#pragma unroll
        for (int g = 0; g < 4; ++g) {
            float S = 0.f, Q = 0.f;
#pragma unroll
            for (int q = 0; q < 4; ++q) { S = dot2(in[g][q], ONES, S); Q = dot2(in[g][q], in[g][q], Q); }
            S = wave_sum63(S); Q = wave_sum63(Q);
            float mb, sb;
            ln_scalars(S, Q, mb, sb);
            o[2 * g] = __builtin_amdgcn_rcpf(sb);
            o[2 * g + 1] = -mb * o[2 * g];
        }
        if (lane == 63) {
            const int pos = 256 * tau + 4 * ln;
            const u32 dst = 16 * st_slot(st_thread(pos), st_item(pos) >> 1, 4);
            st16(WS, dst, WS_LN, __builtin_bit_cast(u32x4, f32x4{o[0], o[1], o[2], o[3]}));
            st16(WS, dst + 1024, WS_LN, __builtin_bit_cast(u32x4, f32x4{o[4], o[5], o[6], o[7]}));
        }
        ST_FLUSH(0);
    }
}

constexpr int CV_KROW = (64 + SEQ) * 2;
constexpr int CV_ZRS = 84 * 16;
constexpr int CV_Z = 2 * CV_KROW;
constexpr int CV_C = CV_Z + 2 * 4 * CV_ZRS;
constexpr int CV_DUMP = CV_C + 2 * 33 * 16;
constexpr int CV_X = CV_DUMP + 1024 + 33 * 16;
constexpr int CV_S = CV_X + 2 * SEQ * 2;
constexpr int CV_LDS = CV_S + SEQ * 16;
#define PF_SLEEP 12
#define CV_GRID 512

template <int HALF> struct TauSet { static constexpr int t(int i) { return HALF == 0 ? (i == 0 ? 0 : i == 1 ? 2 : i == 2 ? 5 : 7) : (i == 0 ? 1 : i == 1 ? 3 : i == 2 ? 4 : 6); } };

template <int HALF>
DI void conv_main(LAS char* L, rsrc_t xout, u32 xoff, int ch, int lane
) {
    using TS = TauSet<HALF>;
    constexpr int TMAX = TS::t(3);
    f32x16 acc[4];
#pragma unroll
    for (int i = 0; i < 4; ++i)
#pragma unroll
        for (int r = 0; r < 16; ++r) acc[i][r] = 0.f;
    const int hh = lane >> 5;
    LAS char* zb = L + CV_Z + (ch * 4 + (lane & 3)) * CV_ZRS + 128 + 16 * (((lane & 31) >> 2) + hh) - 16 * 7;
    LAS char* kb = L + ch * CV_KROW + 128 + 2 * (16 * ((lane >> 4) & 1) - 32 * hh - 4 * ((lane & 15) >> 2) + 4 * (lane & 3)) - 32;
    const int cbase = CV_C + ch * 33 * 16;
    LAS char* cw = L + (lane == 63 ? cbase + 16 : CV_DUMP + lane * 16);
    LAS char* cr[4];
#pragma unroll
    for (int i = 0; i < 4; ++i) cr[i] = L + (lane < 3 ? cbase + 4 + 4 * lane + 64 * TS::t(i) : 0);
    u32x2 xm[4];
    bf16x8 kf[2][TMAX + 1], zf[2][TMAX + 1];
    auto load = [&](auto EI) {
        constexpr int ei = EI, e = 2 * ei + 1;
#pragma unroll
        for (int m = 0; m <= TMAX; ++m) {
            kf[ei & 1][m] = tr_frag(kb + 32 + 64 * (8 * m + e), kb + 64 * (8 * m + e));
            zf[ei & 1][m] = *(LAS bf16x8*)(zb + 16 * (8 * m + 7 - e));
            if constexpr (ei == 0) sched_fence();
        }
    };
    load(std::integral_constant<int, 0>{});
    static_for<3>([&](auto EI) {
        constexpr int ei = EI;
        sched_fence();
        load(std::integral_constant<int, ei + 1>{});
        if constexpr (ei == 2) {
#pragma unroll
            for (int i = 0; i < 4; ++i) xm[i] = *(LAS u32x2*)(L + CV_X + ch * 4096 + 512 * TS::t(i) + 8 * lane);
        }
#pragma unroll
        for (int i = 0; i < 4; ++i)
#pragma unroll
            for (int mu = 0; mu <= TS::t(i); ++mu) acc[i] = MFMA32(zf[ei & 1][TS::t(i) - mu], kf[ei & 1][mu], acc[i]);
        if constexpr (ei == 0) __builtin_amdgcn_sched_group_barrier(0x8, 5, 0);
#pragma unroll
        for (int k = 0; k < 13; ++k) { __builtin_amdgcn_sched_group_barrier(0x8, 1, 0); __builtin_amdgcn_sched_group_barrier(0x100, 2, 0); }
        PIN16(acc[0]); PIN16(acc[1]); PIN16(acc[2]); PIN16(acc[3]);
    });
    sched_fence();
    float y[4][4];
    auto post = [&](auto II) {
        constexpr int i = II;
        float s1[4], s2[4];
#pragma unroll
        for (int g = 0; g < 4; ++g) {
            s2[g] = opaque(acc[i][4 * g + 2] + dppz<0x138>(acc[i][4 * g + 3]));
            y[i][g] = opaque(acc[i][4 * g] + ((g & 1) ? bfhi(xm[i][g >> 1]) : bflo(xm[i][g >> 1])));
        }
#pragma unroll
        for (int g = 0; g < 4; ++g) s1[g] = opaque(acc[i][4 * g + 1] + dppz<0x138>(s2[g]));
#pragma unroll
        for (int g = 0; g < 4; ++g) {
            y[i][g] = opaque(y[i][g] + dppz<0x138>(s1[g]));
            *(LAS f32x4*)(cw + (4 * TS::t(i) + g) * 16) = f32x4{y[i][g], s1[g], s2[g], acc[i][4 * g + 3]};
        }
    };
    static_for<4>([&](auto II) {
        constexpr int i = II;
#pragma unroll
        for (int mu = 0; mu <= TS::t(i); ++mu) acc[i] = MFMA32(zf[1][TS::t(i) - mu], kf[1][mu], acc[i]);
        if constexpr (i > 0) {
            post(std::integral_constant<int, i - 1>{});
            __builtin_amdgcn_sched_group_barrier(0x8, 2, 0);
#pragma unroll
            for (int k = 2; k <= TS::t(i); ++k) { __builtin_amdgcn_sched_group_barrier(0x202, 6, 0); __builtin_amdgcn_sched_group_barrier(0x8, 1, 0); }
        }
        PIN16(acc[i]);
        sched_fence();
    });
    ST(4);
    post(std::integral_constant<int, 3>{});
    ST(5);
    wait_lgkm0();
    raw_barrier();
    ST(6);
    float cv[4][4];
#pragma unroll
    for (int i = 0; i < 4; ++i)
#pragma unroll
        for (int g = 0; g < 4; ++g) cv[i][g] = *(LAS float*)(cr[i] + 16 * g);
    static_for<4>([&](auto II) {
        constexpr int i = II;
        st8(xout, xoff, 4 * TS::t(i) * XTILE, u32x2{pk_bf16(opaque(TS::t(i) ? y[i][0] + cv[i][0] : y[i][0]), opaque(y[i][1] + cv[i][1])),
                                                    pk_bf16(opaque(y[i][2] + cv[i][2]), opaque(y[i][3] + cv[i][3]))});
    });
}

__global__ __launch_bounds__(256, 2) void conv_kernel(char* ws, int layer) {
    extern __shared__ __attribute__((aligned(16))) char smem_raw[];
    LAS char* L = (LAS char*)smem_raw;
    asm volatile("" ::"s"(ws), "s"(layer));
    const int tid = threadIdx.x, lane = tid & 63, wave = __builtin_amdgcn_readfirstlane(tid >> 6);
    if (blockIdx.x >= 256) {
        const int xcd = blockIdx.x & 7, sl = ((blockIdx.x - 256) >> 3) * 4 + wave;
        constexpr int P1 = (96 / (8 / TG1)) * 32, N1 = (P1 + 127) / 128;
        const int k1 = (xcd / TG1) * P1 + sl * N1;
        const u32 dst = (u32)(uintptr_t)L + wave * 1024;
        if (layer == 0 && blockIdx.x < 256 + 3) {
            const int i = (blockIdx.x - 256) * 256 + tid;
            const f32x4* cp = (const f32x4*)(ws + WS_CSP) + i;
            if (i < NU * 8) ((f32x4*)(ws + WS_CS))[i] = (cp[0] + cp[NU * 8]) + (cp[2 * NU * 8] + cp[3 * NU * 8]);
        }
        __builtin_amdgcn_s_sleep(PF_SLEEP);
#pragma unroll 1
        for (int i = 0; i < N1; ++i) glds_s<1>(ws + WS_W1 + (size_t)min(k1 + i, 90 * 32 - 1) * 1024, lane * 16, dst);
        wait_vm<0>();
        return;
    }
    const u16* xin = (const u16*)(ws + WS_X + 2 * layer * WS_XSTEP);
    const rsrc_t xout = make_rsrc(ws + WS_X + (2 * layer + 1) * WS_XSTEP);
    const int ch = wave >> 1, half = wave & 1, d0 = 2 * blockIdx.x, d = d0 + ch;
    ST_DECL;
    const int tau = tid >> 5, c = tid & 3, sp = (tid >> 2) & 7;
    const int p0 = 256 * tau + 32 * sp + 4 * c;
    const u32 l0 = (u32)(uintptr_t)L;
    {
        const int n = layer ? 8 : 4, rot = blockIdx.x >> 3;
        const char* sb = ws + (layer ? WS_ST64 : WS_LN) + wave * n * 1024;
#pragma unroll 1
        for (int j = 0; j < n; ++j) {
            const int k = (j + rot) & (n - 1);
            glds_s<1>(sb + k * 1024, lane * 16, l0 + CV_S + wave * 8192 + k * 1024);
        }
    }
    {
        const char* xb = (const char*)(xin + 4 * wave * XTILE + d0 * 128);
        const u32 vo = (lane >> 4) * (XTILE * 2) + (lane & 15) * 16;
        glds_s<1>(xb, vo, l0 + CV_X + wave * 1024);
        glds_s<1>(xb + 256, vo, l0 + CV_X + 4096 + wave * 1024);
    }
    glds_s<2>(ws + WS_K + (size_t)(d * SEQ + half * 1024) * 2, lane * 16, l0 + ch * CV_KROW + 128 + half * 2048);
    const u32 xoff = 2 * xt_off(d, 4 * lane);
    if (wave == 0) *(LAS u32x4*)(L + CV_Z + (lane >> 3) * CV_ZRS + 16 * (lane & 7)) = u32x4{0u, 0u, 0u, 0u};
    if (wave == 1) *(LAS u32x4*)(L + ((lane >> 3) & 1) * CV_KROW + 16 * (lane & 7)) = u32x4{0u, 0u, 0u, 0u};
    ST(1);
    f32x2 rc[2][4];
    LAS char* sl = L + CV_S + wave * 8192 + lane * 16;
    wait_vm<4>();
    ST(8);
    if (layer != 0) {
#pragma unroll
        for (int j = 0; j < 8; ++j) {
            const f64x2 sq = *(LAS f64x2*)(sl + j * 1024);
            const float nm = (float)sq[0] * (-1.f / DM);
            const float var = fmaxf(__builtin_fmaf(-nm, nm, (float)sq[1] * (1.f / DM)), 0.f);
            const float rs = __builtin_amdgcn_rsqf(var + 1e-5f);
            rc[j >> 2][j & 3] = f32x2{rs, nm * rs};
        }
    } else {
#pragma unroll
        for (int j = 0; j < 4; ++j) {
            const f32x4 v = *(LAS f32x4*)(sl + j * 1024);
            rc[j >> 1][2 * (j & 1)] = f32x2{v[0], v[1]}; rc[j >> 1][2 * (j & 1) + 1] = f32x2{v[2], v[3]};
        }
    }
    wait_vm<2>();
    u32x2 xz[2][2];
#pragma unroll
    for (int k = 0; k < 2; ++k)
#pragma unroll
        for (int s = 0; s < 2; ++s) xz[k][s] = *(LAS u32x2*)(L + CV_X + k * 4096 + (p0 + 16 * s) * 2);
    ST(9);
    {
        LAS char* zw = L + CV_Z + c * CV_ZRS + 128 + 2 * (64 * tau + 2 * sp);
#pragma unroll
        for (int k = 0; k < 2; ++k)
#pragma unroll
            for (int g = 0; g < 4; ++g) {
                const float a = (g & 1) ? bfhi(xz[k][0][g >> 1]) : bflo(xz[k][0][g >> 1]);
                const float e = (g & 1) ? bfhi(xz[k][1][g >> 1]) : bflo(xz[k][1][g >> 1]);
                *(LAS u32*)(zw + k * 4 * CV_ZRS + 32 * g) =
                    pk_bf16(__builtin_fmaf(a, rc[0][g][0], rc[0][g][1]), __builtin_fmaf(e, rc[1][g][0], rc[1][g][1]));
            }
    }
    ST(2);
    asm volatile("s_waitcnt vmcnt(0) lgkmcnt(0)" ::: "memory");
    raw_barrier();
    ST(3);
    if (half) conv_main<1>(L, xout, xoff, ch, lane); else conv_main<0>(L, xout, xoff, ch, lane);
    ST(7);
    ST_FLUSH(layer == 0 ? 1 : 4);
}

#define G1_R 6
#define MAP2 1
#define ROT2 0
#define RAMP2 4
#define W2_START 2
#define W2_PER 0
#define W2_TAIL 6
constexpr int G1_NS = 16, G1_SLOT = 20480;
constexpr int G2_NS = 11, G2_R = 7, G2_SLOT = 16384, KH = NU / 2;
constexpr int ML_RED = 131072, ML_OS = ML_RED, ML_XS = ML_RED + 8192, ML_EX = ML_XS + 8192, ML_LDS = ML_EX + 16384;
constexpr int ML_CTR = ML_EX;

constexpr int G1_LEAD = G1_R - 2;
constexpr int g1_nis(int s) { int a = 2 * s + 2, b = s + G1_LEAD; a = a < b ? a : b; return a < G1_NS ? a : G1_NS; }
constexpr int g1_wlo(int s) {
    int a = s <= W2_START ? 0 : s <= G1_NS - G1_LEAD ? W2_PER * (s - W2_START)
                              : W2_PER * (G1_NS - G1_LEAD - W2_START) + W2_TAIL * (s - (G1_NS - G1_LEAD));
    return a < KH ? a : KH;
}
constexpr int g1_younger(int s) {
    int t = -1;
    while (g1_nis(t + 1) <= s) ++t;
    return 5 * (g1_nis(s) - 1 - s) + g1_wlo(s) - g1_wlo(t < 0 ? 0 : t);
}

__global__ __launch_bounds__(512) void mlp_kernel(char* ws, u16* out, int layer) {
    extern __shared__ __attribute__((aligned(16))) char smem_raw[];
    LAS char* L = (LAS char*)smem_raw;
    asm volatile("" ::"s"(ws), "s"(out), "s"(layer));
    const u16* xT = (const u16*)(ws + WS_X + (2 * layer + 1) * WS_XSTEP);
    char* hF = ws + WS_H + (1 - layer) * WS_HSTEP;
    u32* lnw = (u32*)(ws + WS_LNW) + layer * SEQ;
    const int lane = threadIdx.x & 63, wave = __builtin_amdgcn_readfirstlane(threadIdx.x >> 6);
    const int xcd = blockIdx.x & 7, sl = blockIdx.x >> 3;
    constexpr int MPX = 16 / TG1;
    const int mt = (xcd % TG1) * MPX + (sl % MPX), fg = (xcd / TG1) * (32 / MPX) + (sl / MPX);
    const int m2 = 2 * mt + (fg & 1), nt = fg >> 1;
    const int rot = ROT2 == 0 ? 0 : ROT2 == 1 ? (nt & 1) * 5 : MAP2 ? (nt & 3) * 5 >> 1 : nt + (nt >> 2);
    auto blk_unit = [&](int s, int kh) { return KH * kh + 4 * (s == G2_NS - 1 ? s : s + rot < G2_NS - 1 ? s + rot : s + rot - (G2_NS - 1)); };
    const u32 l0 = (u32)(uintptr_t)L;
    ST_DECL;
    const int g = lane >> 4, q = (lane & 15) >> 2;
    auto xfrag_off = [&](int tb) { return (8 * g + q) * 256 + ((tb ^ (q | ((g & 1) << 2))) << 5) + (lane & 3) * 8; };
    auto xsrc_off = [&](int odd) { return (u32)(g * 256 + ((((lane & 15) >> 1) ^ (g | (odd << 2))) << 5) + (lane & 1) * 16); };
    const char* xtile = (const char*)(xT + mt * XTILE);
    const int u0 = fg * 6;
    using I0 = std::integral_constant<int, 0>; using I1 = std::integral_constant<int, 1>;
    if (wave == 0) { *(LAS u32*)(L + ML_CTR) = 0u; wait_lgkm0(); }

    if (fg == 15) {
        const u32 mine = l0 + wave * 16384;
        const u32 so0 = xsrc_off(0), so1 = xsrc_off(1);
#pragma unroll
        for (int i = 0; i < 8; ++i) glds_s<2>(xtile + (16 * wave + 2 * i) * 1024, (i & 1) ? so1 : so0, mine + i * 2048);
        float S[8], Q[8];
        u32 a[8];
#pragma unroll
        for (int tb = 0; tb < 8; ++tb) { a[tb] = mine + xfrag_off(tb); S[tb] = 0.f; Q[tb] = 0.f; }
        static_for<4>([&](auto KI) {
            constexpr int kk = KI >> 1, tq = (KI & 1) * 4;
            if constexpr (tq == 0) wait_vm<8 - 8 * kk>();
            u32x2 f[8];
            asm volatile("ds_read_b64_tr_b16 %0, %8 offset:%c12\n\tds_read_b64_tr_b16 %1, %8 offset:%c13\n\t"
                         "ds_read_b64_tr_b16 %2, %9 offset:%c12\n\tds_read_b64_tr_b16 %3, %9 offset:%c13\n\t"
                         "ds_read_b64_tr_b16 %4, %10 offset:%c12\n\tds_read_b64_tr_b16 %5, %10 offset:%c13\n\t"
                         "ds_read_b64_tr_b16 %6, %11 offset:%c12\n\tds_read_b64_tr_b16 %7, %11 offset:%c13\n\ts_waitcnt lgkmcnt(0)"
                         : "=&v"(f[0]), "=&v"(f[1]), "=&v"(f[2]), "=&v"(f[3]), "=&v"(f[4]), "=&v"(f[5]), "=&v"(f[6]), "=&v"(f[7])
                         : "v"(a[tq]), "v"(a[tq + 1]), "v"(a[tq + 2]), "v"(a[tq + 3]), "i"(kk * 8192), "i"(kk * 8192 + 1024) : "memory");
#pragma unroll
            for (int j = 0; j < 4; ++j)
#pragma unroll
                for (int tb = 0; tb < 4; ++tb) {
                    const u32 w = f[2 * tb + (j >> 1)][j & 1];
                    S[tq + tb] = dot2(w, ONES, S[tq + tb]); Q[tq + tb] = dot2(w, w, Q[tq + tb]);
                }
        });
        LAS float* red = (LAS float*)(L + ML_RED);
#pragma unroll
        for (int tb = 0; tb < 8; ++tb) {
            const float v = row_pair_sum(half_sum2(S[tb], Q[tb]));
            if ((lane & 16) == 0) red[wave * 256 + (lane >> 5) * 128 + 16 * tb + (lane & 15)] = v;
        }
        wait_lgkm0();
        raw_barrier();
        if (threadIdx.x < 128) {
            float s = 0.f, qq = 0.f;
#pragma unroll
            for (int w = 0; w < 8; ++w) { s += red[w * 256 + threadIdx.x]; qq += red[w * 256 + 128 + threadIdx.x]; }
            float mb, sb;
            ln_scalars(s, qq, mb, sb);
            __hip_atomic_store(lnw + mt * 128 + threadIdx.x, pk_bf16(mb, sb), __ATOMIC_RELAXED, __HIP_MEMORY_SCOPE_AGENT);
        }
    }
    if (wave >= 4) {
        const int lw = wave - 4;
        const int wn = lw & 1, kh = lw >> 1, hh = lane >> 5;
        bf16x8 w2[KH];
        const rsrc_t WR = make_rsrc(ws + WS_W2 + (2 * nt + wn) * NU * 1024);
        auto ld_w2 = [&](auto JI) {
            constexpr int j = JI;
            w2[j] = __builtin_bit_cast(bf16x8, __builtin_amdgcn_raw_buffer_load_b128(WR, lane * 16 + (j & 3) * 1024, blk_unit(j >> 2, kh) * 1024, 0));
        };
        auto ld_res = [&]() {
            const char* xb = (const char*)(xT + mt * XTILE + (64 * nt + 32 * wn) * 128 + (m2 & 1) * 64 + 32 * kh);
            const u32 vo = (lane >> 2) * 256 + (((lane & 3) ^ ((lane >> 4) & 3)) << 4);
            glds_s<1>(xb, vo, l0 + ML_XS + lw * 2048);
            glds_s<1>(xb + 4096, vo, l0 + ML_XS + lw * 2048 + 1024);
        };
        if (fg != 15) {
            const u32 xo = xsrc_off(lw & 1), wo = lane * 16;
            const char* xs = xtile + 2 * lw * 1024;
            const int up = lw + (lw >> 1) + (lw & 1), us = 1 + 3 * (lw >> 1);
            const char* wp = ws + WS_W1 + (u0 + up) * 32 * 1024;
            const char* w1 = ws + WS_W1 + ((u0 + us) * 32 + (lw & 1)) * 1024;
            auto issue = [&](auto SI) {
                constexpr int s = SI;
                const u32 dst = l0 + (s % G1_R) * G1_SLOT;
                glds_s<2>(xs + s * 8192, xo, dst + 2 * lw * 1024);
                glds_s<2>(wp + s * 2048, wo, dst + 8192 + up * 2048);
                glds_s<1>(w1 + s * 2048, wo, dst + 8192 + us * 2048 + (lw & 1) * 1024);
            };
            issue(I0{}); issue(I1{});
            ST(1);
            static_for<G1_NS>([&](auto SI) {
                constexpr int s = SI, n = g1_nis(s), n2 = g1_nis(s + 1);
                wait_vm<g1_younger(s)>();
                raw_barrier();
                if constexpr (n2 > n) issue(std::integral_constant<int, n>{});
                if constexpr (n2 > n + 1) issue(std::integral_constant<int, n + 1>{});
                constexpr int lo = g1_wlo(s), hi = s == G1_NS - 1 ? KH : g1_wlo(s + 1);
                static_for<hi - lo>([&](auto JI) { ld_w2(std::integral_constant<int, lo + JI>{}); });
                if constexpr (s == G1_NS - G1_LEAD) ST(2);
            });
        } else {
            static_for<KH>([&](auto JI) { ld_w2(JI); });
        }
        ST(3);
        ld_res();
        wait_vm<0>();
        ST(4);
        const int item = pi_fwd(lane & 31), row = 32 * wn + item;
        f32x16 acc[2];
        {
            LAS char* xs = L + ML_XS + lw * 2048 + item * 64;
            const u32x4 a = *(LAS u32x4*)(xs + (((2 * hh) ^ ((item >> 2) & 3)) << 4));
            const u32x4 c = *(LAS u32x4*)(xs + (((2 * hh + 1) ^ ((item >> 2) & 3)) << 4));
#pragma unroll
            for (int p = 0; p < 4; ++p) {
                acc[0][2 * p] = bflo(a[p]); acc[0][2 * p + 1] = bfhi(a[p]);
                acc[0][8 + 2 * p] = bflo(c[p]); acc[0][9 + 2 * p] = bfhi(c[p]);
            }
#pragma unroll
            for (int r = 0; r < 16; ++r) acc[1][r] = 0.f;
            PIN16(acc[0]); PIN16(acc[1]);
        }
        const u32 a_ex = pinned(ML_EX + lw * 4096 + lane * 16);
        const u32 a_os = pinned(layer == 0 ? ML_OS + row * 128 + (((4 * kh + 2 * hh) ^ (row & 7)) << 4) : ML_OS + (32 * kh + 16 * hh) * 128 + row * 2);
        const int rr = 4 * (lane >> 4) + ((lane & 15) >> 2), spos = 64 * m2 + 16 * lw + (lane & 15);
        const u32 a_rp = pinned(ML_OS + rr * 128 + (((2 * lw + ((lane & 3) >> 1)) ^ (rr & 7)) << 4) + 8 * (lane & 1));
        const u32 a_st = pinned((lane & 16) ? 0x80000000u : 16 * st_slot(st_thread(spos), st_item(spos), 8) + 8 * hh);
        ST(7);
        raw_barrier();
        ST(5);
        LAS char* hk = L + kh * 8192 + kh * 4096 + lane * 16;
        LAS char* hs = L + (kh ^ 1) * 8192 + kh * 4096 + lane * 16;
        bf16x8 hf[2][4];
        auto rd2 = [&](auto SI, auto HI) {
            constexpr int s = SI, hlf = HI;
#pragma unroll
            for (int j = 0; j < 2; ++j)
                if (4 * s + 2 * hlf + j < KH) {
                    hf[hlf][2 * j] = *(LAS bf16x8*)(hk + (s % G2_R) * G2_SLOT + (2 * hlf + j) * 1024);
                    hf[hlf][2 * j + 1] = *(LAS bf16x8*)(hs + (s % G2_R) * G2_SLOT + (2 * hlf + j) * 1024);
                }
        };
        auto mm2 = [&](auto SI, auto HI) {
            constexpr int s = SI, hlf = HI;
#pragma unroll
            for (int j = 0; j < 2; ++j)
                if (4 * s + 2 * hlf + j < KH) {
                    acc[0] = MFMA32(hf[hlf][2 * j], w2[4 * s + 2 * hlf + j], acc[0]);
                    acc[1] = MFMA32(hf[hlf][2 * j + 1], w2[4 * s + 2 * hlf + j], acc[1]);
                }
        };
        auto mix = [&]() {
#pragma unroll
            for (int k = 0; k < 4; ++k) { __builtin_amdgcn_sched_group_barrier(0x8, 1, 0); __builtin_amdgcn_sched_group_barrier(0x100, 1, 0); }
            PIN16(acc[0]); PIN16(acc[1]);
            sched_fence();
        };
        static_for<G2_NS>([&](auto SI) {
            constexpr int s = SI;
            raw_barrier();
            rd2(SI, I0{});
            if constexpr (s > 0) mm2(std::integral_constant<int, s - 1>{}, I1{});
            mix();
            rd2(SI, I1{});
            mm2(SI, I0{});
            mix();
        });
        mm2(std::integral_constant<int, G2_NS - 1>{}, I1{});
        ST(8);
#pragma unroll
        for (int k = 0; k < 4; ++k) *(LAS f32x4*)(L + a_ex + 1024 * k) = f32x4{acc[1][4 * k], acc[1][4 * k + 1], acc[1][4 * k + 2], acc[1][4 * k + 3]};
        wait_lgkm0();
        raw_barrier();
        u32 o[8];
#pragma unroll
        for (int k = 0; k < 4; ++k) {
            const f32x4 v = *(LAS f32x4*)(L + (a_ex ^ 8192) + 1024 * k);
            o[2 * k] = pk_bf16(acc[0][4 * k] + v[0], acc[0][4 * k + 1] + v[1]);
            o[2 * k + 1] = pk_bf16(acc[0][4 * k + 2] + v[2], acc[0][4 * k + 3] + v[3]);
        }
        if (layer != 0) {
            LAS u16* ow = (LAS u16*)(L + a_os);
#pragma unroll
            for (int p = 0; p < 8; ++p) { ow[(2 * p) * 64] = (u16)o[p]; ow[(2 * p + 1) * 64] = (u16)(o[p] >> 16); }
            wait_lgkm0();
            raw_barrier();
            ST_FLUSH(5);
            return;
        }
        *(LAS u32x4*)(L + a_os) = u32x4{o[0], o[1], o[2], o[3]};
        *(LAS u32x4*)(L + (a_os ^ 16)) = u32x4{o[4], o[5], o[6], o[7]};
        wait_lgkm0();
        raw_barrier();
        ST(9);
        float S = 0.f, Q = 0.f, S2 = 0.f, Q2 = 0.f;
#pragma unroll
        for (int i = 0; i < 4; ++i) {
            const u32x2 v = __builtin_bit_cast(u32x2, __builtin_amdgcn_ds_read_tr16_b64_v4bf16((LAS bf16x4*)(L + a_rp + i * 2048)));
            S = dot2(v[0], ONES, S); Q = dot2(v[0], v[0], Q);
            S2 = dot2(v[1], ONES, S2); Q2 = dot2(v[1], v[1], Q2);
        }
        const float v = row_pair_sum(half_sum2(S + S2, Q + Q2));
        if (a_st != 0x80000000u) atomicAdd((double*)(ws + WS_ST64 + a_st), (double)v);
        ST(10);
        ST_FLUSH(2);
        return;
    }
    if (fg != 15) {
        const int wm = wave & 1, wn = wave >> 1;
        f32x4 acc[6][4];
        bf16x8 xf[2][4], wf[2][6];
#pragma unroll
        for (int i = 0; i < 6; ++i)
#pragma unroll
            for (int t = 0; t < 4; ++t) acc[i][t] = f32x4{0.f, 0.f, 0.f, 0.f};
        int xo[4];
#pragma unroll
        for (int t = 0; t < 4; ++t) xo[t] = xfrag_off(4 * wm + t);
        const int wo = 8192 + 6 * wn * 1024 + lane * 16;
        int slot = 0;
        auto rd = [&](auto CI) {
            constexpr int c = CI;
            LAS char* base = L + slot * G1_SLOT;
            slot = slot == G1_R - 1 ? 0 : slot + 1;
            wf[c][0] = *(LAS bf16x8*)(base + wo);
#pragma unroll
            for (int t = 0; t < 4; ++t) xf[c][t] = tr_frag(base + xo[t], base + xo[t] + 1024);
#pragma unroll
            for (int i = 1; i < 6; ++i) wf[c][i] = *(LAS bf16x8*)(base + wo + i * 1024);
        };
        auto mm = [&](auto CI) {
            constexpr int c = CI;
#pragma unroll
            for (int i = 0; i < 6; ++i)
#pragma unroll
                for (int t = 0; t < 4; ++t) acc[i][t] = MFMA16(wf[c][i], xf[c][t], acc[i][t]);
        };
        auto step = [&](auto CI) {
            constexpr int c = CI;
            raw_barrier();
            rd(std::integral_constant<int, c>{});
            mm(std::integral_constant<int, c ^ 1>{});
#pragma unroll
            for (int k = 0; k < 14; ++k) { __builtin_amdgcn_sched_group_barrier(0x8, 1, 0); __builtin_amdgcn_sched_group_barrier(0x100, 1, 0); }
            PIN16(acc[5][3]);
            sched_fence();
        };
        ST(1);
        raw_barrier();
        rd(I0{});
        sched_fence();
        ST(2);
#pragma unroll 1
        for (int s = 1; s < G1_NS - 3; s += 2) { step(I1{}); step(I0{}); }
        u32 st[4];
        f32x4 cs[6];
        u32* stp = lnw + mt * 128 + 64 * wm + (lane & 15);
#pragma unroll
        for (int t = 0; t < 4; ++t) st[t] = __hip_atomic_load(stp + 16 * t, __ATOMIC_RELAXED, __HIP_MEMORY_SCOPE_AGENT);
#pragma unroll
        for (int i = 0; i < 6; ++i) cs[i] = ((const f32x4*)(ws + WS_CS))[(u0 + 3 * wn) * 8 + 4 * i + g];
        sched_fence();
        step(I1{}); step(I0{}); step(I1{});
        for (int spin = 0; spin < (1 << 22) && !__all(st[0] != 0u && st[1] != 0u && st[2] != 0u && st[3] != 0u); ++spin) {
            __builtin_amdgcn_s_sleep(2);
#pragma unroll
            for (int t = 0; t < 4; ++t) st[t] = __hip_atomic_load(stp + 16 * t, __ATOMIC_RELAXED, __HIP_MEMORY_SCOPE_AGENT);
        }
        ST(3);
        mm(I1{});
        ST(4);
        const rsrc_t HR = make_rsrc(hF + ((4 * mt + 2 * wm) * NU + u0 + 3 * wn) * 1024);
        const u32 hoff = (g >> 1) * 512 + pi_inv(16 * (g & 1) + (lane & 15)) * 16;
        auto glu = [&](int i, int t) {
            const float sb = bfhi(st[t]), nm = -bflo(st[t]);
            const float ce = -1.44269504088896341f * __builtin_amdgcn_rcpf(sb);
            const f32x2 nm2 = {nm, nm}, sb2 = {sb, sb};
            const f32x4 ga = acc[2 * i][t], ua = acc[2 * i + 1][t], cg = cs[2 * i], cu = cs[2 * i + 1];
            u32x2 o;
#pragma unroll
            for (int j = 0; j < 2; ++j) {
                const f32x2 arg = __builtin_elementwise_fma(f32x2{cg[2 * j], cg[2 * j + 1]}, nm2, f32x2{ga[2 * j], ga[2 * j + 1]}) * ce;
                const f32x2 e = {__builtin_amdgcn_exp2f(arg[0]), __builtin_amdgcn_exp2f(arg[1])};
                const f32x2 den = __builtin_elementwise_fma(e, sb2, sb2);
                const f32x2 r = {__builtin_amdgcn_rcpf(den[0]), __builtin_amdgcn_rcpf(den[1])};
                o[j] = pk_bf16(__builtin_elementwise_fma(f32x2{cu[2 * j], cu[2 * j + 1]}, nm2, f32x2{ua[2 * j], ua[2 * j + 1]}) * r);
            }
            return o;
        };
#pragma unroll
        for (int tp = 0; tp < 2; ++tp)
#pragma unroll
            for (int i = 0; i < 3; ++i) {
                const u32x2 a = glu(i, 2 * tp), b = glu(i, 2 * tp + 1);
                const auto r0 = __builtin_amdgcn_permlane16_swap(a[0], b[0], false, false);
                const auto r1 = __builtin_amdgcn_permlane16_swap(a[1], b[1], false, false);
                st16(HR, hoff + i * 1024, (u0 + 3 * wn + i < NU ? 0u : 0x80000000u) + tp * NU * 1024, u32x4{r0[0], r1[0], r0[1], r1[1]});
            }
        ST(5);
    }
    wait_vm<0>();
    ST(6);
    u32 order = 0;
    if (lane == 0) order = __hip_atomic_fetch_add((LAS u32*)(L + ML_CTR), 1u, __ATOMIC_RELAXED, __HIP_MEMORY_SCOPE_WORKGROUP);
    if (__builtin_amdgcn_readfirstlane(order) == 3u) {
        u32* cnt = (u32*)(ws + WS_CNT) + (layer * 16 + mt) * 512;
        u32 v = 0;
        if (fg == 15) __builtin_amdgcn_s_sleep(100);
        else if (lane < 16) v = __hip_atomic_fetch_add(cnt + lane * 32, 1u, __ATOMIC_RELAXED, __HIP_MEMORY_SCOPE_AGENT) + 1u;
        v = __builtin_amdgcn_readfirstlane(v);
#pragma unroll 1
        for (int spin = 0; spin < (1 << 22) && v < 15u; ++spin)
            v = __builtin_amdgcn_readfirstlane(__hip_atomic_load(cnt + fg * 32, __ATOMIC_RELAXED, __HIP_MEMORY_SCOPE_AGENT));
    }
    raw_barrier();
    ST(7);
    {
        const char* src = hF + (2 * m2 + (wave >> 1)) * NU * 1024;
        const u32 vo = lane * 16, dst = l0 + wave * 4096;
        auto issue = [&](auto SI) {
            constexpr int s = SI;
            glds_s<(s < G2_NS - 1 ? 4 : 3), true>(src + blk_unit(s, wave & 1) * 1024, vo, dst + (s % G2_R) * G2_SLOT);
        };
        constexpr int LEAD = G2_R - 2;
        auto nissued = [](int s) { int a = 2 * s + RAMP2, b = s + LEAD + (s < 2 ? 2 - s : 0); a = a < b ? a : b; return a < G2_NS ? a : G2_NS; };
        static_for<RAMP2>([&](auto SI) { issue(SI); });
        static_for<G2_NS>([&](auto SI) {
            constexpr int s = SI, n = nissued(s), n2 = nissued(s + 1);
            constexpr int fly = n - 1 - s;
            wait_vm<(fly > 0 ? 4 * fly - (n == G2_NS ? 1 : 0) : 0)>();
            if constexpr (s == 0) ST(9);
            if constexpr (s == 2) ST(10);
            if constexpr (s == 5) ST(11);
            if constexpr (s == 8) ST(12);
            raw_barrier();
            if constexpr (n2 > n) issue(std::integral_constant<int, n>{});
            if constexpr (n2 > n + 1) issue(std::integral_constant<int, n + 1>{});
        });
    }
    ST(8);
    {
        const rsrc_t xout = make_rsrc(layer ? (char*)out : ws + WS_X + 2 * WS_XSTEP);
        const int pos = 64 * m2 + 16 * wave + (lane >> 3);
        const int tok = (pos & ~255) | ((pos & 3) << 6) | ((pos >> 2) & 63);
        const u32 vo = pinned(layer ? tok * (DM * 2) + 128 * nt + ((lane & 7) << 4)
                                    : 2 * (mt * XTILE + (64 * nt + 16 * wave + (lane >> 3)) * 128 + (m2 & 1) * 64) + (((lane & 7) ^ (lane >> 3)) << 4));
        const u32 a_rd = pinned(ML_OS + wave * 2048 + lane * 16);
        raw_barrier();
        raw_barrier();
        st16(xout, vo, 0, *(LAS u32x4*)(L + a_rd));
        st16(xout, vo, 2048, *(LAS u32x4*)(L + a_rd + 1024));
    }
    ST_FLUSH(layer ? 5 : 2);
}

extern "C" void kernel_launch(void* const* d_in, const int* in_sizes, int n_in, void* d_out, int out_size,
                              void* d_ws, size_t ws_size, hipStream_t stream) {
    (void)in_sizes; (void)n_in; (void)out_size; (void)ws_size;
    const u16* x0 = (const u16*)d_in[0];
    const u16* logA = (const u16*)d_in[1];
    const u16* Aim = (const u16*)d_in[2];
    const u16* Cre = (const u16*)d_in[3];
    const u16* Cim = (const u16*)d_in[4];
    const u16* logdt = (const u16*)d_in[5];
    const u16* Dsk = (const u16*)d_in[6];
    const u16* Wg = (const u16*)d_in[8];
    const u16* Wu = (const u16*)d_in[9];
    const u16* Wd = (const u16*)d_in[10];
    char* ws = (char*)d_ws;
    (void)hipFuncSetAttribute((const void*)conv_kernel, hipFuncAttributeMaxDynamicSharedMemorySize, CV_LDS);
    (void)hipFuncSetAttribute((const void*)mlp_kernel, hipFuncAttributeMaxDynamicSharedMemorySize, ML_LDS);

    prep_kernel<<<PREP_BLOCKS, 256, PREP_LDS, stream>>>(x0, logA, Aim, Cre, Cim, logdt, Dsk, Wg, Wu, Wd, ws);
    for (int layer = 0; layer < 2; ++layer) {
        conv_kernel<<<CV_GRID, 256, CV_LDS, stream>>>(ws, layer);
        mlp_kernel<<<256, 512, ML_LDS, stream>>>(ws, (u16*)d_out, layer);
    }
}
```

```cpp
#include <hip/hip_runtime.h>
#include <hip/hip_bf16.h>
#include <stdint.h>
#include <utility>

typedef unsigned short u16;
typedef unsigned int u32;
typedef __bf16 bf16x8 __attribute__((ext_vector_type(8)));
typedef __bf16 bf16x4 __attribute__((ext_vector_type(4)));
typedef __bf16 bf16x2 __attribute__((ext_vector_type(2)));
typedef _Float16 f16x8 __attribute__((ext_vector_type(8)));
typedef _Float16 f16x2 __attribute__((ext_vector_type(2)));
typedef float f32x16 __attribute__((ext_vector_type(16)));
typedef float f32x4 __attribute__((ext_vector_type(4)));
typedef float f32x2 __attribute__((ext_vector_type(2)));
typedef double f64x2 __attribute__((ext_vector_type(2)));
typedef u32 u32x4 __attribute__((ext_vector_type(4)));
typedef u32 u32x2 __attribute__((ext_vector_type(2)));

#define LAS __attribute__((address_space(3)))
#define GAS __attribute__((address_space(1)))
#define DI __device__ __forceinline__

constexpr int SEQ = 2048, DM = 512, NST = 128, FF = 1376, NU = FF / 16;
constexpr int XTILE = DM * 128;

constexpr size_t MB = 1 << 20;
constexpr size_t WS_XA = 0;
constexpr size_t WS_XB = 2 * MB;
constexpr size_t WS_K = 4 * MB, WS_W1 = 6 * MB, WS_W2 = 9 * MB, WS_MISC = 11 * MB - 262144, WS_H = 11 * MB, WS_HSTEP = 6 * MB;
constexpr size_t WS_BYTES = 23 * MB;
constexpr size_t WS_ST64 = WS_MISC;
constexpr size_t WS_ST0 = WS_MISC + 32768;
constexpr size_t WS_CSP = WS_MISC + 65536;
constexpr size_t WS_CS = WS_MISC + 114688;
constexpr size_t WS_ZERO = WS_MISC + 131072;
constexpr size_t WS_LNW = WS_ZERO, WS_CNT = WS_LNW + 2 * SEQ * 4;
constexpr int ZERO_BYTES = 2 * SEQ * 4 + 512 * 128;
__device__ __attribute__((aligned(4096))) char g_ws[WS_BYTES];

#define TG1 4

#define ST_DECL do {} while (0)
#define ST(i) do {} while (0)
#define ST_FLUSH(kid) do {} while (0)

DI float bf2f(u16 h) { return __uint_as_float((u32)h << 16); }
DI float bflo(u32 w) { return __uint_as_float(w << 16); }
DI float bfhi(u32 w) { return __uint_as_float(w & 0xffff0000u); }
DI u32 pk_bf16(f32x2 v) { return __builtin_bit_cast(u32, __builtin_convertvector(v, bf16x2)); }
DI u32 pk_bf16(float lo, float hi) { return pk_bf16(f32x2{lo, hi}); }
DI float rbf(float x) { return bflo(pk_bf16(x, x)); }
DI u32 pk_f16(f32x2 v) { return __builtin_bit_cast(u32, __builtin_convertvector(v, f16x2)); }
DI float dot2(u32 a, u32 b, float c) { return __builtin_amdgcn_fdot2_f32_bf16(__builtin_bit_cast(bf16x2, a), __builtin_bit_cast(bf16x2, b), c, false); }
DI u32 perm_lo(u32 hi, u32 lo) { return __builtin_amdgcn_perm(hi, lo, 0x05040100u); }
DI u32 perm_hi(u32 hi, u32 lo) { return __builtin_amdgcn_perm(hi, lo, 0x07060302u); }
DI int pi_fwd(int r) { return (((r >> 2) & 1) << 4) | ((r >> 3) << 2) | (r & 3); }
DI int pi_inv(int p) { return (((p >> 2) & 3) << 3) | ((p >> 4) << 2) | (p & 3); }
DI int xt_off(int ch, int pos) { return (pos >> 7) * XTILE + ch * 128 + (pos & 127); }
DI int st_thread(int pos) { return ((pos >> 8) << 5) | (((pos >> 5) & 7) << 2) | ((pos >> 2) & 3); }
DI int st_item(int pos) { return (((pos >> 4) & 1) << 2) | (pos & 3); }
DI int st_slot(int thread, int item, int nitem) { return ((thread >> 6) * nitem + item) * 64 + (thread & 63); }
constexpr u32 ONES = 0x3f803f80u;

template <int N> DI void wait_vm() { asm volatile("s_waitcnt vmcnt(%0)" ::"n"(N) : "memory"); }
DI void wait_lgkm0() { asm volatile("s_waitcnt lgkmcnt(0)" ::: "memory"); }
DI void raw_barrier() { asm volatile("" ::: "memory"); __builtin_amdgcn_s_barrier(); asm volatile("" ::: "memory"); }
DI float opaque(float v) { asm("" : "+v"(v)); return v; }
DI u32 pinned(u32 v) { asm volatile("" : "+v"(v)); return v; }
#define PIN16(x) asm volatile("" : "+v"(x))
DI void sched_fence() { __builtin_amdgcn_sched_barrier(0); }
DI void glds16(const void* src, LAS char* dst) {
    __builtin_amdgcn_global_load_lds((const GAS void*)src, (LAS void*)dst, 16, 0, 0);
}
DI bf16x8 tr_frag(LAS char* a0, LAS char* a1) {
    bf16x4 lo = __builtin_amdgcn_ds_read_tr16_b64_v4bf16((LAS bf16x4*)a0);
    bf16x4 hi = __builtin_amdgcn_ds_read_tr16_b64_v4bf16((LAS bf16x4*)a1);
    return __builtin_shufflevector(lo, hi, 0, 1, 2, 3, 4, 5, 6, 7);
}
#define GLDS_HEAD "s_mov_b32 %0, m0\n\ts_mov_b32 m0, %3\n\ts_nop 0\n\t"
#define GLDS_ONE(off, sc) "global_load_lds_dwordx4 %1, %2 offset:" #off sc "\n\t"
#define GLDS_TAIL "s_mov_b32 m0, %0" : "=&s"(keep) : "v"(voff), "s"(ubase), "s"(lds_dst) : "memory"
#define GLDS_BODY(sc)                                                                                                          \
    if constexpr (N == 1) asm volatile(GLDS_HEAD GLDS_ONE(0, sc) GLDS_TAIL);                                                   \
    else if constexpr (N == 2) asm volatile(GLDS_HEAD GLDS_ONE(0, sc) GLDS_ONE(1024, sc) GLDS_TAIL);                           \
    else if constexpr (N == 3) asm volatile(GLDS_HEAD GLDS_ONE(0, sc) GLDS_ONE(1024, sc) GLDS_ONE(2048, sc) GLDS_TAIL);        \
    else asm volatile(GLDS_HEAD GLDS_ONE(0, sc) GLDS_ONE(1024, sc) GLDS_ONE(2048, sc) GLDS_ONE(3072, sc) GLDS_TAIL)
template <int N, bool DEV = false> DI void glds_s(const char* ubase, u32 voff, u32 lds_dst) {
    u32 keep;
    lds_dst = __builtin_amdgcn_readfirstlane(lds_dst);
    if constexpr (DEV) { GLDS_BODY(" sc1"); } else { GLDS_BODY(""); }
}
typedef __amdgpu_buffer_rsrc_t rsrc_t;
DI rsrc_t make_rsrc(void* base) { return __builtin_amdgcn_make_buffer_rsrc(base, 0, 0x7fffffff, 0x00020000); }
template <int AUX = 16> DI void st16(rsrc_t r, u32 voff, u32 soff, u32x4 v) { __builtin_amdgcn_raw_buffer_store_b128(v, r, voff + soff, 0, AUX); }
template <int AUX = 16> DI void st8(rsrc_t r, u32 voff, u32 soff, u32x2 v) { __builtin_amdgcn_raw_buffer_store_b64(v, r, voff, soff, AUX); }
#define H_AUX 16
#define MFMA16(a, b, c) __builtin_amdgcn_mfma_f32_16x16x32_bf16((a), (b), (c), 0, 0, 0)
#define MFMA32(a, b, c) __builtin_amdgcn_mfma_f32_32x32x16_bf16((a), (b), (c), 0, 0, 0)

template <class F, int... I> DI void static_for_impl(F&& f, std::integer_sequence<int, I...>) { (f(std::integral_constant<int, I>{}), ...); }
template <int N, class F> DI void static_for(F&& f) { static_for_impl(f, std::make_integer_sequence<int, N>{}); }

template <int CTRL, int RM = 0xf> DI float dppz(float v) { return __int_as_float(__builtin_amdgcn_update_dpp(0, __float_as_int(v), CTRL, RM, 0xf, true)); }
DI float wave_sum63(float v) {
    v += dppz<0x111>(v); v += dppz<0x112>(v); v += dppz<0x114>(v); v += dppz<0x118>(v);
    v += dppz<0x142, 0xa>(v); v += dppz<0x143, 0xc>(v);
    return v;
}
DI float wave_max63(float v) {
    v = fmaxf(v, dppz<0x111>(v)); v = fmaxf(v, dppz<0x112>(v)); v = fmaxf(v, dppz<0x114>(v)); v = fmaxf(v, dppz<0x118>(v));
    v = fmaxf(v, dppz<0x142, 0xa>(v)); v = fmaxf(v, dppz<0x143, 0xc>(v));
    return v;
}
DI float rdlane63(float v) { return __int_as_float(__builtin_amdgcn_readlane(__float_as_int(v), 63)); }
DI float half_sum2(float a, float b) {
    auto r = __builtin_amdgcn_permlane32_swap(__float_as_uint(a), __float_as_uint(b), false, false);
    return __uint_as_float(r[0]) + __uint_as_float(r[1]);
}
DI float row_pair_sum(float a) {
    auto r = __builtin_amdgcn_permlane16_swap(__float_as_uint(a), __float_as_uint(a), false, false);
    return __uint_as_float(r[0]) + __uint_as_float(r[1]);
}
DI void ln_scalars(float S, float Q, float& mb, float& sb) {
    const float mean = S * (1.f / DM);
    const float var = fmaxf(__builtin_fmaf(-mean, mean, Q * (1.f / DM)), 0.f);
    const u32 mv = pk_bf16(mean, var);
    mb = bflo(mv);
    sb = rbf(__builtin_amdgcn_sqrtf(rbf(bfhi(mv) + 1e-5f)));
}

DI f32x2 cmul(f32x2 a, f32x2 b) {
    f32x2 t, r;
    asm("v_pk_mul_f32 %0, %1, %2 op_sel_hi:[0,1]" : "=v"(t) : "v"(a), "v"(b));
    asm("v_pk_fma_f32 %0, %1, %2, %3 op_sel:[1,1,0] op_sel_hi:[1,0,1] neg_lo:[1,0,0]" : "=v"(r) : "v"(a), "v"(b), "v"(t));
    return r;
}
DI f32x2 cexp_p(float arl, float aiv, float p) {
    const float mag = __builtin_amdgcn_exp2f(arl * p);
    const float th = __builtin_amdgcn_fractf(aiv * p);
    return f32x2{mag * __builtin_amdgcn_cosf(th), mag * __builtin_amdgcn_sinf(th)};
}
DI float exp_bf(float x) {
    const float t = x * 1.44269504088896341f;
    const float r = __builtin_fmaf(x, 1.44269504088896341f, -t) + x * 1.92596299e-8f;
    const float e = __builtin_amdgcn_exp2f(t);
    return rbf(__builtin_fmaf(e * r, 0.693147181f, e));
}

constexpr int KG_ROW = 528;
constexpr int KG_B = 21 * KG_ROW, KG_TAB = 53 * KG_ROW, PREP_LDS = KG_TAB + 2320 * 2;
constexpr int PB_KGEN = 512, PB_W1 = 176, PB_W2 = 88, PB_TR = 128, PB_ST = 128;
constexpr int PREP_BLOCKS = PB_KGEN + PB_TR + PB_ST + PB_W1 + PB_W2;

extern "C" __global__ __launch_bounds__(256, 2) void s4_prep_kernel(
    const u16* __restrict__ x0, const u16* __restrict__ logA, const u16* __restrict__ Aim,
    const u16* __restrict__ Cre, const u16* __restrict__ Cim, const u16* __restrict__ logdt, const u16* __restrict__ Dskip,
    const u16* __restrict__ Wg, const u16* __restrict__ Wu, const u16* __restrict__ Wd) {
    char* ws = g_ws;
    extern __shared__ __attribute__((aligned(16))) char smem_raw[];
    LAS char* L = (LAS char*)smem_raw;
    u32 warm = 0;
    {
        const u32 k = (blockIdx.x >> 3) * 256 + threadIdx.x;
        unsigned long long pc;
        int lo, hi;
        asm("s_getpc_b64 %0\n1:\n\ts_mov_b32 %1, s4_prep_kernel-1b\n\ts_mov_b32 %2, s4_tail_kernel-1b" : "=s"(pc), "=s"(lo), "=s"(hi));
        if (lo + (int)(k * 128u) < hi && k < 1024u) warm = *(const GAS u32*)(pc + lo + k * 128ull);
    }
    asm volatile("" ::"s"(x0), "s"(logA), "s"(Aim), "s"(Cre), "s"(Cim), "s"(logdt), "s"(Dskip), "s"(Wg), "s"(Wu), "s"(Wd));
    const rsrc_t WS = make_rsrc(ws);
    const int tid = threadIdx.x, lane = tid & 63;
    const int wave = __builtin_amdgcn_readfirstlane(tid >> 6);
    int b = blockIdx.x;
    ST_DECL;
    if (b < PB_KGEN) {
        const int d = b, n = tid & 127;
        const u16 h_dt = logdt[d], h_D = Dskip[d], h_la = logA[d * NST + n], h_ai = Aim[d * NST + n];
        const u16 h_cr = Cre[d * NST + n], h_ci = Cim[d * NST + n];
        const u32 ca = ((const u32*)(Cre + d * NST))[lane], cc = ((const u32*)(Cim + d * NST))[lane];
        const float dt = exp_bf(bf2f(h_dt));
        const float arl = -exp_bf(bf2f(h_la)) * dt * 1.44269504088896341f;
        float aiv = bf2f(h_ai) * dt * 0.159154943091895336f;
        float mx = fmaxf(fmaxf(fabsf(bflo(ca)), fabsf(bfhi(ca))), fmaxf(fabsf(bflo(cc)), fabsf(bfhi(cc))));
        mx = rdlane63(wave_max63(mx));
        ST(1);
        const bool okm = mx > 1e-30f && mx < 1e30f;
        const float sc = okm ? 256.f * __builtin_amdgcn_rcpf(mx) : 1.f;
        const float back = dt * (2.f / 256.f) / sc;
        if (wave < 2) {
            aiv = -aiv;
            const f32x2 t0 = {bf2f(h_cr) * sc, -bf2f(h_ci) * sc};
            const f32x2 w1 = cexp_p(arl, aiv, 1.f), v = cexp_p(arl, aiv, 256.f);
            const f32x2 w2 = cmul(w1, w1), w4 = cmul(w2, w2);
            LAS u32* A = (LAS u32*)(L + 4 * n);
            auto put = [&](int row, f32x2 z) { A[row * (KG_ROW / 4)] = pk_f16(z); };
            const f32x2 r1 = cmul(t0, w1), r2 = cmul(t0, w2), r3 = cmul(r1, w2);
            put(0, t0); put(1, r1); put(2, r2); put(3, r3);
            put(4, cmul(t0, w4)); put(5, cmul(r1, w4)); put(6, cmul(r2, w4)); put(7, cmul(r3, w4));
            f32x2 t = cmul(t0, v);
            const f32x2 r9 = cmul(t, w2);
            put(8, t); put(9, r9); put(10, cmul(t, w4)); put(11, cmul(r9, w4));
            t = cmul(t, v); put(12, t); put(13, cmul(t, w4));
            t = cmul(t, v); put(14, t); put(15, cmul(t, w4));
#pragma unroll
            for (int r = 16; r <= 20; ++r) { t = cmul(t, v); put(r, t); }
        } else {
            f32x2 e[16];
            const f32x2 b1 = cexp_p(arl, aiv, 8.f), b8 = cexp_p(arl, aiv, 64.f);
            const f32x2 b2 = cmul(b1, b1), b4 = cmul(b2, b2), b16 = cmul(b8, b8);
            LAS u32* B = (LAS u32*)(L + KG_B + 4 * n);
            e[0] = f32x2{256.f, 0.f}; e[1] = b1 * 256.f; e[2] = b2 * 256.f; e[3] = cmul(e[1], b2);
            e[4] = b4 * 256.f; e[8] = b8 * 256.f;
#pragma unroll
            for (int c = 1; c < 4; ++c) e[4 + c] = cmul(e[c], b4);
#pragma unroll
            for (int c = 1; c < 8; ++c) e[8 + c] = cmul(e[c], b8);
#pragma unroll
            for (int c = 0; c < 16; ++c) {
                B[c * (KG_ROW / 4)] = pk_f16(e[c]);
                B[(16 + c) * (KG_ROW / 4)] = pk_f16(c ? cmul(e[c], b16) : b16 * 256.f);
            }
        }
        const int rb = wave & 1, cb = wave >> 1, q = lane >> 4;
        const int col8 = 8 * (16 * cb + (lane & 15));
        int ph[4];
#pragma unroll
        for (int j = 0; j < 4; ++j)
            ph[j] = col8 + (rb ? (q == 0 ? 1024 + 256 * j : (q == 1 && j == 0) ? 2048 : 2049)
                               : (q < 2 ? 4 * q + j : q == 2 ? 256 + 2 * j : 512 + 256 * (j >> 1) + 4 * (j & 1)));
        ST(2);
        __syncthreads();
        ST(3);
        {
            LAS char* pa = L + (16 * rb + (lane & 15)) * KG_ROW + q * 16;
            LAS char* pb = L + KG_B + (16 * cb + (lane & 15)) * KG_ROW + q * 16;
            f32x4 acc = {0.f, 0.f, 0.f, 0.f}, acc2 = {0.f, 0.f, 0.f, 0.f};
#pragma unroll
            for (int ks = 0; ks < 8; ks += 2) {
                acc = __builtin_amdgcn_mfma_f32_16x16x32_f16(*(LAS f16x8*)(pa + 64 * ks), *(LAS f16x8*)(pb + 64 * ks), acc, 0, 0, 0);
                acc2 = __builtin_amdgcn_mfma_f32_16x16x32_f16(*(LAS f16x8*)(pa + 64 * ks + 64), *(LAS f16x8*)(pb + 64 * ks + 64), acc2, 0, 0, 0);
            }
            acc += acc2;
            LAS u16* tab = (LAS u16*)(L + KG_TAB);
            const float add0 = tid == 0 ? bf2f(h_D) : 0.f;
            const u32 v01 = pk_bf16(__builtin_fmaf(acc[0], back, add0), acc[1] * back), v23 = pk_bf16(acc[2] * back, acc[3] * back);
            tab[ph[0]] = (u16)v01; tab[ph[1]] = (u16)(v01 >> 16);
            tab[ph[2]] = (u16)v23; tab[ph[3]] = (u16)(v23 >> 16);
        }
        ST(4);
        __syncthreads();
        ST(5);
        {
            const u32x4 w = *(LAS u32x4*)(L + KG_TAB + 16 * tid);
            const u32 w4 = *(LAS u32*)(L + KG_TAB + 16 * tid + 16);
            u32x4 o;
            if (wave == 0) {
                const u32x4 s1 = {perm_lo(w[0], w[0]), perm_lo(w[2], w[1]), perm_lo(w[2], w[2]), perm_lo(w4, w[3])};
                o = lane < 32 ? w : s1;
            } else if (wave == 1) {
                o = u32x4{perm_lo(w[0], w[0]), perm_lo(w[2], w[0]), perm_lo(w[2], w[2]), perm_lo(w4, w4)};
            } else {
                const u32 lo = perm_lo(w[0], w[0]);
                o = u32x4{lo, lo, perm_lo(w4, (lane & 1) ? w4 : w[0]), perm_lo(w4, w4)};
            }
            st16(WS, 16 * tid, WS_K + d * SEQ * 2, o);
        }
        asm volatile("" ::"v"(warm));
        ST(6);
        ST_FLUSH(0);
        return;
    }
    b -= PB_KGEN;
    if (b < PB_W1 + PB_W2) {
        const bool is1 = b < PB_W1;
        const int bb = is1 ? b : b - PB_W1;
        const int ct = is1 ? bb >> 3 : bb & 7, ty = (bb >> 2) & 1, rt = is1 ? bb & 3 : bb >> 3;
        const int pitch = is1 ? FF * 2 : DM * 2, nrow = is1 ? DM : FF;
        const char* W = (const char*)(is1 ? (ty ? Wu : Wg) : Wd);
        const int cbytes = is1 && ct == 21 ? 64 : 128;
        {
            const int r0 = 128 * rt + 32 * wave + (lane >> 3);
            const int co = ct * 128 + (((lane & 7) * 16) & (cbytes - 1));
#pragma unroll
            for (int i = 0; i < 4; ++i)
                glds_s<1>(W, (u32)(min(r0 + 8 * i, nrow - 1) * pitch + co), (u32)(uintptr_t)L + wave * 4096 + i * 1024);
        }
        ST(1);
        wait_vm<0>();
        ST(2);
        const u32 lb = (u32)(uintptr_t)L + wave * 4096;
        const int g = lane >> 4, q = (lane & 15) >> 2, pp = lane & 3;
        u32x2 f[8];
        if (is1) {
            const u32 a = lb + (8 * g + q) * 128 + pp * 8;
            asm volatile("ds_read_b64_tr_b16 %0, %8\n\tds_read_b64_tr_b16 %1, %8 offset:512\n\t"
                         "ds_read_b64_tr_b16 %2, %8 offset:32\n\tds_read_b64_tr_b16 %3, %8 offset:544\n\t"
                         "ds_read_b64_tr_b16 %4, %8 offset:64\n\tds_read_b64_tr_b16 %5, %8 offset:576\n\t"
                         "ds_read_b64_tr_b16 %6, %8 offset:96\n\tds_read_b64_tr_b16 %7, %8 offset:608\n\ts_waitcnt lgkmcnt(0)"
                         : "=&v"(f[0]), "=&v"(f[1]), "=&v"(f[2]), "=&v"(f[3]), "=&v"(f[4]), "=&v"(f[5]), "=&v"(f[6]), "=&v"(f[7])
                         : "v"(a) : "memory");
            LAS float* red = (LAS float*)(L + 16384);
#pragma unroll
            for (int i = 0; i < 4; ++i) {
                const int u = 4 * ct + i;
                if (u < NU) st16(WS, lane * 16, WS_W1 + ((u * 16 + 4 * rt + wave) * 2 + ty) * 1024, u32x4{f[2 * i][0], f[2 * i][1], f[2 * i + 1][0], f[2 * i + 1][1]});
            }
#pragma unroll
            for (int i = 0; i < 4; i += 2) {
                const float s0 = dot2(f[2 * i][0], ONES, dot2(f[2 * i][1], ONES, dot2(f[2 * i + 1][0], ONES, dot2(f[2 * i + 1][1], ONES, 0.f))));
                const float s1 = dot2(f[2 * i + 2][0], ONES, dot2(f[2 * i + 2][1], ONES, dot2(f[2 * i + 3][0], ONES, dot2(f[2 * i + 3][1], ONES, 0.f))));
                const float v = row_pair_sum(half_sum2(s0, s1));
                if ((lane & 16) == 0) red[wave * 64 + (i + (lane >> 5)) * 16 + (lane & 15)] = v;
            }
            __syncthreads();
            if (tid < 64 && 4 * ct + (tid >> 4) < NU)
                ((float*)(ws + WS_CSP))[((rt * NU + 4 * ct + (tid >> 4)) * 2 + ty) * 16 + (tid & 15)] =
                    (red[tid] + red[64 + tid]) + (red[128 + tid] + red[192 + tid]);
        } else {
            const u32 a = lb + (8 * (g >> 1) + q) * 128 + pi_fwd(16 * (g & 1) + 4 * pp) * 2;
            asm volatile("ds_read_b64_tr_b16 %0, %8\n\tds_read_b64_tr_b16 %1, %8 offset:512\n\t"
                         "ds_read_b64_tr_b16 %2, %8 offset:64\n\tds_read_b64_tr_b16 %3, %8 offset:576\n\t"
                         "ds_read_b64_tr_b16 %4, %8 offset:2048\n\tds_read_b64_tr_b16 %5, %8 offset:2560\n\t"
                         "ds_read_b64_tr_b16 %6, %8 offset:2112\n\tds_read_b64_tr_b16 %7, %8 offset:2624\n\ts_waitcnt lgkmcnt(0)"
                         : "=&v"(f[0]), "=&v"(f[1]), "=&v"(f[2]), "=&v"(f[3]), "=&v"(f[4]), "=&v"(f[5]), "=&v"(f[6]), "=&v"(f[7])
                         : "v"(a) : "memory");
#pragma unroll
            for (int i = 0; i < 4; ++i) {
                const int u = 8 * rt + 2 * wave + (i >> 1), cb = 2 * ct + (i & 1);
                if (u < NU) st16(WS, lane * 16, WS_W2 + (cb * NU + u) * 1024, u32x4{f[2 * i][0], f[2 * i][1], f[2 * i + 1][0], f[2 * i + 1][1]});
            }
        }
        ST(3);
        ST_FLUSH(0);
        return;
    }
    b -= PB_W1 + PB_W2;
    if (b < PB_TR) {
        const int w = b * 4 + wave, tau = w >> 6, lg = (w >> 4) & 3, cgg = w & 15;
        const int ln = lg * 16 + (lane & 15), cg = cgg * 4 + (lane >> 4);
        u32x4 in[4];
#pragma unroll
        for (int g = 0; g < 4; ++g) in[g] = *(const u32x4*)(x0 + (256 * tau + 64 * g + ln) * DM + 8 * cg);
        const u32 dst = 2 * xt_off(8 * cg, 256 * tau + 4 * ln);
#pragma unroll
        for (int q = 0; q < 4; ++q) {
            st8(WS, dst + (2 * q) * 256, WS_XA, u32x2{perm_lo(in[1][q], in[0][q]), perm_lo(in[3][q], in[2][q])});
            st8(WS, dst + (2 * q + 1) * 256, WS_XA, u32x2{perm_hi(in[1][q], in[0][q]), perm_hi(in[3][q], in[2][q])});
        }
        ST_FLUSH(0);
        return;
    }
    b -= PB_TR;
    {
        const int w = b * 4 + wave, tau = w >> 6, ln = w & 63;
        u32x4 in[4];
#pragma unroll
        for (int g = 0; g < 4; ++g) in[g] = *(const u32x4*)(x0 + (256 * tau + 64 * g + ln) * DM + 8 * lane);
        if ((b * 256 + tid) * 16 < ZERO_BYTES) st16(WS, (b * 256 + tid) * 16, WS_ZERO, u32x4{0u, 0u, 0u, 0u});
        if (b >= 96) st16(WS, ((b - 96) * 256 + tid) * 16, WS_W1 + NU * 32 * 1024, u32x4{0u, 0u, 0u, 0u});
#pragma unroll
        for (int g = 0; g < 4; ++g) {
            float S = 0.f, Q = 0.f;
#pragma unroll
            for (int q = 0; q < 4; ++q) { S = dot2(in[g][q], ONES, S); Q = dot2(in[g][q], in[g][q], Q); }
            S = wave_sum63(S); Q = wave_sum63(Q);
            const int pos = 256 * tau + 4 * ln + g;
            const u32 dst = lane == 63 ? 16 * st_slot(st_thread(pos), st_item(pos), 8) : 0x80000000u;
            const u32x4 v = __builtin_bit_cast(u32x4, f64x2{(double)S, (double)Q});
            st16(WS, dst, WS_ST64, v);
            st16(WS, dst, WS_ST0, v);
        }
        ST_FLUSH(0);
    }
}

constexpr int CV_KROW = (64 + SEQ) * 2;
constexpr int CV_ZRS = 84 * 16;
constexpr int CV_Z = 2 * CV_KROW;
constexpr int CV_C = CV_Z + 2 * 4 * CV_ZRS;
constexpr int CV_DUMP = CV_C + 2 * 33 * 16;
constexpr int CV_X = CV_DUMP + 1024 + 33 * 16;
constexpr int CV_S = CV_X + 2 * SEQ * 2;
constexpr int CV_LDS = CV_S + SEQ * 16;
#define PF_SLEEP 12
#define CV_GRID 512

template <int HALF> struct TauSet { static constexpr int t(int i) { return HALF == 0 ? (i == 0 ? 0 : i == 1 ? 2 : i == 2 ? 5 : 7) : (i == 0 ? 1 : i == 1 ? 3 : i == 2 ? 4 : 6); } };

template <int HALF>
DI void conv_main(LAS char* L, rsrc_t xout, u32 xoff, int ch, int lane
) {
    using TS = TauSet<HALF>;
    constexpr int TMAX = TS::t(3);
    f32x16 acc[4];
#pragma unroll
    for (int i = 0; i < 4; ++i)
#pragma unroll
        for (int r = 0; r < 16; ++r) acc[i][r] = 0.f;
    const int hh = lane >> 5;
    LAS char* zb = L + CV_Z + (ch * 4 + (lane & 3)) * CV_ZRS + 128 + 16 * (((lane & 31) >> 2) + hh) - 16 * 7;
    LAS char* kb = L + ch * CV_KROW + 128 + 2 * (16 * ((lane >> 4) & 1) - 32 * hh - 4 * ((lane & 15) >> 2) + 4 * (lane & 3)) - 32;
    const int cbase = CV_C + ch * 33 * 16;
    LAS char* cw = L + (lane == 63 ? cbase + 16 : CV_DUMP + lane * 16);
    LAS char* cr[4];
#pragma unroll
    for (int i = 0; i < 4; ++i) cr[i] = L + (lane < 3 ? cbase + 4 + 4 * lane + 64 * TS::t(i) : 0);
    u32x2 xm[4];
    bf16x8 kf[2][TMAX + 1], zf[2][TMAX + 1];
    auto load = [&](auto EI) {
        constexpr int ei = EI, e = 2 * ei + 1;
#pragma unroll
        for (int m = 0; m <= TMAX; ++m) {
            kf[ei & 1][m] = tr_frag(kb + 32 + 64 * (8 * m + e), kb + 64 * (8 * m + e));
            zf[ei & 1][m] = *(LAS bf16x8*)(zb + 16 * (8 * m + 7 - e));
            if constexpr (ei == 0) sched_fence();
        }
    };
    load(std::integral_constant<int, 0>{});
    static_for<3>([&](auto EI) {
        constexpr int ei = EI;
        sched_fence();
        load(std::integral_constant<int, ei + 1>{});
        if constexpr (ei == 2) {
#pragma unroll
            for (int i = 0; i < 4; ++i) xm[i] = *(LAS u32x2*)(L + CV_X + ch * 4096 + 512 * TS::t(i) + 8 * lane);
        }
#pragma unroll
        for (int i = 0; i < 4; ++i)
#pragma unroll
            for (int mu = 0; mu <= TS::t(i); ++mu) acc[i] = MFMA32(zf[ei & 1][TS::t(i) - mu], kf[ei & 1][mu], acc[i]);
        if constexpr (ei == 0) __builtin_amdgcn_sched_group_barrier(0x8, 5, 0);
#pragma unroll
        for (int k = 0; k < 13; ++k) { __builtin_amdgcn_sched_group_barrier(0x8, 1, 0); __builtin_amdgcn_sched_group_barrier(0x100, 2, 0); }
        PIN16(acc[0]); PIN16(acc[1]); PIN16(acc[2]); PIN16(acc[3]);
    });
    sched_fence();
    float y[4][4];
    auto post = [&](auto II) {
        constexpr int i = II;
        float s1[4], s2[4];
#pragma unroll
        for (int g = 0; g < 4; ++g) {
            s2[g] = opaque(acc[i][4 * g + 2] + dppz<0x138>(acc[i][4 * g + 3]));
            y[i][g] = opaque(acc[i][4 * g] + ((g & 1) ? bfhi(xm[i][g >> 1]) : bflo(xm[i][g >> 1])));
        }
#pragma unroll
        for (int g = 0; g < 4; ++g) s1[g] = opaque(acc[i][4 * g + 1] + dppz<0x138>(s2[g]));
#pragma unroll
        for (int g = 0; g < 4; ++g) {
            y[i][g] = opaque(y[i][g] + dppz<0x138>(s1[g]));
            *(LAS f32x4*)(cw + (4 * TS::t(i) + g) * 16) = f32x4{y[i][g], s1[g], s2[g], acc[i][4 * g + 3]};
        }
    };
    static_for<4>([&](auto II) {
        constexpr int i = II;
#pragma unroll
        for (int mu = 0; mu <= TS::t(i); ++mu) acc[i] = MFMA32(zf[1][TS::t(i) - mu], kf[1][mu], acc[i]);
        if constexpr (i > 0) {
            post(std::integral_constant<int, i - 1>{});
            __builtin_amdgcn_sched_group_barrier(0x8, 2, 0);
#pragma unroll
            for (int k = 2; k <= TS::t(i); ++k) { __builtin_amdgcn_sched_group_barrier(0x202, 6, 0); __builtin_amdgcn_sched_group_barrier(0x8, 1, 0); }
        }
        PIN16(acc[i]);
        sched_fence();
    });
    ST(4);
    post(std::integral_constant<int, 3>{});
    ST(5);
    wait_lgkm0();
    raw_barrier();
    ST(6);
    float cv[4][4];
#pragma unroll
    for (int i = 0; i < 4; ++i)
#pragma unroll
        for (int g = 0; g < 4; ++g) cv[i][g] = *(LAS float*)(cr[i] + 16 * g);
    static_for<4>([&](auto II) {
        constexpr int i = II;
        st8(xout, xoff, 4 * TS::t(i) * XTILE, u32x2{pk_bf16(opaque(TS::t(i) ? y[i][0] + cv[i][0] : y[i][0]), opaque(y[i][1] + cv[i][1])),
                                                    pk_bf16(opaque(y[i][2] + cv[i][2]), opaque(y[i][3] + cv[i][3]))});
    });
}

extern "C" __global__ __launch_bounds__(256, 2) void s4_conv_kernel(int layer) {
    char* ws = g_ws;
    extern __shared__ __attribute__((aligned(16))) char smem_raw[];
    LAS char* L = (LAS char*)smem_raw;
    ST_DECL;
    const int tid = threadIdx.x, lane = tid & 63, wave = __builtin_amdgcn_readfirstlane(tid >> 6);
    if (blockIdx.x >= 256) {
        const int xcd = blockIdx.x & 7, sl = ((blockIdx.x - 256) >> 3) * 4 + wave;
        constexpr int P1 = (96 / (8 / TG1)) * 32, N1 = (P1 + 127) / 128;
        const int k1 = (xcd / TG1) * P1 + sl * N1;
        const u32 dst = (u32)(uintptr_t)L + wave * 1024;
        if (layer == 0 && blockIdx.x < 256 + 3) {
            const int i = (blockIdx.x - 256) * 256 + tid;
            const f32x4* cp = (const f32x4*)(ws + WS_CSP) + i;
            if (i < NU * 8) ((f32x4*)(ws + WS_CS))[i] = (cp[0] + cp[NU * 8]) + (cp[2 * NU * 8] + cp[3 * NU * 8]);
        }
        __builtin_amdgcn_s_sleep(PF_SLEEP);
#pragma unroll 1
        for (int i = 0; i < N1; ++i) glds_s<1>(ws + WS_W1 + (size_t)min(k1 + i, 90 * 32 - 1) * 1024, lane * 16, dst);
        wait_vm<0>();
        return;
    }
    const u16* xin = (const u16*)(ws + WS_XA);
    const rsrc_t xout = make_rsrc(ws + WS_XB);
    const int ch = wave >> 1, half = wave & 1, d0 = 2 * blockIdx.x, d = d0 + ch;
    const int tau = tid >> 5, c = tid & 3, sp = (tid >> 2) & 7;
    const int p0 = 256 * tau + 32 * sp + 4 * c;
    const u32 l0 = (u32)(uintptr_t)L;
    {
        const int rot = (blockIdx.x >> 3) & 3;
        const char* sb = ws + WS_ST64 + wave * 8192;
#pragma unroll
        for (int j = 0; j < 4; ++j) {
            const int k = (j + rot) & 3;
            glds_s<2>(sb + k * 2048, lane * 16, l0 + CV_S + wave * 8192 + k * 2048);
        }
    }
    {
        const char* xb = (const char*)(xin + 4 * wave * XTILE + d0 * 128);
        const u32 vo = (lane >> 4) * (XTILE * 2) + (lane & 15) * 16;
        glds_s<1>(xb, vo, l0 + CV_X + wave * 1024);
        glds_s<1>(xb + 256, vo, l0 + CV_X + 4096 + wave * 1024);
    }
    glds_s<2>(ws + WS_K + (size_t)(d * SEQ + half * 1024) * 2, lane * 16, l0 + ch * CV_KROW + 128 + half * 2048);
    const u32 xoff = 2 * xt_off(d, 4 * lane);
    if (wave == 0) *(LAS u32x4*)(L + CV_Z + (lane >> 3) * CV_ZRS + 16 * (lane & 7)) = u32x4{0u, 0u, 0u, 0u};
    if (wave == 1) *(LAS u32x4*)(L + ((lane >> 3) & 1) * CV_KROW + 16 * (lane & 7)) = u32x4{0u, 0u, 0u, 0u};
    ST(1);
    f32x2 rc[2][4];
    LAS char* sl = L + CV_S + wave * 8192 + lane * 16;
    wait_vm<4>();
    ST(8);
#pragma unroll
    for (int j = 0; j < 8; ++j) {
        const f64x2 sq = *(LAS f64x2*)(sl + j * 1024);
        const float nm = (float)sq[0] * (-1.f / DM);
        const float var = fmaxf(__builtin_fmaf(-nm, nm, (float)sq[1] * (1.f / DM)), 0.f);
        const float rs = __builtin_amdgcn_rsqf(var + 1e-5f);
        rc[j >> 2][j & 3] = f32x2{rs, nm * rs};
    }
    wait_vm<2>();
    u32x2 xz[2][2];
#pragma unroll
    for (int k = 0; k < 2; ++k)
#pragma unroll
        for (int s = 0; s < 2; ++s) xz[k][s] = *(LAS u32x2*)(L + CV_X + k * 4096 + (p0 + 16 * s) * 2);
    ST(9);
    {
        LAS char* zw = L + CV_Z + c * CV_ZRS + 128 + 2 * (64 * tau + 2 * sp);
#pragma unroll
        for (int k = 0; k < 2; ++k)
#pragma unroll
            for (int g = 0; g < 4; ++g) {
                const float a = (g & 1) ? bfhi(xz[k][0][g >> 1]) : bflo(xz[k][0][g >> 1]);
                const float e = (g & 1) ? bfhi(xz[k][1][g >> 1]) : bflo(xz[k][1][g >> 1]);
                *(LAS u32*)(zw + k * 4 * CV_ZRS + 32 * g) =
                    pk_bf16(__builtin_fmaf(a, rc[0][g][0], rc[0][g][1]), __builtin_fmaf(e, rc[1][g][0], rc[1][g][1]));
            }
    }
    ST(2);
    asm volatile("s_waitcnt vmcnt(0) lgkmcnt(0)" ::: "memory");
    raw_barrier();
    ST(3);
    if (half) conv_main<1>(L, xout, xoff, ch, lane); else conv_main<0>(L, xout, xoff, ch, lane);
    ST(7);
    ST_FLUSH(layer == 0 ? 1 : 4);
}

#define G1_R 6
#define MAP2 1
#define ROT2 0
#define RAMP2 4
#define W2_START 2
#define W2_PER 0
#define W2_TAIL 6
constexpr int G1_NS = 16, G1_SLOT = 20480;
constexpr int G2_NS = 11, G2_R = 7, G2_SLOT = 16384, KH = NU / 2;
constexpr int ML_RED = 131072, ML_OS = ML_RED, ML_XS = ML_RED + 8192, ML_EX = ML_XS + 8192, ML_LDS = ML_EX + 16384;
constexpr int ML_CTR = ML_EX;

constexpr int G1_LEAD = G1_R - 2;
constexpr int g1_nis(int s) { int a = 2 * s + 2, b = s + G1_LEAD; a = a < b ? a : b; return a < G1_NS ? a : G1_NS; }
constexpr int g1_wlo(int s) {
    int a = s <= W2_START ? 0 : s <= G1_NS - G1_LEAD ? W2_PER * (s - W2_START)
                              : W2_PER * (G1_NS - G1_LEAD - W2_START) + W2_TAIL * (s - (G1_NS - G1_LEAD));
    return a < KH ? a : KH;
}
constexpr int g1_younger(int s) {
    int t = -1;
    while (g1_nis(t + 1) <= s) ++t;
    return 5 * (g1_nis(s) - 1 - s) + g1_wlo(s) - g1_wlo(t < 0 ? 0 : t);
}

extern "C" __global__ __launch_bounds__(512) void s4_mlp_kernel(u16* out, int layer_arg) {
    char* ws = g_ws;
    extern __shared__ __attribute__((aligned(16))) char smem_raw[];
    LAS char* L = (LAS char*)smem_raw;
    const u16* xT = (const u16*)(ws + WS_XB);
    auto late_layer = [&]() { int v = layer_arg; asm volatile("" : "+s"(v)); return v; };
    const int lane = threadIdx.x & 63, wave = __builtin_amdgcn_readfirstlane(threadIdx.x >> 6);
    const int xcd = blockIdx.x & 7, sl = blockIdx.x >> 3;
    constexpr int MPX = 16 / TG1;
    const int mt = (xcd % TG1) * MPX + (sl % MPX), fg = (xcd / TG1) * (32 / MPX) + (sl / MPX);
    const int m2 = 2 * mt + (fg & 1), nt = fg >> 1;
    const int rot = ROT2 == 0 ? 0 : ROT2 == 1 ? (nt & 1) * 5 : MAP2 ? (nt & 3) * 5 >> 1 : nt + (nt >> 2);
    auto blk_unit = [&](int s, int kh) { return KH * kh + 4 * (s == G2_NS - 1 ? s : s + rot < G2_NS - 1 ? s + rot : s + rot - (G2_NS - 1)); };
    const u32 l0 = (u32)(uintptr_t)L;
    ST_DECL;
    const int g = lane >> 4, q = (lane & 15) >> 2;
    auto xfrag_off = [&](int tb) { return (8 * g + q) * 256 + ((tb ^ (q | ((g & 1) << 2))) << 5) + (lane & 3) * 8; };
    auto xsrc_off = [&](int odd) { return (u32)(g * 256 + ((((lane & 15) >> 1) ^ (g | (odd << 2))) << 5) + (lane & 1) * 16); };
    const char* xtile = (const char*)(xT + mt * XTILE);
    const int u0 = fg * 6;
    using I0 = std::integral_constant<int, 0>; using I1 = std::integral_constant<int, 1>;
    if (wave == 0) { *(LAS u32*)(L + ML_CTR) = 0u; wait_lgkm0(); }

    if (fg == 15) {
        const u32 mine = l0 + wave * 16384;
        const u32 so0 = xsrc_off(0), so1 = xsrc_off(1);
#pragma unroll
        for (int i = 0; i < 8; ++i) glds_s<2>(xtile + (16 * wave + 2 * i) * 1024, (i & 1) ? so1 : so0, mine + i * 2048);
        float S[8], Q[8];
        u32 a[8];
#pragma unroll
        for (int tb = 0; tb < 8; ++tb) { a[tb] = mine + xfrag_off(tb); S[tb] = 0.f; Q[tb] = 0.f; }
        static_for<4>([&](auto KI) {
            constexpr int kk = KI >> 1, tq = (KI & 1) * 4;
            if constexpr (tq == 0) wait_vm<8 - 8 * kk>();
            u32x2 f[8];
            asm volatile("ds_read_b64_tr_b16 %0, %8 offset:%c12\n\tds_read_b64_tr_b16 %1, %8 offset:%c13\n\t"
                         "ds_read_b64_tr_b16 %2, %9 offset:%c12\n\tds_read_b64_tr_b16 %3, %9 offset:%c13\n\t"
                         "ds_read_b64_tr_b16 %4, %10 offset:%c12\n\tds_read_b64_tr_b16 %5, %10 offset:%c13\n\t"
                         "ds_read_b64_tr_b16 %6, %11 offset:%c12\n\tds_read_b64_tr_b16 %7, %11 offset:%c13\n\ts_waitcnt lgkmcnt(0)"
                         : "=&v"(f[0]), "=&v"(f[1]), "=&v"(f[2]), "=&v"(f[3]), "=&v"(f[4]), "=&v"(f[5]), "=&v"(f[6]), "=&v"(f[7])
                         : "v"(a[tq]), "v"(a[tq + 1]), "v"(a[tq + 2]), "v"(a[tq + 3]), "i"(kk * 8192), "i"(kk * 8192 + 1024) : "memory");
#pragma unroll
            for (int j = 0; j < 4; ++j)
#pragma unroll
                for (int tb = 0; tb < 4; ++tb) {
                    const u32 w = f[2 * tb + (j >> 1)][j & 1];
                    S[tq + tb] = dot2(w, ONES, S[tq + tb]); Q[tq + tb] = dot2(w, w, Q[tq + tb]);
                }
        });
        LAS float* red = (LAS float*)(L + ML_RED);
#pragma unroll
        for (int tb = 0; tb < 8; ++tb) {
            const float v = row_pair_sum(half_sum2(S[tb], Q[tb]));
            if ((lane & 16) == 0) red[wave * 256 + (lane >> 5) * 128 + 16 * tb + (lane & 15)] = v;
        }
        wait_lgkm0();
        raw_barrier();
        if (threadIdx.x < 128) {
            float s = 0.f, qq = 0.f;
#pragma unroll
            for (int w = 0; w < 8; ++w) { s += red[w * 256 + threadIdx.x]; qq += red[w * 256 + 128 + threadIdx.x]; }
            float mb, sb;
            ln_scalars(s, qq, mb, sb);
            __hip_atomic_store((u32*)(ws + WS_LNW) + late_layer() * SEQ + mt * 128 + threadIdx.x, pk_bf16(mb, sb), __ATOMIC_RELAXED, __HIP_MEMORY_SCOPE_AGENT);
        }
    }
    if (wave >= 4) {
        const int lw = wave - 4;
        const int wn = lw & 1, kh = lw >> 1, hh = lane >> 5;
        bf16x8 w2[KH];
        const rsrc_t WR = make_rsrc(ws + WS_W2 + (2 * nt + wn) * NU * 1024);
        auto ld_w2 = [&](auto JI) {
            constexpr int j = JI;
            w2[j] = __builtin_bit_cast(bf16x8, __builtin_amdgcn_raw_buffer_load_b128(WR, lane * 16 + (j & 3) * 1024, blk_unit(j >> 2, kh) * 1024, 0));
        };
        auto ld_res = [&]() {
            const char* xb = (const char*)(xT + mt * XTILE + (64 * nt + 32 * wn) * 128 + (m2 & 1) * 64 + 32 * kh);
            const u32 vo = (lane >> 2) * 256 + (((lane & 3) ^ ((lane >> 4) & 3)) << 4);
            glds_s<1>(xb, vo, l0 + ML_XS + lw * 2048);
            glds_s<1>(xb + 4096, vo, l0 + ML_XS + lw * 2048 + 1024);
        };
        if (fg != 15) {
            const u32 xo = xsrc_off(lw & 1), wo = lane * 16;
            const char* xs = xtile + 2 * lw * 1024;
            const int up = lw + (lw >> 1) + (lw & 1), us = 1 + 3 * (lw >> 1);
            const char* wp = ws + WS_W1 + (u0 + up) * 32 * 1024;
            const char* w1 = ws + WS_W1 + ((u0 + us) * 32 + (lw & 1)) * 1024;
            auto issue = [&](auto SI) {
                constexpr int s = SI;
                const u32 dst = l0 + (s % G1_R) * G1_SLOT;
                glds_s<2>(xs + s * 8192, xo, dst + 2 * lw * 1024);
                glds_s<2>(wp + s * 2048, wo, dst + 8192 + up * 2048);
                glds_s<1>(w1 + s * 2048, wo, dst + 8192 + us * 2048 + (lw & 1) * 1024);
            };
            issue(I0{}); issue(I1{});
            ST(1);
            static_for<G1_NS>([&](auto SI) {
                constexpr int s = SI, n = g1_nis(s), n2 = g1_nis(s + 1);
                wait_vm<g1_younger(s)>();
                raw_barrier();
                if constexpr (n2 > n) issue(std::integral_constant<int, n>{});
                if constexpr (n2 > n + 1) issue(std::integral_constant<int, n + 1>{});
                constexpr int lo = g1_wlo(s), hi = s == G1_NS - 1 ? KH : g1_wlo(s + 1);
                static_for<hi - lo>([&](auto JI) { ld_w2(std::integral_constant<int, lo + JI>{}); });
                if constexpr (s == G1_NS - G1_LEAD) ST(2);
            });
        } else {
            static_for<KH>([&](auto JI) { ld_w2(JI); });
        }
        ST(3);
        const int layer = late_layer();
        ld_res();
        wait_vm<0>();
        ST(4);
        const int item = pi_fwd(lane & 31), row = 32 * wn + item;
        f32x16 acc[2];
        {
            LAS char* xs = L + ML_XS + lw * 2048 + item * 64;
            const u32x4 a = *(LAS u32x4*)(xs + (((2 * hh) ^ ((item >> 2) & 3)) << 4));
            const u32x4 c = *(LAS u32x4*)(xs + (((2 * hh + 1) ^ ((item >> 2) & 3)) << 4));
#pragma unroll
            for (int p = 0; p < 4; ++p) {
                acc[0][2 * p] = bflo(a[p]); acc[0][2 * p + 1] = bfhi(a[p]);
                acc[0][8 + 2 * p] = bflo(c[p]); acc[0][9 + 2 * p] = bfhi(c[p]);
            }
#pragma unroll
            for (int r = 0; r < 16; ++r) acc[1][r] = 0.f;
            PIN16(acc[0]); PIN16(acc[1]);
        }
        const u32 a_ex = pinned(ML_EX + lw * 4096 + lane * 16);
        const u32 a_os = pinned(layer == 0 ? ML_OS + row * 128 + (((4 * kh + 2 * hh) ^ (row & 7)) << 4) : ML_OS + (32 * kh + 16 * hh) * 128 + row * 2);
        const int rr = 4 * (lane >> 4) + ((lane & 15) >> 2), spos = 64 * m2 + 16 * lw + (lane & 15);
        const u32 a_rp = pinned(ML_OS + rr * 128 + (((2 * lw + ((lane & 3) >> 1)) ^ (rr & 7)) << 4) + 8 * (lane & 1));
        const u32 a_st = pinned((lane & 16) ? 0x80000000u : 16 * st_slot(st_thread(spos), st_item(spos), 8) + 8 * hh);
        double old = 0.0;
        if (nt == 0 && a_st != 0x80000000u) old = *(const double*)(ws + WS_ST0 + a_st);
        asm volatile("" : "+v"(old));
        ST(7);
        raw_barrier();
        ST(5);
        LAS char* hk = L + kh * 8192 + kh * 4096 + lane * 16;
        LAS char* hs = L + (kh ^ 1) * 8192 + kh * 4096 + lane * 16;
        bf16x8 hf[2][4];
        auto rd2 = [&](auto SI, auto HI) {
            constexpr int s = SI, hlf = HI;
#pragma unroll
            for (int j = 0; j < 2; ++j)
                if (4 * s + 2 * hlf + j < KH) {
                    hf[hlf][2 * j] = *(LAS bf16x8*)(hk + (s % G2_R) * G2_SLOT + (2 * hlf + j) * 1024);
                    hf[hlf][2 * j + 1] = *(LAS bf16x8*)(hs + (s % G2_R) * G2_SLOT + (2 * hlf + j) * 1024);
                }
        };
        auto mm2 = [&](auto SI, auto HI) {
            constexpr int s = SI, hlf = HI;
#pragma unroll
            for (int j = 0; j < 2; ++j)
                if (4 * s + 2 * hlf + j < KH) {
                    acc[0] = MFMA32(hf[hlf][2 * j], w2[4 * s + 2 * hlf + j], acc[0]);
                    acc[1] = MFMA32(hf[hlf][2 * j + 1], w2[4 * s + 2 * hlf + j], acc[1]);
                }
        };
        auto mix = [&]() {
#pragma unroll
            for (int k = 0; k < 4; ++k) { __builtin_amdgcn_sched_group_barrier(0x8, 1, 0); __builtin_amdgcn_sched_group_barrier(0x100, 1, 0); }
            PIN16(acc[0]); PIN16(acc[1]);
            sched_fence();
        };
        static_for<G2_NS>([&](auto SI) {
            constexpr int s = SI;
            raw_barrier();
            rd2(SI, I0{});
            if constexpr (s > 0) mm2(std::integral_constant<int, s - 1>{}, I1{});
            mix();
            rd2(SI, I1{});
            mm2(SI, I0{});
            mix();
        });
        mm2(std::integral_constant<int, G2_NS - 1>{}, I1{});
        ST(8);
#pragma unroll
        for (int k = 0; k < 4; ++k) *(LAS f32x4*)(L + a_ex + 1024 * k) = f32x4{acc[1][4 * k], acc[1][4 * k + 1], acc[1][4 * k + 2], acc[1][4 * k + 3]};
        wait_lgkm0();
        raw_barrier();
        u32 o[8];
#pragma unroll
        for (int k = 0; k < 4; ++k) {
            const f32x4 v = *(LAS f32x4*)(L + (a_ex ^ 8192) + 1024 * k);
            o[2 * k] = pk_bf16(acc[0][4 * k] + v[0], acc[0][4 * k + 1] + v[1]);
            o[2 * k + 1] = pk_bf16(acc[0][4 * k + 2] + v[2], acc[0][4 * k + 3] + v[3]);
        }
        if (layer != 0) {
            LAS u16* ow = (LAS u16*)(L + a_os);
#pragma unroll
            for (int p = 0; p < 8; ++p) { ow[(2 * p) * 64] = (u16)o[p]; ow[(2 * p + 1) * 64] = (u16)(o[p] >> 16); }
            wait_lgkm0();
            raw_barrier();
            ST_FLUSH(5);
            return;
        }
        *(LAS u32x4*)(L + a_os) = u32x4{o[0], o[1], o[2], o[3]};
        *(LAS u32x4*)(L + (a_os ^ 16)) = u32x4{o[4], o[5], o[6], o[7]};
        wait_lgkm0();
        raw_barrier();
        ST(9);
        float S = 0.f, Q = 0.f, S2 = 0.f, Q2 = 0.f;
#pragma unroll
        for (int i = 0; i < 4; ++i) {
            const u32x2 v = __builtin_bit_cast(u32x2, __builtin_amdgcn_ds_read_tr16_b64_v4bf16((LAS bf16x4*)(L + a_rp + i * 2048)));
            S = dot2(v[0], ONES, S); Q = dot2(v[0], v[0], Q);
            S2 = dot2(v[1], ONES, S2); Q2 = dot2(v[1], v[1], Q2);
        }
        const float v = row_pair_sum(half_sum2(S + S2, Q + Q2));
        if (a_st != 0x80000000u) atomicAdd((double*)(ws + WS_ST64 + a_st), (double)v - old);
        ST(10);
        ST_FLUSH(2);
        return;
    }
    const int layer = late_layer();
    char* hF = ws + WS_H + (1 - layer) * WS_HSTEP;
    if (fg != 15) {
        const int wm = wave & 1, wn = wave >> 1;
        f32x4 acc[6][4];
        bf16x8 xf[2][4], wf[2][6];
#pragma unroll
        for (int i = 0; i < 6; ++i)
#pragma unroll
            for (int t = 0; t < 4; ++t) acc[i][t] = f32x4{0.f, 0.f, 0.f, 0.f};
        int xo[4];
#pragma unroll
        for (int t = 0; t < 4; ++t) xo[t] = xfrag_off(4 * wm + t);
        const int wo = 8192 + 6 * wn * 1024 + lane * 16;
        int slot = 0;
        auto rd = [&](auto CI) {
            constexpr int c = CI;
            LAS char* base = L + slot * G1_SLOT;
            slot = slot == G1_R - 1 ? 0 : slot + 1;
            wf[c][0] = *(LAS bf16x8*)(base + wo);
#pragma unroll
            for (int t = 0; t < 4; ++t) xf[c][t] = tr_frag(base + xo[t], base + xo[t] + 1024);
#pragma unroll
            for (int i = 1; i < 6; ++i) wf[c][i] = *(LAS bf16x8*)(base + wo + i * 1024);
        };
        auto mm = [&](auto CI) {
            constexpr int c = CI;
#pragma unroll
            for (int i = 0; i < 6; ++i)
#pragma unroll
                for (int t = 0; t < 4; ++t) acc[i][t] = MFMA16(wf[c][i], xf[c][t], acc[i][t]);
        };
        auto step = [&](auto CI) {
            constexpr int c = CI;
            raw_barrier();
            rd(std::integral_constant<int, c>{});
            mm(std::integral_constant<int, c ^ 1>{});
#pragma unroll
            for (int k = 0; k < 14; ++k) { __builtin_amdgcn_sched_group_barrier(0x8, 1, 0); __builtin_amdgcn_sched_group_barrier(0x100, 1, 0); }
            PIN16(acc[5][3]);
            sched_fence();
        };
        ST(1);
        raw_barrier();
        rd(I0{});
        sched_fence();
        ST(2);
#pragma unroll 1
        for (int s = 1; s < G1_NS - 3; s += 2) { step(I1{}); step(I0{}); }
        u32 st[4];
        f32x4 cs[6];
        u32* stp = (u32*)(ws + WS_LNW) + layer * SEQ + mt * 128 + 64 * wm + (lane & 15);
#pragma unroll
        for (int t = 0; t < 4; ++t) st[t] = __hip_atomic_load(stp + 16 * t, __ATOMIC_RELAXED, __HIP_MEMORY_SCOPE_AGENT);
#pragma unroll
        for (int i = 0; i < 6; ++i) cs[i] = ((const f32x4*)(ws + WS_CS))[(u0 + 3 * wn) * 8 + 4 * i + g];
        sched_fence();
        step(I1{}); step(I0{}); step(I1{});
        for (int spin = 0; spin < (1 << 22) && !__all(st[0] != 0u && st[1] != 0u && st[2] != 0u && st[3] != 0u); ++spin) {
            __builtin_amdgcn_s_sleep(2);
#pragma unroll
            for (int t = 0; t < 4; ++t) st[t] = __hip_atomic_load(stp + 16 * t, __ATOMIC_RELAXED, __HIP_MEMORY_SCOPE_AGENT);
        }
        ST(3);
        mm(I1{});
        ST(4);
        const rsrc_t HR = make_rsrc(hF + ((4 * mt + 2 * wm) * NU + u0 + 3 * wn) * 1024);
        const u32 hoff = (g >> 1) * 512 + pi_inv(16 * (g & 1) + (lane & 15)) * 16;
        auto glu = [&](int i, int t) {
            const float sb = bfhi(st[t]), nm = -bflo(st[t]);
            const float ce = -1.44269504088896341f * __builtin_amdgcn_rcpf(sb);
            const f32x2 nm2 = {nm, nm}, sb2 = {sb, sb};
            const f32x4 ga = acc[2 * i][t], ua = acc[2 * i + 1][t], cg = cs[2 * i], cu = cs[2 * i + 1];
            u32x2 o;
#pragma unroll
            for (int j = 0; j < 2; ++j) {
                const f32x2 arg = __builtin_elementwise_fma(f32x2{cg[2 * j], cg[2 * j + 1]}, nm2, f32x2{ga[2 * j], ga[2 * j + 1]}) * ce;
                const f32x2 e = {__builtin_amdgcn_exp2f(arg[0]), __builtin_amdgcn_exp2f(arg[1])};
                const f32x2 den = __builtin_elementwise_fma(e, sb2, sb2);
                const f32x2 r = {__builtin_amdgcn_rcpf(den[0]), __builtin_amdgcn_rcpf(den[1])};
                o[j] = pk_bf16(__builtin_elementwise_fma(f32x2{cu[2 * j], cu[2 * j + 1]}, nm2, f32x2{ua[2 * j], ua[2 * j + 1]}) * r);
            }
            return o;
        };
#pragma unroll
        for (int tp = 0; tp < 2; ++tp)
#pragma unroll
            for (int i = 0; i < 3; ++i) {
                const u32x2 a = glu(i, 2 * tp), b = glu(i, 2 * tp + 1);
                const auto r0 = __builtin_amdgcn_permlane16_swap(a[0], b[0], false, false);
                const auto r1 = __builtin_amdgcn_permlane16_swap(a[1], b[1], false, false);
                st16(HR, hoff + i * 1024, (u0 + 3 * wn + i < NU ? 0u : 0x80000000u) + tp * NU * 1024, u32x4{r0[0], r1[0], r0[1], r1[1]});
            }
        ST(5);
    }
    wait_vm<0>();
    ST(6);
    u32 order = 0;
    if (lane == 0) order = __hip_atomic_fetch_add((LAS u32*)(L + ML_CTR), 1u, __ATOMIC_RELAXED, __HIP_MEMORY_SCOPE_WORKGROUP);
    if (__builtin_amdgcn_readfirstlane(order) == 3u) {
        u32* cnt = (u32*)(ws + WS_CNT) + (layer * 16 + mt) * 512;
        u32 v = 0;
        if (fg == 15) __builtin_amdgcn_s_sleep(100);
        else if (lane < 16) v = __hip_atomic_fetch_add(cnt + lane * 32, 1u, __ATOMIC_RELAXED, __HIP_MEMORY_SCOPE_AGENT) + 1u;
        v = __builtin_amdgcn_readfirstlane(v);
#pragma unroll 1
        for (int spin = 0; spin < (1 << 22) && v < 15u; ++spin)
            v = __builtin_amdgcn_readfirstlane(__hip_atomic_load(cnt + fg * 32, __ATOMIC_RELAXED, __HIP_MEMORY_SCOPE_AGENT));
    }
    raw_barrier();
    ST(7);
    {
        const char* src = hF + (2 * m2 + (wave >> 1)) * NU * 1024;
        const u32 vo = lane * 16, dst = l0 + wave * 4096;
        auto issue = [&](auto SI) {
            constexpr int s = SI;
            glds_s<(s < G2_NS - 1 ? 4 : 3), true>(src + blk_unit(s, wave & 1) * 1024, vo, dst + (s % G2_R) * G2_SLOT);
        };
        constexpr int LEAD = G2_R - 2;
        auto nissued = [](int s) { int a = 2 * s + RAMP2, b = s + LEAD + (s < 2 ? 2 - s : 0); a = a < b ? a : b; return a < G2_NS ? a : G2_NS; };
        static_for<RAMP2>([&](auto SI) { issue(SI); });
        static_for<G2_NS>([&](auto SI) {
            constexpr int s = SI, n = nissued(s), n2 = nissued(s + 1);
            constexpr int fly = n - 1 - s;
            wait_vm<(fly > 0 ? 4 * fly - (n == G2_NS ? 1 : 0) : 0)>();
            if constexpr (s == 0) ST(9);
            if constexpr (s == 2) ST(10);
            if constexpr (s == 5) ST(11);
            if constexpr (s == 8) ST(12);
            raw_barrier();
            if constexpr (n2 > n) issue(std::integral_constant<int, n>{});
            if constexpr (n2 > n + 1) issue(std::integral_constant<int, n + 1>{});
        });
    }
    ST(8);
    {
        const rsrc_t xout = make_rsrc(layer ? (char*)out : ws + WS_XA);
        const int pos = 64 * m2 + 16 * wave + (lane >> 3);
        const int tok = (pos & ~255) | ((pos & 3) << 6) | ((pos >> 2) & 63);
        const u32 vo = pinned(layer ? tok * (DM * 2) + 128 * nt + ((lane & 7) << 4)
                                    : 2 * (mt * XTILE + (64 * nt + 16 * wave + (lane >> 3)) * 128 + (m2 & 1) * 64) + (((lane & 7) ^ (lane >> 3)) << 4));
        const u32 a_rd = pinned(ML_OS + wave * 2048 + lane * 16);
        raw_barrier();
        raw_barrier();
        st16(xout, vo, 0, *(LAS u32x4*)(L + a_rd));
        st16(xout, vo, 2048, *(LAS u32x4*)(L + a_rd + 1024));
    }
    ST_FLUSH(layer ? 5 : 2);
}

extern "C" __global__ void s4_tail_kernel() {}

extern "C" void kernel_launch(void* const* d_in, const int* in_sizes, int n_in, void* d_out, int out_size,
                              void* d_ws, size_t ws_size, hipStream_t stream) {
    (void)in_sizes; (void)n_in; (void)out_size; (void)ws_size;
    const u16* x0 = (const u16*)d_in[0];
    const u16* logA = (const u16*)d_in[1];
    const u16* Aim = (const u16*)d_in[2];
    const u16* Cre = (const u16*)d_in[3];
    const u16* Cim = (const u16*)d_in[4];
    const u16* logdt = (const u16*)d_in[5];
    const u16* Dsk = (const u16*)d_in[6];
    const u16* Wg = (const u16*)d_in[8];
    const u16* Wu = (const u16*)d_in[9];
    const u16* Wd = (const u16*)d_in[10];
    (void)d_ws;
    (void)hipFuncSetAttribute((const void*)s4_conv_kernel, hipFuncAttributeMaxDynamicSharedMemorySize, CV_LDS);
    (void)hipFuncSetAttribute((const void*)s4_mlp_kernel, hipFuncAttributeMaxDynamicSharedMemorySize, ML_LDS);

    s4_prep_kernel<<<PREP_BLOCKS, 256, PREP_LDS, stream>>>(x0, logA, Aim, Cre, Cim, logdt, Dsk, Wg, Wu, Wd);
    for (int layer = 0; layer < 2; ++layer) {
        s4_conv_kernel<<<CV_GRID, 256, CV_LDS, stream>>>(layer);
        s4_mlp_kernel<<<256, 512, ML_LDS, stream>>>((u16*)d_out, layer);
    }
}
```

```cpp
#include <hip/hip_runtime.h>
#include <hip/hip_bf16.h>
#include <stdint.h>
#include <utility>

typedef unsigned short u16;
typedef unsigned int u32;
typedef __bf16 bf16x8 __attribute__((ext_vector_type(8)));
typedef __bf16 bf16x4 __attribute__((ext_vector_type(4)));
typedef __bf16 bf16x2 __attribute__((ext_vector_type(2)));
typedef _Float16 f16x8 __attribute__((ext_vector_type(8)));
typedef _Float16 f16x2 __attribute__((ext_vector_type(2)));
typedef float f32x16 __attribute__((ext_vector_type(16)));
typedef float f32x4 __attribute__((ext_vector_type(4)));
typedef float f32x2 __attribute__((ext_vector_type(2)));
typedef double f64x2 __attribute__((ext_vector_type(2)));
typedef u32 u32x4 __attribute__((ext_vector_type(4)));
typedef u32 u32x2 __attribute__((ext_vector_type(2)));

#define LAS __attribute__((address_space(3)))
#define GAS __attribute__((address_space(1)))
#define DI __device__ __forceinline__

constexpr int SEQ = 2048, DM = 512, NST = 128, FF = 1376, NU = FF / 16;
constexpr int XTILE = DM * 128;

constexpr size_t MB = 1 << 20;
constexpr size_t WS_XA = 0;
constexpr size_t WS_XB = 2 * MB;
constexpr size_t WS_K = 4 * MB, WS_W1 = 6 * MB, WS_W2 = 9 * MB, WS_MISC = 11 * MB - 262144, WS_H = 11 * MB, WS_HSTEP = 6 * MB;
constexpr size_t WS_BYTES = 23 * MB;
constexpr size_t WS_ST64 = WS_MISC;
constexpr size_t WS_ST0 = WS_MISC + 32768;
constexpr size_t WS_CSP = WS_MISC + 65536;
constexpr size_t WS_ZERO = WS_MISC + 131072;
constexpr size_t WS_LNW = WS_ZERO, WS_CNT = WS_LNW + 2 * SEQ * 4;
constexpr int ZERO_BYTES = 2 * SEQ * 4 + 512 * 128;
__device__ __attribute__((aligned(4096))) char g_ws[WS_BYTES];

#define TG1 4

#define ST_DECL do {} while (0)
#define ST(i) do {} while (0)
#define ST_FLUSH(kid) do {} while (0)

DI float bf2f(u16 h) { return __uint_as_float((u32)h << 16); }
DI float bflo(u32 w) { return __uint_as_float(w << 16); }
DI float bfhi(u32 w) { return __uint_as_float(w & 0xffff0000u); }
DI u32 pk_bf16(f32x2 v) { return __builtin_bit_cast(u32, __builtin_convertvector(v, bf16x2)); }
DI u32 pk_bf16(float lo, float hi) { return pk_bf16(f32x2{lo, hi}); }
DI float rbf(float x) { return bflo(pk_bf16(x, x)); }
DI u32 pk_f16(f32x2 v) { return __builtin_bit_cast(u32, __builtin_convertvector(v, f16x2)); }
DI float dot2(u32 a, u32 b, float c) { return __builtin_amdgcn_fdot2_f32_bf16(__builtin_bit_cast(bf16x2, a), __builtin_bit_cast(bf16x2, b), c, false); }
DI u32 perm_lo(u32 hi, u32 lo) { return __builtin_amdgcn_perm(hi, lo, 0x05040100u); }
DI u32 perm_hi(u32 hi, u32 lo) { return __builtin_amdgcn_perm(hi, lo, 0x07060302u); }
DI int pi_fwd(int r) { return (((r >> 2) & 1) << 4) | ((r >> 3) << 2) | (r & 3); }
DI int pi_inv(int p) { return (((p >> 2) & 3) << 3) | ((p >> 4) << 2) | (p & 3); }
DI int xt_off(int ch, int pos) { return (pos >> 7) * XTILE + ch * 128 + (pos & 127); }
DI int st_thread(int pos) { return ((pos >> 8) << 5) | (((pos >> 5) & 7) << 2) | ((pos >> 2) & 3); }
DI int st_item(int pos) { return (((pos >> 4) & 1) << 2) | (pos & 3); }
DI int st_slot(int thread, int item, int nitem) { return ((thread >> 6) * nitem + item) * 64 + (thread & 63); }
constexpr u32 ONES = 0x3f803f80u;

template <int N> DI void wait_vm() { asm volatile("s_waitcnt vmcnt(%0)" ::"n"(N) : "memory"); }
DI void wait_lgkm0() { asm volatile("s_waitcnt lgkmcnt(0)" ::: "memory"); }
DI void raw_barrier() { asm volatile("" ::: "memory"); __builtin_amdgcn_s_barrier(); asm volatile("" ::: "memory"); }
DI float opaque(float v) { asm("" : "+v"(v)); return v; }
DI u32 pinned(u32 v) { asm volatile("" : "+v"(v)); return v; }
#define PIN16(x) asm volatile("" : "+v"(x))
DI void sched_fence() { __builtin_amdgcn_sched_barrier(0); }
DI void glds16(const void* src, LAS char* dst) {
    __builtin_amdgcn_global_load_lds((const GAS void*)src, (LAS void*)dst, 16, 0, 0);
}
DI bf16x8 tr_frag(LAS char* a0, LAS char* a1) {
    bf16x4 lo = __builtin_amdgcn_ds_read_tr16_b64_v4bf16((LAS bf16x4*)a0);
    bf16x4 hi = __builtin_amdgcn_ds_read_tr16_b64_v4bf16((LAS bf16x4*)a1);
    return __builtin_shufflevector(lo, hi, 0, 1, 2, 3, 4, 5, 6, 7);
}
#define GLDS_HEAD "s_mov_b32 %0, m0\n\ts_mov_b32 m0, %3\n\ts_nop 0\n\t"
#define GLDS_ONE(off, sc) "global_load_lds_dwordx4 %1, %2 offset:" #off sc "\n\t"
#define GLDS_TAIL "s_mov_b32 m0, %0" : "=&s"(keep) : "v"(voff), "s"(ubase), "s"(lds_dst) : "memory"
#define GLDS_BODY(sc)                                                                                                          \
    if constexpr (N == 1) asm volatile(GLDS_HEAD GLDS_ONE(0, sc) GLDS_TAIL);                                                   \
    else if constexpr (N == 2) asm volatile(GLDS_HEAD GLDS_ONE(0, sc) GLDS_ONE(1024, sc) GLDS_TAIL);                           \
    else if constexpr (N == 3) asm volatile(GLDS_HEAD GLDS_ONE(0, sc) GLDS_ONE(1024, sc) GLDS_ONE(2048, sc) GLDS_TAIL);        \
    else asm volatile(GLDS_HEAD GLDS_ONE(0, sc) GLDS_ONE(1024, sc) GLDS_ONE(2048, sc) GLDS_ONE(3072, sc) GLDS_TAIL)
template <int N, bool DEV = false> DI void glds_s(const char* ubase, u32 voff, u32 lds_dst) {
    u32 keep;
    lds_dst = __builtin_amdgcn_readfirstlane(lds_dst);
    if constexpr (DEV) { GLDS_BODY(" sc1"); } else { GLDS_BODY(""); }
}
typedef __amdgpu_buffer_rsrc_t rsrc_t;
DI rsrc_t make_rsrc(void* base) { return __builtin_amdgcn_make_buffer_rsrc(base, 0, 0x7fffffff, 0x00020000); }
template <int AUX = 16> DI void st16(rsrc_t r, u32 voff, u32 soff, u32x4 v) { __builtin_amdgcn_raw_buffer_store_b128(v, r, voff + soff, 0, AUX); }
template <int AUX = 16> DI void st8(rsrc_t r, u32 voff, u32 soff, u32x2 v) { __builtin_amdgcn_raw_buffer_store_b64(v, r, voff, soff, AUX); }
#define H_AUX 16
#define MFMA16(a, b, c) __builtin_amdgcn_mfma_f32_16x16x32_bf16((a), (b), (c), 0, 0, 0)
#define MFMA32(a, b, c) __builtin_amdgcn_mfma_f32_32x32x16_bf16((a), (b), (c), 0, 0, 0)

template <class F, int... I> DI void static_for_impl(F&& f, std::integer_sequence<int, I...>) { (f(std::integral_constant<int, I>{}), ...); }
template <int N, class F> DI void static_for(F&& f) { static_for_impl(f, std::make_integer_sequence<int, N>{}); }

template <int CTRL, int RM = 0xf> DI float dppz(float v) { return __int_as_float(__builtin_amdgcn_update_dpp(0, __float_as_int(v), CTRL, RM, 0xf, true)); }
DI float wave_sum63(float v) {
    v += dppz<0x111>(v); v += dppz<0x112>(v); v += dppz<0x114>(v); v += dppz<0x118>(v);
    v += dppz<0x142, 0xa>(v); v += dppz<0x143, 0xc>(v);
    return v;
}
DI float wave_max63(float v) {
    v = fmaxf(v, dppz<0x111>(v)); v = fmaxf(v, dppz<0x112>(v)); v = fmaxf(v, dppz<0x114>(v)); v = fmaxf(v, dppz<0x118>(v));
    v = fmaxf(v, dppz<0x142, 0xa>(v)); v = fmaxf(v, dppz<0x143, 0xc>(v));
    return v;
}
DI float rdlane63(float v) { return __int_as_float(__builtin_amdgcn_readlane(__float_as_int(v), 63)); }
DI float half_sum2(float a, float b) {
    auto r = __builtin_amdgcn_permlane32_swap(__float_as_uint(a), __float_as_uint(b), false, false);
    return __uint_as_float(r[0]) + __uint_as_float(r[1]);
}
DI float row_pair_sum(float a) {
    auto r = __builtin_amdgcn_permlane16_swap(__float_as_uint(a), __float_as_uint(a), false, false);
    return __uint_as_float(r[0]) + __uint_as_float(r[1]);
}
DI void ln_scalars(float S, float Q, float& mb, float& sb) {
    const float mean = S * (1.f / DM);
    const float var = fmaxf(__builtin_fmaf(-mean, mean, Q * (1.f / DM)), 0.f);
    const u32 mv = pk_bf16(mean, var);
    mb = bflo(mv);
    sb = rbf(__builtin_amdgcn_sqrtf(rbf(bfhi(mv) + 1e-5f)));
}

struct cm { f32x2 b, r; };
DI cm mk(f32x2 b) { return {b, f32x2{-b[1], b[0]}}; }
DI f32x2 cmul(f32x2 a, const cm& m) {
    return __builtin_elementwise_fma(__builtin_shufflevector(a, a, 1, 1), m.r, __builtin_shufflevector(a, a, 0, 0) * m.b);
}
DI f32x2 cexp_p(float arl, float aiv, float p) {
    const float mag = __builtin_amdgcn_exp2f(arl * p);
    const float th = __builtin_amdgcn_fractf(aiv * p);
    return f32x2{mag * __builtin_amdgcn_cosf(th), mag * __builtin_amdgcn_sinf(th)};
}
DI float exp_bf(float x) {
    const float t = x * 1.44269504088896341f;
    const float r = __builtin_fmaf(x, 1.44269504088896341f, -t) + x * 1.92596299e-8f;
    const float e = __builtin_amdgcn_exp2f(t);
    return rbf(__builtin_fmaf(e * r, 0.693147181f, e));
}

constexpr int KG_ROW = 528;
constexpr int KG_TROW = 80;
constexpr int KG_B = 21 * KG_ROW, KG_TAB = 53 * KG_ROW, PREP_LDS = KG_TAB + 32 * KG_TROW;
constexpr int PB_WARM = 16, PB_KGEN = 512, PB_TR = 128, PB_ST = 128;
constexpr int PREP_BLOCKS = PB_WARM + PB_KGEN + PB_TR + PB_ST;

extern "C" __global__ __launch_bounds__(256, 2) void s4_prep_kernel(
    const u16* __restrict__ x0, const u16* __restrict__ logA, const u16* __restrict__ Aim,
    const u16* __restrict__ Cre, const u16* __restrict__ Cim, const u16* __restrict__ logdt, const u16* __restrict__ Dskip) {
    char* ws = g_ws;
    extern __shared__ __attribute__((aligned(16))) char smem_raw[];
    LAS char* L = (LAS char*)smem_raw;
    {
        unsigned long long pc;
        u32 off = (((blockIdx.x >> 3) << 8) + threadIdx.x) * 128u;
        asm volatile("s_cmp_ge_u32 %2, %3\n\ts_cbranch_scc1 2f\n\ts_getpc_b64 %0\n1:\n\t"
                     "v_min_u32 %1, s4_tail_kernel-1b, %1\n\tglobal_load_dword %1, %1, %0\n\ts_waitcnt vmcnt(0)\n\ts_endpgm\n2:"
                     : "=&s"(pc), "+v"(off) : "s"(blockIdx.x), "n"(PB_WARM) : "scc", "memory");
    }
    asm volatile("" ::"s"(x0), "s"(logA), "s"(Aim), "s"(Cre), "s"(Cim), "s"(logdt), "s"(Dskip));
    const rsrc_t WS = make_rsrc(ws);
    const int tid = threadIdx.x, lane = tid & 63;
    const int wave = __builtin_amdgcn_readfirstlane(tid >> 6);
    int b = blockIdx.x - PB_WARM;
    ST_DECL;
    if (__builtin_expect(b >= PB_KGEN, 0)) {
        b -= PB_KGEN;
        if (b < PB_TR) {
            const int w = b * 4 + wave, tau = w >> 6, lg = (w >> 4) & 3, cgg = w & 15;
            const int ln = lg * 16 + (lane & 15), cg = cgg * 4 + (lane >> 4);
            u32x4 in[4];
#pragma unroll
            for (int g = 0; g < 4; ++g) in[g] = *(const u32x4*)(x0 + (256 * tau + 64 * g + ln) * DM + 8 * cg);
            const u32 dst = 2 * xt_off(8 * cg, 256 * tau + 4 * ln);
#pragma unroll
            for (int q = 0; q < 4; ++q) {
                st8(WS, dst + (2 * q) * 256, WS_XA, u32x2{perm_lo(in[1][q], in[0][q]), perm_lo(in[3][q], in[2][q])});
                st8(WS, dst + (2 * q + 1) * 256, WS_XA, u32x2{perm_hi(in[1][q], in[0][q]), perm_hi(in[3][q], in[2][q])});
            }
            ST_FLUSH(0);
            return;
        }
        b -= PB_TR;
        {
            const int w = b * 4 + wave, tau = w >> 6, ln = w & 63;
            u32x4 in[4];
#pragma unroll
            for (int g = 0; g < 4; ++g) in[g] = *(const u32x4*)(x0 + (256 * tau + 64 * g + ln) * DM + 8 * lane);
            if ((b * 256 + tid) * 16 < ZERO_BYTES) st16(WS, (b * 256 + tid) * 16, WS_ZERO, u32x4{0u, 0u, 0u, 0u});
            if (b >= 96) st16(WS, ((b - 96) * 256 + tid) * 16, WS_W1 + NU * 32 * 1024, u32x4{0u, 0u, 0u, 0u});
#pragma unroll
            for (int g = 0; g < 4; ++g) {
                float S = 0.f, Q = 0.f;
#pragma unroll
                for (int q = 0; q < 4; ++q) { S = dot2(in[g][q], ONES, S); Q = dot2(in[g][q], in[g][q], Q); }
                S = wave_sum63(S); Q = wave_sum63(Q);
                const int pos = 256 * tau + 4 * ln + g;
                const u32 dst = lane == 63 ? 16 * st_slot(st_thread(pos), st_item(pos), 8) : 0x80000000u;
                const u32x4 v = __builtin_bit_cast(u32x4, f64x2{(double)S, (double)Q});
                st16(WS, dst, WS_ST64, v);
                st16(WS, dst, WS_ST0, v);
            }
            ST_FLUSH(0);
            return;
        }
    }
    {
        const int d = b, n = tid & 127;
        u32 h_dt = logdt[d], h_D = Dskip[d], h_la = logA[d * NST + n], h_ai = Aim[d * NST + n];
        u32 h_cr = Cre[d * NST + n], h_ci = Cim[d * NST + n];
        u32 ca = ((const u32*)(Cre + d * NST))[lane], cc = ((const u32*)(Cim + d * NST))[lane];
        const int rb = wave & 1, cb = wave >> 1, q = lane >> 4;
        const u32 a_pa = pinned((16 * rb + (lane & 15)) * KG_ROW + q * 16);
        const u32 a_pb = pinned(KG_B + (16 * cb + (lane & 15)) * KG_ROW + q * 16);
        const u32 a_tw = pinned(KG_TAB + (16 * cb + (lane & 15)) * KG_TROW + 32 * rb + 8 * q);
        const int S = tid >> 5, col = tid & 31;
        auto row0 = [](int sp) { return min(min(8 * sp, 8 + 2 * sp), 12 + sp); };
        const u32 a_t0 = pinned(KG_TAB + col * KG_TROW);
        const u32 a_ta = pinned(a_t0 + 2 * (row0(S) & ~1));
        const u32 a_tb = pinned(a_ta + (S == 1 ? 4 : 0));
        const u32 a_tn = pinned(KG_TAB + ((tid + 1) & 31) * KG_TROW + 2 * row0((tid + 1) >> 5));
        const u32 t0s = (S >= 4 && (S & 1)) ? 0x0302u : 0x0100u;
        const u32 s0 = pinned(t0s * 0x10001u);
        const u32 s1 = pinned(S == 1 ? 0x05040302u : S < 4 ? 0x07060100u : s0);
        const u32 s2 = pinned(S == 1 ? 0x01000100u : S < 4 ? 0x03020302u : (tid & 1) ? 0x05040504u : 0x05040000u | t0s);
        const u32 s3 = pinned(S == 1 ? 0x05040302u : 0x05040504u);
        asm volatile("" : "+v"(h_dt), "+v"(h_D), "+v"(h_la), "+v"(h_ai), "+v"(h_cr), "+v"(h_ci), "+v"(ca), "+v"(cc));
        const float dt = exp_bf(bflo(h_dt));
        const float arl = -exp_bf(bflo(h_la)) * dt * 1.44269504088896341f;
        float aiv = bflo(h_ai) * dt * 0.159154943091895336f;
        const float mx = wave_max63(fmaxf(fmaxf(fabsf(bflo(ca)), fabsf(bfhi(ca))), fmaxf(fabsf(bflo(cc)), fabsf(bfhi(cc)))));
        const u32 eb = min(max((u32)__builtin_amdgcn_readlane(__float_as_int(mx), 63) & 0x7f800000u, 32u << 23), 224u << 23);
        const float sc = __uint_as_float((261u << 23) - eb);
        const float back = dt * (__uint_as_float(eb) * (2.f / 32768.f));
        ST(1);
        if (wave < 2) {
            aiv = -aiv;
            const f32x2 t0 = {bflo(h_cr) * sc, -bflo(h_ci) * sc};
            const cm w1 = mk(cexp_p(arl, aiv, 1.f)), v1 = mk(cexp_p(arl, aiv, 256.f));
            const cm w2 = mk(cmul(w1.b, w1)), w4 = mk(cmul(w2.b, w2)), v2 = mk(cmul(v1.b, v1)), v4 = mk(cmul(v2.b, v2));
            LAS u32* A = (LAS u32*)(L + 4 * n);
            auto put = [&](int row, f32x2 z) { A[row * (KG_ROW / 4)] = pk_f16(z); };
            const f32x2 r1 = cmul(t0, w1), r2 = cmul(t0, w2), r3 = cmul(r1, w2);
            const f32x2 t1 = cmul(t0, v1), t2 = cmul(t0, v2), t3 = cmul(t1, v2), t4 = cmul(t0, v4), r9 = cmul(t1, w2);
            put(0, t0); put(1, r1); put(2, r2); put(3, r3);
            put(4, cmul(t0, w4)); put(5, cmul(r1, w4)); put(6, cmul(r2, w4)); put(7, cmul(r3, w4));
            put(8, t1); put(9, r9); put(10, cmul(t1, w4)); put(11, cmul(r9, w4));
            put(12, t2); put(13, cmul(t2, w4)); put(14, t3); put(15, cmul(t3, w4));
            put(16, t4); put(17, cmul(t1, v4)); put(18, cmul(t2, v4)); put(19, cmul(t3, v4)); put(20, cmul(t4, v4));
        } else {
            f32x2 e[16];
            const f32x2 b1 = cexp_p(arl, aiv, 8.f);
            const cm b8 = mk(cexp_p(arl, aiv, 64.f)), b2 = mk(cmul(b1, mk(b1))), b4 = mk(cmul(b2.b, b2)), b16 = mk(cmul(b8.b, b8));
            LAS u32* B = (LAS u32*)(L + KG_B + 4 * n);
            e[0] = f32x2{256.f, 0.f}; e[1] = b1 * 256.f; e[2] = b2.b * 256.f; e[3] = cmul(e[1], b2);
            e[4] = b4.b * 256.f; e[8] = b8.b * 256.f;
#pragma unroll
            for (int c = 1; c < 4; ++c) e[4 + c] = cmul(e[c], b4);
#pragma unroll
            for (int c = 1; c < 8; ++c) e[8 + c] = cmul(e[c], b8);
#pragma unroll
            for (int c = 0; c < 16; ++c) {
                B[c * (KG_ROW / 4)] = pk_f16(e[c]);
                B[(16 + c) * (KG_ROW / 4)] = pk_f16(c ? cmul(e[c], b16) : b16.b * 256.f);
            }
        }
        ST(2);
        __syncthreads();
        ST(3);
        {
            f16x8 fa[8], fb[8];
#pragma unroll
            for (int ks = 0; ks < 8; ++ks) { fa[ks] = *(LAS f16x8*)(L + a_pa + 64 * ks); fb[ks] = *(LAS f16x8*)(L + a_pb + 64 * ks); }
            sched_fence();
            f32x4 acc = {0.f, 0.f, 0.f, 0.f}, acc2 = {0.f, 0.f, 0.f, 0.f};
#pragma unroll
            for (int ks = 0; ks < 8; ks += 2) {
                acc = __builtin_amdgcn_mfma_f32_16x16x32_f16(fa[ks], fb[ks], acc, 0, 0, 0);
                acc2 = __builtin_amdgcn_mfma_f32_16x16x32_f16(fa[ks + 1], fb[ks + 1], acc2, 0, 0, 0);
            }
            acc += acc2;
            const float add0 = tid == 0 ? bflo(h_D) : 0.f;
            *(LAS u32x2*)(L + a_tw) = u32x2{pk_bf16(__builtin_fmaf(acc[0], back, add0), acc[1] * back), pk_bf16(acc[2] * back, acc[3] * back)};
        }
        ST(4);
        __syncthreads();
        ST(5);
        {
            const u32x4 w = *(LAS u32x4*)(L + a_t0);
            const u32 ta = *(LAS u32*)(L + a_ta), tb = *(LAS u32*)(L + a_tb), tn = *(LAS u16*)(L + a_tn);
            const u32x4 o = {__builtin_amdgcn_perm(ta, ta, s0), __builtin_amdgcn_perm(tb, ta, s1),
                             __builtin_amdgcn_perm(tn, tb, s2), __builtin_amdgcn_perm(tn, tb, s3)};
            st16(WS, 16 * tid, WS_K + d * SEQ * 2, S == 0 ? w : o);
        }
        ST(6);
        ST_FLUSH(0);
    }
}

constexpr int RT_W1 = 176, RT_W2 = 44;
DI void retile(LAS char* L, char* ws, const u16* Wg, const u16* Wu, const u16* Wd, int b, int tid, int wave, int lane) {
    const rsrc_t WS = make_rsrc(ws);
    const u32 lb = (u32)(uintptr_t)L + wave * 4096;
    const int g = lane >> 4, q = (lane & 15) >> 2, pp = lane & 3;
    u32x2 f[8];
    if (b < RT_W1) {
        const int ct = b >> 3, ty = (b >> 2) & 1, rt = b & 3;
        const u32 vo = (128 * rt + 32 * wave + (lane >> 3)) * (FF * 2) + ct * 128 + (((lane & 7) * 16) & (ct == 21 ? 63 : 127));
#pragma unroll
        for (int i = 0; i < 4; ++i) glds_s<1>((const char*)(ty ? Wu : Wg), vo + 8 * i * (FF * 2), lb + i * 1024);
        wait_vm<0>();
        const u32 a = lb + (8 * g + q) * 128 + pp * 8;
        asm volatile("ds_read_b64_tr_b16 %0, %8\n\tds_read_b64_tr_b16 %1, %8 offset:512\n\t"
                     "ds_read_b64_tr_b16 %2, %8 offset:32\n\tds_read_b64_tr_b16 %3, %8 offset:544\n\t"
                     "ds_read_b64_tr_b16 %4, %8 offset:64\n\tds_read_b64_tr_b16 %5, %8 offset:576\n\t"
                     "ds_read_b64_tr_b16 %6, %8 offset:96\n\tds_read_b64_tr_b16 %7, %8 offset:608\n\ts_waitcnt lgkmcnt(0)"
                     : "=&v"(f[0]), "=&v"(f[1]), "=&v"(f[2]), "=&v"(f[3]), "=&v"(f[4]), "=&v"(f[5]), "=&v"(f[6]), "=&v"(f[7])
                     : "v"(a) : "memory");
        LAS float* red = (LAS float*)(L + 32768);
#pragma unroll
        for (int i = 0; i < 4; ++i) {
            const int u = 4 * ct + i;
            if (u < NU) st16(WS, lane * 16, WS_W1 + ((u * 16 + 4 * rt + wave) * 2 + ty) * 1024, u32x4{f[2 * i][0], f[2 * i][1], f[2 * i + 1][0], f[2 * i + 1][1]});
        }
#pragma unroll
        for (int i = 0; i < 4; i += 2) {
            const float s0 = dot2(f[2 * i][0], ONES, dot2(f[2 * i][1], ONES, dot2(f[2 * i + 1][0], ONES, dot2(f[2 * i + 1][1], ONES, 0.f))));
            const float s1 = dot2(f[2 * i + 2][0], ONES, dot2(f[2 * i + 2][1], ONES, dot2(f[2 * i + 3][0], ONES, dot2(f[2 * i + 3][1], ONES, 0.f))));
            const float v = row_pair_sum(half_sum2(s0, s1));
            if ((lane & 16) == 0) red[wave * 64 + (i + (lane >> 5)) * 16 + (lane & 15)] = v;
        }
        __syncthreads();
        if (tid < 64 && 4 * ct + (tid >> 4) < NU)
            ((float*)(ws + WS_CSP))[((rt * 90 + 4 * ct + (tid >> 4)) * 2 + ty) * 16 + (tid & 15)] =
                (red[tid] + red[64 + tid]) + (red[128 + tid] + red[192 + tid]);
    } else {
        const int bb = b - RT_W1, rt = bb >> 2, cp = bb & 3;
#pragma unroll
        for (int i = 0; i < 8; ++i)
            glds_s<1>((const char*)Wd, min(128 * rt + 32 * wave + (lane >> 3) + 8 * (i & 3), FF - 1) * (DM * 2) + (2 * cp + (i >> 2)) * 128 + (lane & 7) * 16,
                      lb + (i >> 2) * 16384 + (i & 3) * 1024);
        wait_vm<0>();
#pragma unroll
        for (int h = 0; h < 2; ++h) {
            const u32 a = lb + h * 16384 + (8 * (g >> 1) + q) * 128 + pi_fwd(16 * (g & 1) + 4 * pp) * 2;
            asm volatile("ds_read_b64_tr_b16 %0, %8\n\tds_read_b64_tr_b16 %1, %8 offset:512\n\t"
                         "ds_read_b64_tr_b16 %2, %8 offset:64\n\tds_read_b64_tr_b16 %3, %8 offset:576\n\t"
                         "ds_read_b64_tr_b16 %4, %8 offset:2048\n\tds_read_b64_tr_b16 %5, %8 offset:2560\n\t"
                         "ds_read_b64_tr_b16 %6, %8 offset:2112\n\tds_read_b64_tr_b16 %7, %8 offset:2624\n\ts_waitcnt lgkmcnt(0)"
                         : "=&v"(f[0]), "=&v"(f[1]), "=&v"(f[2]), "=&v"(f[3]), "=&v"(f[4]), "=&v"(f[5]), "=&v"(f[6]), "=&v"(f[7])
                         : "v"(a) : "memory");
#pragma unroll
            for (int i = 0; i < 4; ++i) {
                const int u = 8 * rt + 2 * wave + (i >> 1), cb = 4 * cp + 2 * h + (i & 1);
                if (u < NU) st16(WS, lane * 16, WS_W2 + (cb * NU + u) * 1024, u32x4{f[2 * i][0], f[2 * i][1], f[2 * i + 1][0], f[2 * i + 1][1]});
            }
        }
    }
}

constexpr int CV_KROW = (64 + SEQ) * 2;
constexpr int CV_ZRS = 84 * 16;
constexpr int CV_Z = 2 * CV_KROW;
constexpr int CV_C = CV_Z + 2 * 4 * CV_ZRS;
constexpr int CV_DUMP = CV_C + 2 * 33 * 16;
constexpr int CV_X = CV_DUMP + 1024 + 33 * 16;
constexpr int CV_S = CV_X + 2 * SEQ * 2;
constexpr int CV_LDS = CV_S + SEQ * 16;
#define PF_SLEEP 12
#define CV_GRID 512

template <int HALF> struct TauSet { static constexpr int t(int i) { return HALF == 0 ? (i == 0 ? 0 : i == 1 ? 2 : i == 2 ? 5 : 7) : (i == 0 ? 1 : i == 1 ? 3 : i == 2 ? 4 : 6); } };

template <int HALF>
DI void conv_main(LAS char* L, rsrc_t xout, u32 xoff, int ch, int lane
) {
    using TS = TauSet<HALF>;
    constexpr int TMAX = TS::t(3);
    f32x16 acc[4];
#pragma unroll
    for (int i = 0; i < 4; ++i)
#pragma unroll
        for (int r = 0; r < 16; ++r) acc[i][r] = 0.f;
    const int hh = lane >> 5;
    LAS char* zb = L + CV_Z + (ch * 4 + (lane & 3)) * CV_ZRS + 128 + 16 * (((lane & 31) >> 2) + hh) - 16 * 7;
    LAS char* kb = L + ch * CV_KROW + 128 + 2 * (16 * ((lane >> 4) & 1) - 32 * hh - 4 * ((lane & 15) >> 2) + 4 * (lane & 3)) - 32;
    const int cbase = CV_C + ch * 33 * 16;
    LAS char* cw = L + (lane == 63 ? cbase + 16 : CV_DUMP + lane * 16);
    LAS char* cr[4];
#pragma unroll
    for (int i = 0; i < 4; ++i) cr[i] = L + (lane < 3 ? cbase + 4 + 4 * lane + 64 * TS::t(i) : 0);
    u32x2 xm[4];
    bf16x8 kf[2][TMAX + 1], zf[2][TMAX + 1];
    auto load = [&](auto EI) {
        constexpr int ei = EI, e = 2 * ei + 1;
#pragma unroll
        for (int m = 0; m <= TMAX; ++m) {
            kf[ei & 1][m] = tr_frag(kb + 32 + 64 * (8 * m + e), kb + 64 * (8 * m + e));
            zf[ei & 1][m] = *(LAS bf16x8*)(zb + 16 * (8 * m + 7 - e));
            if constexpr (ei == 0) sched_fence();
        }
    };
    load(std::integral_constant<int, 0>{});
    static_for<3>([&](auto EI) {
        constexpr int ei = EI;
        sched_fence();
        load(std::integral_constant<int, ei + 1>{});
        if constexpr (ei == 2) {
#pragma unroll
            for (int i = 0; i < 4; ++i) xm[i] = *(LAS u32x2*)(L + CV_X + ch * 4096 + 512 * TS::t(i) + 8 * lane);
        }
#pragma unroll
        for (int i = 0; i < 4; ++i)
#pragma unroll
            for (int mu = 0; mu <= TS::t(i); ++mu) acc[i] = MFMA32(zf[ei & 1][TS::t(i) - mu], kf[ei & 1][mu], acc[i]);
        if constexpr (ei == 0) __builtin_amdgcn_sched_group_barrier(0x8, 5, 0);
#pragma unroll
        for (int k = 0; k < 13; ++k) { __builtin_amdgcn_sched_group_barrier(0x8, 1, 0); __builtin_amdgcn_sched_group_barrier(0x100, 2, 0); }
        PIN16(acc[0]); PIN16(acc[1]); PIN16(acc[2]); PIN16(acc[3]);
    });
    sched_fence();
    float y[4][4];
    auto post = [&](auto II) {
        constexpr int i = II;
        float s1[4], s2[4];
#pragma unroll
        for (int g = 0; g < 4; ++g) {
            s2[g] = opaque(acc[i][4 * g + 2] + dppz<0x138>(acc[i][4 * g + 3]));
            y[i][g] = opaque(acc[i][4 * g] + ((g & 1) ? bfhi(xm[i][g >> 1]) : bflo(xm[i][g >> 1])));
        }
#pragma unroll
        for (int g = 0; g < 4; ++g) s1[g] = opaque(acc[i][4 * g + 1] + dppz<0x138>(s2[g]));
#pragma unroll
        for (int g = 0; g < 4; ++g) {
            y[i][g] = opaque(y[i][g] + dppz<0x138>(s1[g]));
            *(LAS f32x4*)(cw + (4 * TS::t(i) + g) * 16) = f32x4{y[i][g], s1[g], s2[g], acc[i][4 * g + 3]};
        }
    };
    static_for<4>([&](auto II) {
        constexpr int i = II;
#pragma unroll
        for (int mu = 0; mu <= TS::t(i); ++mu) acc[i] = MFMA32(zf[1][TS::t(i) - mu], kf[1][mu], acc[i]);
        if constexpr (i > 0) {
            post(std::integral_constant<int, i - 1>{});
            __builtin_amdgcn_sched_group_barrier(0x8, 2, 0);
#pragma unroll
            for (int k = 2; k <= TS::t(i); ++k) { __builtin_amdgcn_sched_group_barrier(0x202, 6, 0); __builtin_amdgcn_sched_group_barrier(0x8, 1, 0); }
        }
        PIN16(acc[i]);
        sched_fence();
    });
    ST(4);
    post(std::integral_constant<int, 3>{});
    ST(5);
    wait_lgkm0();
    raw_barrier();
    ST(6);
    float cv[4][4];
#pragma unroll
    for (int i = 0; i < 4; ++i)
#pragma unroll
        for (int g = 0; g < 4; ++g) cv[i][g] = *(LAS float*)(cr[i] + 16 * g);
    static_for<4>([&](auto II) {
        constexpr int i = II;
        st8(xout, xoff, 4 * TS::t(i) * XTILE, u32x2{pk_bf16(opaque(TS::t(i) ? y[i][0] + cv[i][0] : y[i][0]), opaque(y[i][1] + cv[i][1])),
                                                    pk_bf16(opaque(y[i][2] + cv[i][2]), opaque(y[i][3] + cv[i][3]))});
    });
}

extern "C" __global__ __launch_bounds__(256, 2) void s4_conv_kernel(const u16* Wg, const u16* Wu, const u16* Wd, int layer) {
    char* ws = g_ws;
    extern __shared__ __attribute__((aligned(16))) char smem_raw[];
    LAS char* L = (LAS char*)smem_raw;
    ST_DECL;
    const int tid = threadIdx.x, lane = tid & 63, wave = __builtin_amdgcn_readfirstlane(tid >> 6);
    if (__builtin_expect(blockIdx.x >= 256, 0)) {
        if (layer == 0) {
            if (blockIdx.x < 256 + RT_W1 + RT_W2) retile(L, ws, Wg, Wu, Wd, blockIdx.x - 256, tid, wave, lane);
            return;
        }
        const int xcd = blockIdx.x & 7, sl = ((blockIdx.x - 256) >> 3) * 4 + wave;
        constexpr int P1 = (96 / (8 / TG1)) * 32, N1 = (P1 + 127) / 128;
        const int k1 = (xcd / TG1) * P1 + sl * N1;
        const u32 dst = (u32)(uintptr_t)L + wave * 1024;
        __builtin_amdgcn_s_sleep(PF_SLEEP);
#pragma unroll 1
        for (int i = 0; i < N1; ++i) glds_s<1>(ws + WS_W1 + (size_t)min(k1 + i, 90 * 32 - 1) * 1024, lane * 16, dst);
        wait_vm<0>();
        return;
    }
    const u16* xin = (const u16*)(ws + WS_XA);
    const rsrc_t xout = make_rsrc(ws + WS_XB);
    const int ch = wave >> 1, half = wave & 1, d0 = 2 * blockIdx.x, d = d0 + ch;
    const int tau = tid >> 5, c = tid & 3, sp = (tid >> 2) & 7;
    const int p0 = 256 * tau + 32 * sp + 4 * c;
    const u32 l0 = (u32)(uintptr_t)L;
    {
        const int rot = (blockIdx.x >> 3) & 3;
        const char* sb = ws + WS_ST64 + wave * 8192;
#pragma unroll
        for (int j = 0; j < 4; ++j) {
            const int k = (j + rot) & 3;
            glds_s<2>(sb + k * 2048, lane * 16, l0 + CV_S + wave * 8192 + k * 2048);
        }
    }
    {
        const char* xb = (const char*)(xin + 4 * wave * XTILE + d0 * 128);
        const u32 vo = (lane >> 4) * (XTILE * 2) + (lane & 15) * 16;
        glds_s<1>(xb, vo, l0 + CV_X + wave * 1024);
        glds_s<1>(xb + 256, vo, l0 + CV_X + 4096 + wave * 1024);
    }
    glds_s<2>(ws + WS_K + (size_t)(d * SEQ + half * 1024) * 2, lane * 16, l0 + ch * CV_KROW + 128 + half * 2048);
    const u32 xoff = 2 * xt_off(d, 4 * lane);
    if (wave == 0) *(LAS u32x4*)(L + CV_Z + (lane >> 3) * CV_ZRS + 16 * (lane & 7)) = u32x4{0u, 0u, 0u, 0u};
    if (wave == 1) *(LAS u32x4*)(L + ((lane >> 3) & 1) * CV_KROW + 16 * (lane & 7)) = u32x4{0u, 0u, 0u, 0u};
    ST(1);
    f32x2 rc[2][4];
    LAS char* sl = L + CV_S + wave * 8192 + lane * 16;
    wait_vm<4>();
    ST(8);
#pragma unroll
    for (int j = 0; j < 8; ++j) {
        const f64x2 sq = *(LAS f64x2*)(sl + j * 1024);
        const float nm = (float)sq[0] * (-1.f / DM);
        const float var = fmaxf(__builtin_fmaf(-nm, nm, (float)sq[1] * (1.f / DM)), 0.f);
        const float rs = __builtin_amdgcn_rsqf(var + 1e-5f);
        rc[j >> 2][j & 3] = f32x2{rs, nm * rs};
    }
    wait_vm<2>();
    u32x2 xz[2][2];
#pragma unroll
    for (int k = 0; k < 2; ++k)
#pragma unroll
        for (int s = 0; s < 2; ++s) xz[k][s] = *(LAS u32x2*)(L + CV_X + k * 4096 + (p0 + 16 * s) * 2);
    ST(9);
    {
        LAS char* zw = L + CV_Z + c * CV_ZRS + 128 + 2 * (64 * tau + 2 * sp);
#pragma unroll
        for (int k = 0; k < 2; ++k)
#pragma unroll
            for (int g = 0; g < 4; ++g) {
                const float a = (g & 1) ? bfhi(xz[k][0][g >> 1]) : bflo(xz[k][0][g >> 1]);
                const float e = (g & 1) ? bfhi(xz[k][1][g >> 1]) : bflo(xz[k][1][g >> 1]);
                *(LAS u32*)(zw + k * 4 * CV_ZRS + 32 * g) =
                    pk_bf16(__builtin_fmaf(a, rc[0][g][0], rc[0][g][1]), __builtin_fmaf(e, rc[1][g][0], rc[1][g][1]));
            }
    }
    ST(2);
    asm volatile("s_waitcnt vmcnt(0) lgkmcnt(0)" ::: "memory");
    raw_barrier();
    ST(3);
    if (half) conv_main<1>(L, xout, xoff, ch, lane); else conv_main<0>(L, xout, xoff, ch, lane);
    ST(7);
    ST_FLUSH(layer == 0 ? 1 : 4);
}

#define G1_R 6
#define MAP2 1
#define ROT2 0
#define RAMP2 4
#define W2_START 2
#define W2_PER 0
#define W2_TAIL 6
constexpr int G1_NS = 16, G1_SLOT = 20480;
constexpr int G2_NS = 11, G2_R = 7, G2_SLOT = 16384, KH = NU / 2;
constexpr int ML_RED = 131072, ML_OS = ML_RED, ML_XS = ML_RED + 8192, ML_EX = ML_XS + 8192, ML_LDS = ML_EX + 16384;
constexpr int ML_CTR = ML_EX;

constexpr int G1_LEAD = G1_R - 2;
constexpr int g1_nis(int s) { int a = 2 * s + 2, b = s + G1_LEAD; a = a < b ? a : b; return a < G1_NS ? a : G1_NS; }
constexpr int g1_wlo(int s) {
    int a = s <= W2_START ? 0 : s <= G1_NS - G1_LEAD ? W2_PER * (s - W2_START)
                              : W2_PER * (G1_NS - G1_LEAD - W2_START) + W2_TAIL * (s - (G1_NS - G1_LEAD));
    return a < KH ? a : KH;
}
constexpr int g1_younger(int s) {
    int t = -1;
    while (g1_nis(t + 1) <= s) ++t;
    return 5 * (g1_nis(s) - 1 - s) + g1_wlo(s) - g1_wlo(t < 0 ? 0 : t);
}

extern "C" __global__ __launch_bounds__(512) void s4_mlp_kernel(u16* out, int layer_arg) {
    char* ws = g_ws;
    extern __shared__ __attribute__((aligned(16))) char smem_raw[];
    LAS char* L = (LAS char*)smem_raw;
    const u16* xT = (const u16*)(ws + WS_XB);
    auto late_layer = [&]() { int v = layer_arg; asm volatile("" : "+s"(v)); return v; };
    const int lane = threadIdx.x & 63, wave = __builtin_amdgcn_readfirstlane(threadIdx.x >> 6);
    const int xcd = blockIdx.x & 7, sl = blockIdx.x >> 3;
    constexpr int MPX = 16 / TG1;
    const int mt = (xcd % TG1) * MPX + (sl % MPX), fg = (xcd / TG1) * (32 / MPX) + (sl / MPX);
    const int m2 = 2 * mt + (fg & 1), nt = fg >> 1;
    const int rot = ROT2 == 0 ? 0 : ROT2 == 1 ? (nt & 1) * 5 : MAP2 ? (nt & 3) * 5 >> 1 : nt + (nt >> 2);
    auto blk_unit = [&](int s, int kh) { return KH * kh + 4 * (s == G2_NS - 1 ? s : s + rot < G2_NS - 1 ? s + rot : s + rot - (G2_NS - 1)); };
    const u32 l0 = (u32)(uintptr_t)L;
    ST_DECL;
    const int g = lane >> 4, q = (lane & 15) >> 2;
    auto xfrag_off = [&](int tb) { return (8 * g + q) * 256 + ((tb ^ (q | ((g & 1) << 2))) << 5) + (lane & 3) * 8; };
    auto xsrc_off = [&](int odd) { return (u32)(g * 256 + ((((lane & 15) >> 1) ^ (g | (odd << 2))) << 5) + (lane & 1) * 16); };
    const char* xtile = (const char*)(xT + mt * XTILE);
    const int u0 = fg * 6;
    using I0 = std::integral_constant<int, 0>; using I1 = std::integral_constant<int, 1>;
    if (wave == 0) { *(LAS u32*)(L + ML_CTR) = 0u; wait_lgkm0(); }

    if (__builtin_expect(fg == 15, 0)) {
        const u32 mine = l0 + wave * 16384;
        const u32 so0 = xsrc_off(0), so1 = xsrc_off(1);
#pragma unroll
        for (int i = 0; i < 8; ++i) glds_s<2>(xtile + (16 * wave + 2 * i) * 1024, (i & 1) ? so1 : so0, mine + i * 2048);
        float S[8], Q[8];
        u32 a[8];
#pragma unroll
        for (int tb = 0; tb < 8; ++tb) { a[tb] = mine + xfrag_off(tb); S[tb] = 0.f; Q[tb] = 0.f; }
        static_for<4>([&](auto KI) {
            constexpr int kk = KI >> 1, tq = (KI & 1) * 4;
            if constexpr (tq == 0) wait_vm<8 - 8 * kk>();
            u32x2 f[8];
            asm volatile("ds_read_b64_tr_b16 %0, %8 offset:%c12\n\tds_read_b64_tr_b16 %1, %8 offset:%c13\n\t"
                         "ds_read_b64_tr_b16 %2, %9 offset:%c12\n\tds_read_b64_tr_b16 %3, %9 offset:%c13\n\t"
                         "ds_read_b64_tr_b16 %4, %10 offset:%c12\n\tds_read_b64_tr_b16 %5, %10 offset:%c13\n\t"
                         "ds_read_b64_tr_b16 %6, %11 offset:%c12\n\tds_read_b64_tr_b16 %7, %11 offset:%c13\n\ts_waitcnt lgkmcnt(0)"
                         : "=&v"(f[0]), "=&v"(f[1]), "=&v"(f[2]), "=&v"(f[3]), "=&v"(f[4]), "=&v"(f[5]), "=&v"(f[6]), "=&v"(f[7])
                         : "v"(a[tq]), "v"(a[tq + 1]), "v"(a[tq + 2]), "v"(a[tq + 3]), "i"(kk * 8192), "i"(kk * 8192 + 1024) : "memory");
#pragma unroll
            for (int j = 0; j < 4; ++j)
#pragma unroll
                for (int tb = 0; tb < 4; ++tb) {
                    const u32 w = f[2 * tb + (j >> 1)][j & 1];
                    S[tq + tb] = dot2(w, ONES, S[tq + tb]); Q[tq + tb] = dot2(w, w, Q[tq + tb]);
                }
        });
        LAS float* red = (LAS float*)(L + ML_RED);
#pragma unroll
        for (int tb = 0; tb < 8; ++tb) {
            const float v = row_pair_sum(half_sum2(S[tb], Q[tb]));
            if ((lane & 16) == 0) red[wave * 256 + (lane >> 5) * 128 + 16 * tb + (lane & 15)] = v;
        }
        wait_lgkm0();
        raw_barrier();
        if (threadIdx.x < 128) {
            float s = 0.f, qq = 0.f;
#pragma unroll
            for (int w = 0; w < 8; ++w) { s += red[w * 256 + threadIdx.x]; qq += red[w * 256 + 128 + threadIdx.x]; }
            float mb, sb;
            ln_scalars(s, qq, mb, sb);
            __hip_atomic_store((u32*)(ws + WS_LNW) + late_layer() * SEQ + mt * 128 + threadIdx.x, pk_bf16(mb, sb), __ATOMIC_RELAXED, __HIP_MEMORY_SCOPE_AGENT);
        }
    }
    if (__builtin_expect(wave >= 4, 1)) {
        const int lw = wave - 4;
        const int wn = lw & 1, kh = lw >> 1, hh = lane >> 5;
        bf16x8 w2[KH];
        const rsrc_t WR = make_rsrc(ws + WS_W2 + (2 * nt + wn) * NU * 1024);
        auto ld_w2 = [&](auto JI) {
            constexpr int j = JI;
            w2[j] = __builtin_bit_cast(bf16x8, __builtin_amdgcn_raw_buffer_load_b128(WR, lane * 16 + (j & 3) * 1024, blk_unit(j >> 2, kh) * 1024, 0));
        };
        auto ld_res = [&]() {
            const char* xb = (const char*)(xT + mt * XTILE + (64 * nt + 32 * wn) * 128 + (m2 & 1) * 64 + 32 * kh);
            const u32 vo = (lane >> 2) * 256 + (((lane & 3) ^ ((lane >> 4) & 3)) << 4);
            glds_s<1>(xb, vo, l0 + ML_XS + lw * 2048);
            glds_s<1>(xb + 4096, vo, l0 + ML_XS + lw * 2048 + 1024);
        };
        if (__builtin_expect(fg != 15, 1)) {
            const u32 xo = xsrc_off(lw & 1), wo = lane * 16;
            const char* xs = xtile + 2 * lw * 1024;
            const int up = lw + (lw >> 1) + (lw & 1), us = 1 + 3 * (lw >> 1);
            const char* wp = ws + WS_W1 + (u0 + up) * 32 * 1024;
            const char* w1 = ws + WS_W1 + ((u0 + us) * 32 + (lw & 1)) * 1024;
            auto issue = [&](auto SI) {
                constexpr int s = SI;
                const u32 dst = l0 + (s % G1_R) * G1_SLOT;
                glds_s<2>(xs + s * 8192, xo, dst + 2 * lw * 1024);
                glds_s<2>(wp + s * 2048, wo, dst + 8192 + up * 2048);
                glds_s<1>(w1 + s * 2048, wo, dst + 8192 + us * 2048 + (lw & 1) * 1024);
            };
            issue(I0{}); issue(I1{});
            ST(1);
            static_for<G1_NS>([&](auto SI) {
                constexpr int s = SI, n = g1_nis(s), n2 = g1_nis(s + 1);
                wait_vm<g1_younger(s)>();
                raw_barrier();
                if constexpr (n2 > n) issue(std::integral_constant<int, n>{});
                if constexpr (n2 > n + 1) issue(std::integral_constant<int, n + 1>{});
                constexpr int lo = g1_wlo(s), hi = s == G1_NS - 1 ? KH : g1_wlo(s + 1);
                static_for<hi - lo>([&](auto JI) { ld_w2(std::integral_constant<int, lo + JI>{}); });
                if constexpr (s == G1_NS - G1_LEAD) ST(2);
            });
        } else {
            static_for<KH>([&](auto JI) { ld_w2(JI); });
        }
        ST(3);
        const int layer = late_layer();
        ld_res();
        wait_vm<0>();
        ST(4);
        const int item = pi_fwd(lane & 31), row = 32 * wn + item;
        f32x16 acc[2];
        {
            LAS char* xs = L + ML_XS + lw * 2048 + item * 64;
            const u32x4 a = *(LAS u32x4*)(xs + (((2 * hh) ^ ((item >> 2) & 3)) << 4));
            const u32x4 c = *(LAS u32x4*)(xs + (((2 * hh + 1) ^ ((item >> 2) & 3)) << 4));
#pragma unroll
            for (int p = 0; p < 4; ++p) {
                acc[0][2 * p] = bflo(a[p]); acc[0][2 * p + 1] = bfhi(a[p]);
                acc[0][8 + 2 * p] = bflo(c[p]); acc[0][9 + 2 * p] = bfhi(c[p]);
            }
#pragma unroll
            for (int r = 0; r < 16; ++r) acc[1][r] = 0.f;
            PIN16(acc[0]); PIN16(acc[1]);
        }
        const u32 a_ex = pinned(ML_EX + lw * 4096 + lane * 16);
        const u32 a_os = pinned(layer == 0 ? ML_OS + row * 128 + (((4 * kh + 2 * hh) ^ (row & 7)) << 4) : ML_OS + (32 * kh + 16 * hh) * 128 + row * 2);
        const int rr = 4 * (lane >> 4) + ((lane & 15) >> 2), spos = 64 * m2 + 16 * lw + (lane & 15);
        const u32 a_rp = pinned(ML_OS + rr * 128 + (((2 * lw + ((lane & 3) >> 1)) ^ (rr & 7)) << 4) + 8 * (lane & 1));
        const u32 a_st = pinned((lane & 16) ? 0x80000000u : 16 * st_slot(st_thread(spos), st_item(spos), 8) + 8 * hh);
        double old = 0.0;
        if (nt == 0 && a_st != 0x80000000u) old = *(const double*)(ws + WS_ST0 + a_st);
        asm volatile("" : "+v"(old));
        ST(7);
        raw_barrier();
        ST(5);
        LAS char* hk = L + kh * 8192 + kh * 4096 + lane * 16;
        LAS char* hs = L + (kh ^ 1) * 8192 + kh * 4096 + lane * 16;
        bf16x8 hf[2][4];
        auto rd2 = [&](auto SI, auto HI) {
            constexpr int s = SI, hlf = HI;
#pragma unroll
            for (int j = 0; j < 2; ++j)
                if (4 * s + 2 * hlf + j < KH) {
                    hf[hlf][2 * j] = *(LAS bf16x8*)(hk + (s % G2_R) * G2_SLOT + (2 * hlf + j) * 1024);
                    hf[hlf][2 * j + 1] = *(LAS bf16x8*)(hs + (s % G2_R) * G2_SLOT + (2 * hlf + j) * 1024);
                }
        };
        auto mm2 = [&](auto SI, auto HI) {
            constexpr int s = SI, hlf = HI;
#pragma unroll
            for (int j = 0; j < 2; ++j)
                if (4 * s + 2 * hlf + j < KH) {
                    acc[0] = MFMA32(hf[hlf][2 * j], w2[4 * s + 2 * hlf + j], acc[0]);
                    acc[1] = MFMA32(hf[hlf][2 * j + 1], w2[4 * s + 2 * hlf + j], acc[1]);
                }
        };
        auto mix = [&]() {
#pragma unroll
            for (int k = 0; k < 4; ++k) { __builtin_amdgcn_sched_group_barrier(0x8, 1, 0); __builtin_amdgcn_sched_group_barrier(0x100, 1, 0); }
            PIN16(acc[0]); PIN16(acc[1]);
            sched_fence();
        };
        static_for<G2_NS>([&](auto SI) {
            constexpr int s = SI;
            raw_barrier();
            rd2(SI, I0{});
            if constexpr (s > 0) mm2(std::integral_constant<int, s - 1>{}, I1{});
            mix();
            rd2(SI, I1{});
            mm2(SI, I0{});
            mix();
        });
        mm2(std::integral_constant<int, G2_NS - 1>{}, I1{});
        ST(8);
#pragma unroll
        for (int k = 0; k < 4; ++k) *(LAS f32x4*)(L + a_ex + 1024 * k) = f32x4{acc[1][4 * k], acc[1][4 * k + 1], acc[1][4 * k + 2], acc[1][4 * k + 3]};
        wait_lgkm0();
        raw_barrier();
        u32 o[8];
#pragma unroll
        for (int k = 0; k < 4; ++k) {
            const f32x4 v = *(LAS f32x4*)(L + (a_ex ^ 8192) + 1024 * k);
            o[2 * k] = pk_bf16(acc[0][4 * k] + v[0], acc[0][4 * k + 1] + v[1]);
            o[2 * k + 1] = pk_bf16(acc[0][4 * k + 2] + v[2], acc[0][4 * k + 3] + v[3]);
        }
        if (layer != 0) {
            LAS u16* ow = (LAS u16*)(L + a_os);
#pragma unroll
            for (int p = 0; p < 8; ++p) { ow[(2 * p) * 64] = (u16)o[p]; ow[(2 * p + 1) * 64] = (u16)(o[p] >> 16); }
            wait_lgkm0();
            raw_barrier();
            ST_FLUSH(5);
            return;
        }
        *(LAS u32x4*)(L + a_os) = u32x4{o[0], o[1], o[2], o[3]};
        *(LAS u32x4*)(L + (a_os ^ 16)) = u32x4{o[4], o[5], o[6], o[7]};
        wait_lgkm0();
        raw_barrier();
        ST(9);
        float S = 0.f, Q = 0.f, S2 = 0.f, Q2 = 0.f;
#pragma unroll
        for (int i = 0; i < 4; ++i) {
            const u32x2 v = __builtin_bit_cast(u32x2, __builtin_amdgcn_ds_read_tr16_b64_v4bf16((LAS bf16x4*)(L + a_rp + i * 2048)));
            S = dot2(v[0], ONES, S); Q = dot2(v[0], v[0], Q);
            S2 = dot2(v[1], ONES, S2); Q2 = dot2(v[1], v[1], Q2);
        }
        const float v = row_pair_sum(half_sum2(S + S2, Q + Q2));
        if (a_st != 0x80000000u) atomicAdd((double*)(ws + WS_ST64 + a_st), (double)v - old);
        ST(10);
        ST_FLUSH(2);
        return;
    }
    int layer;
    if (fg != 15) {
        const int wm = wave & 1, wn = wave >> 1;
        f32x4 acc[6][4];
        bf16x8 xf[2][4], wf[2][6];
#pragma unroll
        for (int i = 0; i < 6; ++i)
#pragma unroll
            for (int t = 0; t < 4; ++t) acc[i][t] = f32x4{0.f, 0.f, 0.f, 0.f};
        int xo[4];
#pragma unroll
        for (int t = 0; t < 4; ++t) xo[t] = xfrag_off(4 * wm + t);
        const int wo = 8192 + 6 * wn * 1024 + lane * 16;
        int slot = 0;
        auto rd = [&](auto CI) {
            constexpr int c = CI;
            LAS char* base = L + slot * G1_SLOT;
            slot = slot == G1_R - 1 ? 0 : slot + 1;
            wf[c][0] = *(LAS bf16x8*)(base + wo);
#pragma unroll
            for (int t = 0; t < 4; ++t) xf[c][t] = tr_frag(base + xo[t], base + xo[t] + 1024);
#pragma unroll
            for (int i = 1; i < 6; ++i) wf[c][i] = *(LAS bf16x8*)(base + wo + i * 1024);
        };
        auto mm = [&](auto CI) {
            constexpr int c = CI;
#pragma unroll
            for (int i = 0; i < 6; ++i)
#pragma unroll
                for (int t = 0; t < 4; ++t) acc[i][t] = MFMA16(wf[c][i], xf[c][t], acc[i][t]);
        };
        auto step = [&](auto CI) {
            constexpr int c = CI;
            raw_barrier();
            rd(std::integral_constant<int, c>{});
            mm(std::integral_constant<int, c ^ 1>{});
#pragma unroll
            for (int k = 0; k < 14; ++k) { __builtin_amdgcn_sched_group_barrier(0x8, 1, 0); __builtin_amdgcn_sched_group_barrier(0x100, 1, 0); }
            PIN16(acc[5][3]);
            sched_fence();
        };
        float cp[4];
#pragma unroll
        for (int k = 0; k < 4; ++k) cp[k] = ((const float*)(ws + WS_CSP))[(k * 90 + u0) * 32 + (threadIdx.x < 192 ? threadIdx.x : 0)];
        ST(1);
        raw_barrier();
        rd(I0{});
        sched_fence();
        ST(2);
#pragma unroll 1
        for (int s = 1; s < G1_NS - 3; s += 2) { step(I1{}); step(I0{}); }
        layer = late_layer();
        *(LAS float*)(L + ML_RED + threadIdx.x * 4) = (cp[0] + cp[1]) + (cp[2] + cp[3]);
        wait_lgkm0();
        u32 st[4];
        f32x4 cs[6];
        u32* stp = (u32*)(ws + WS_LNW) + layer * SEQ + mt * 128 + 64 * wm + (lane & 15);
#pragma unroll
        for (int t = 0; t < 4; ++t) st[t] = __hip_atomic_load(stp + 16 * t, __ATOMIC_RELAXED, __HIP_MEMORY_SCOPE_AGENT);
        sched_fence();
        step(I1{}); step(I0{}); step(I1{});
#pragma unroll
        for (int i = 0; i < 6; ++i) cs[i] = *(LAS f32x4*)(L + ML_RED + (6 * wn + i) * 64 + g * 16);
        for (int spin = 0; spin < (1 << 22) && !__all(st[0] != 0u && st[1] != 0u && st[2] != 0u && st[3] != 0u); ++spin) {
            __builtin_amdgcn_s_sleep(2);
#pragma unroll
            for (int t = 0; t < 4; ++t) st[t] = __hip_atomic_load(stp + 16 * t, __ATOMIC_RELAXED, __HIP_MEMORY_SCOPE_AGENT);
        }
        ST(3);
        mm(I1{});
        ST(4);
        const rsrc_t HR = make_rsrc(ws + WS_H + (1 - layer) * WS_HSTEP + ((4 * mt + 2 * wm) * NU + u0 + 3 * wn) * 1024);
        const u32 hoff = (g >> 1) * 512 + pi_inv(16 * (g & 1) + (lane & 15)) * 16;
        auto glu = [&](int i, int t) {
            const float sb = bfhi(st[t]), nm = -bflo(st[t]);
            const float ce = -1.44269504088896341f * __builtin_amdgcn_rcpf(sb);
            const f32x2 nm2 = {nm, nm}, sb2 = {sb, sb};
            const f32x4 ga = acc[2 * i][t], ua = acc[2 * i + 1][t], cg = cs[2 * i], cu = cs[2 * i + 1];
            u32x2 o;
#pragma unroll
            for (int j = 0; j < 2; ++j) {
                const f32x2 arg = __builtin_elementwise_fma(f32x2{cg[2 * j], cg[2 * j + 1]}, nm2, f32x2{ga[2 * j], ga[2 * j + 1]}) * ce;
                const f32x2 e = {__builtin_amdgcn_exp2f(arg[0]), __builtin_amdgcn_exp2f(arg[1])};
                const f32x2 den = __builtin_elementwise_fma(e, sb2, sb2);
                const f32x2 r = {__builtin_amdgcn_rcpf(den[0]), __builtin_amdgcn_rcpf(den[1])};
                o[j] = pk_bf16(__builtin_elementwise_fma(f32x2{cu[2 * j], cu[2 * j + 1]}, nm2, f32x2{ua[2 * j], ua[2 * j + 1]}) * r);
            }
            return o;
        };
#pragma unroll
        for (int tp = 0; tp < 2; ++tp)
#pragma unroll
            for (int i = 0; i < 3; ++i) {
                const u32x2 a = glu(i, 2 * tp), b = glu(i, 2 * tp + 1);
                const auto r0 = __builtin_amdgcn_permlane16_swap(a[0], b[0], false, false);
                const auto r1 = __builtin_amdgcn_permlane16_swap(a[1], b[1], false, false);
                st16(HR, hoff + i * 1024, (u0 + 3 * wn + i < NU ? 0u : 0x80000000u) + tp * NU * 1024, u32x4{r0[0], r1[0], r0[1], r1[1]});
            }
        ST(5);
    } else {
        layer = late_layer();
    }
    const char* hF = ws + WS_H + (1 - layer) * WS_HSTEP;
    wait_vm<0>();
    ST(6);
    u32 order = 0;
    if (lane == 0) order = __hip_atomic_fetch_add((LAS u32*)(L + ML_CTR), 1u, __ATOMIC_RELAXED, __HIP_MEMORY_SCOPE_WORKGROUP);
    if (__builtin_amdgcn_readfirstlane(order) == 3u) {
        u32* cnt = (u32*)(ws + WS_CNT) + (layer * 16 + mt) * 512;
        u32 v = 0;
        if (fg == 15) __builtin_amdgcn_s_sleep(100);
        else if (lane < 16) v = __hip_atomic_fetch_add(cnt + lane * 32, 1u, __ATOMIC_RELAXED, __HIP_MEMORY_SCOPE_AGENT) + 1u;
        v = __builtin_amdgcn_readfirstlane(v);
#pragma unroll 1
        for (int spin = 0; spin < (1 << 22) && v < 15u; ++spin)
            v = __builtin_amdgcn_readfirstlane(__hip_atomic_load(cnt + fg * 32, __ATOMIC_RELAXED, __HIP_MEMORY_SCOPE_AGENT));
    }
    raw_barrier();
    ST(7);
    {
        const char* src = hF + (2 * m2 + (wave >> 1)) * NU * 1024;
        const u32 vo = lane * 16, dst = l0 + wave * 4096;
        auto issue = [&](auto SI) {
            constexpr int s = SI;
            glds_s<(s < G2_NS - 1 ? 4 : 3), true>(src + blk_unit(s, wave & 1) * 1024, vo, dst + (s % G2_R) * G2_SLOT);
        };
        constexpr int LEAD = G2_R - 2;
        auto nissued = [](int s) { int a = 2 * s + RAMP2, b = s + LEAD + (s < 2 ? 2 - s : 0); a = a < b ? a : b; return a < G2_NS ? a : G2_NS; };
        static_for<RAMP2>([&](auto SI) { issue(SI); });
        static_for<G2_NS>([&](auto SI) {
            constexpr int s = SI, n = nissued(s), n2 = nissued(s + 1);
            constexpr int fly = n - 1 - s;
            wait_vm<(fly > 0 ? 4 * fly - (n == G2_NS ? 1 : 0) : 0)>();
            if constexpr (s == 0) ST(9);
            if constexpr (s == 2) ST(10);
            if constexpr (s == 5) ST(11);
            if constexpr (s == 8) ST(12);
            raw_barrier();
            if constexpr (n2 > n) issue(std::integral_constant<int, n>{});
            if constexpr (n2 > n + 1) issue(std::integral_constant<int, n + 1>{});
        });
    }
    ST(8);
    {
        const rsrc_t xout = make_rsrc(layer ? (char*)out : ws + WS_XA);
        const int pos = 64 * m2 + 16 * wave + (lane >> 3);
        const int tok = (pos & ~255) | ((pos & 3) << 6) | ((pos >> 2) & 63);
        const u32 vo = pinned(layer ? tok * (DM * 2) + 128 * nt + ((lane & 7) << 4)
                                    : 2 * (mt * XTILE + (64 * nt + 16 * wave + (lane >> 3)) * 128 + (m2 & 1) * 64) + (((lane & 7) ^ (lane >> 3)) << 4));
        const u32 a_rd = pinned(ML_OS + wave * 2048 + lane * 16);
        raw_barrier();
        raw_barrier();
        st16(xout, vo, 0, *(LAS u32x4*)(L + a_rd));
        st16(xout, vo, 2048, *(LAS u32x4*)(L + a_rd + 1024));
    }
    ST_FLUSH(layer ? 5 : 2);
}

extern "C" __global__ void s4_tail_kernel() {}

extern "C" void kernel_launch(void* const* d_in, const int* in_sizes, int n_in, void* d_out, int out_size,
                              void* d_ws, size_t ws_size, hipStream_t stream) {
    (void)in_sizes; (void)n_in; (void)out_size; (void)ws_size;
    const u16* x0 = (const u16*)d_in[0];
    const u16* logA = (const u16*)d_in[1];
    const u16* Aim = (const u16*)d_in[2];
    const u16* Cre = (const u16*)d_in[3];
    const u16* Cim = (const u16*)d_in[4];
    const u16* logdt = (const u16*)d_in[5];
    const u16* Dsk = (const u16*)d_in[6];
    const u16* Wg = (const u16*)d_in[8];
    const u16* Wu = (const u16*)d_in[9];
    const u16* Wd = (const u16*)d_in[10];
    (void)d_ws;
    (void)hipFuncSetAttribute((const void*)s4_conv_kernel, hipFuncAttributeMaxDynamicSharedMemorySize, CV_LDS);
    (void)hipFuncSetAttribute((const void*)s4_mlp_kernel, hipFuncAttributeMaxDynamicSharedMemorySize, ML_LDS);

    s4_prep_kernel<<<PREP_BLOCKS, 256, PREP_LDS, stream>>>(x0, logA, Aim, Cre, Cim, logdt, Dsk);
    for (int layer = 0; layer < 2; ++layer) {
        s4_conv_kernel<<<CV_GRID, 256, CV_LDS, stream>>>(Wg, Wu, Wd, layer);
        s4_mlp_kernel<<<256, 512, ML_LDS, stream>>>((u16*)d_out, layer);
    }
}
```

```cpp
#include <hip/hip_runtime.h>
#include <hip/hip_bf16.h>
#include <stdint.h>
#include <utility>

typedef unsigned short u16;
typedef unsigned int u32;
typedef __bf16 bf16x8 __attribute__((ext_vector_type(8)));
typedef __bf16 bf16x4 __attribute__((ext_vector_type(4)));
typedef __bf16 bf16x2 __attribute__((ext_vector_type(2)));
typedef _Float16 f16x8 __attribute__((ext_vector_type(8)));
typedef _Float16 f16x2 __attribute__((ext_vector_type(2)));
typedef float f32x16 __attribute__((ext_vector_type(16)));
typedef float f32x4 __attribute__((ext_vector_type(4)));
typedef float f32x2 __attribute__((ext_vector_type(2)));
typedef double f64x2 __attribute__((ext_vector_type(2)));
typedef u32 u32x4 __attribute__((ext_vector_type(4)));
typedef u32 u32x2 __attribute__((ext_vector_type(2)));

#define LAS __attribute__((address_space(3)))
#define GAS __attribute__((address_space(1)))
#define DI __device__ __forceinline__

constexpr int SEQ = 2048, DM = 512, NST = 128, FF = 1376, NU = FF / 16;
constexpr int XTILE = DM * 128;

constexpr size_t MB = 1 << 20;
constexpr size_t WS_X = 0, WS_XSTEP = 2 * MB;
constexpr size_t WS_K = 8 * MB, WS_W1 = 10 * MB, WS_W2 = 13 * MB, WS_MISC = 15 * MB - 262144, WS_H = 15 * MB, WS_HSTEP = 6 * MB;
constexpr size_t WS_BYTES = 27 * MB;
constexpr size_t WS_CSP = WS_MISC;
constexpr size_t WS_ST64 = WS_MISC + 65536;
constexpr size_t WS_ZERO = WS_ST64 + SEQ * 16;
constexpr size_t WS_LNW = WS_ZERO + SEQ * 16, WS_CNT = WS_LNW + 2 * SEQ * 4;
constexpr int ZERO_BYTES = SEQ * 16 + 2 * SEQ * 4 + 512 * 128;
__device__ __attribute__((aligned(4096))) char g_ws[WS_BYTES];

#define TG1 4

#define ST_DECL do {} while (0)
#define ST_DECLN do {} while (0)
#define STN(i) do {} while (0)
#define ST(i) do {} while (0)
#define ST_FLUSH(kid) do {} while (0)

#define TID_ (threadIdx.x + threadIdx.y)
#define LAYER_ (__builtin_amdgcn_readlane(threadIdx.y, 1) != 0u ? 1 : 0)

DI float bf2f(u16 h) { return __uint_as_float((u32)h << 16); }
DI float bflo(u32 w) { return __uint_as_float(w << 16); }
DI float bfhi(u32 w) { return __uint_as_float(w & 0xffff0000u); }
DI u32 pk_bf16(f32x2 v) { return __builtin_bit_cast(u32, __builtin_convertvector(v, bf16x2)); }
DI u32 pk_bf16(float lo, float hi) { return pk_bf16(f32x2{lo, hi}); }
DI float rbf(float x) { return bflo(pk_bf16(x, x)); }
DI u32 pk_f16(f32x2 v) { return __builtin_bit_cast(u32, __builtin_convertvector(v, f16x2)); }
DI float dot2(u32 a, u32 b, float c) { return __builtin_amdgcn_fdot2_f32_bf16(__builtin_bit_cast(bf16x2, a), __builtin_bit_cast(bf16x2, b), c, false); }
DI u32 perm_lo(u32 hi, u32 lo) { return __builtin_amdgcn_perm(hi, lo, 0x05040100u); }
DI u32 perm_hi(u32 hi, u32 lo) { return __builtin_amdgcn_perm(hi, lo, 0x07060302u); }
DI int pi_fwd(int r) { return (((r >> 2) & 1) << 4) | ((r >> 3) << 2) | (r & 3); }
DI int pi_inv(int p) { return (((p >> 2) & 3) << 3) | ((p >> 4) << 2) | (p & 3); }
DI int xt_off(int ch, int pos) { return (pos >> 7) * XTILE + ch * 128 + (pos & 127); }
DI int st_thread(int pos) { return ((pos >> 8) << 5) | (((pos >> 5) & 7) << 2) | ((pos >> 2) & 3); }
DI int st_item(int pos) { return (((pos >> 4) & 1) << 2) | (pos & 3); }
DI int st_slot(int thread, int item, int nitem) { return ((thread >> 6) * nitem + item) * 64 + (thread & 63); }
constexpr u32 ONES = 0x3f803f80u;

template <int N> DI void wait_vm() { asm volatile("s_waitcnt vmcnt(%0)" ::"n"(N) : "memory"); }
DI void wait_lgkm0() { asm volatile("s_waitcnt lgkmcnt(0)" ::: "memory"); }
DI void raw_barrier() { asm volatile("" ::: "memory"); __builtin_amdgcn_s_barrier(); asm volatile("" ::: "memory"); }
DI float opaque(float v) { asm("" : "+v"(v)); return v; }
DI u32 pinned(u32 v) { asm volatile("" : "+v"(v)); return v; }
#define PIN16(x) asm volatile("" : "+v"(x))
DI void sched_fence() { __builtin_amdgcn_sched_barrier(0); }
DI void glds16(const void* src, LAS char* dst) {
    __builtin_amdgcn_global_load_lds((const GAS void*)src, (LAS void*)dst, 16, 0, 0);
}
DI bf16x8 tr_frag(LAS char* a0, LAS char* a1) {
    bf16x4 lo = __builtin_amdgcn_ds_read_tr16_b64_v4bf16((LAS bf16x4*)a0);
    bf16x4 hi = __builtin_amdgcn_ds_read_tr16_b64_v4bf16((LAS bf16x4*)a1);
    return __builtin_shufflevector(lo, hi, 0, 1, 2, 3, 4, 5, 6, 7);
}
#define GLDS_HEAD "s_mov_b32 %0, m0\n\ts_mov_b32 m0, %3\n\ts_nop 0\n\t"
#define GLDS_ONE(off, sc) "global_load_lds_dwordx4 %1, %2 offset:" #off sc "\n\t"
#define GLDS_TAIL "s_mov_b32 m0, %0" : "=&s"(keep) : "v"(voff), "s"(ubase), "s"(lds_dst) : "memory"
#define GLDS_BODY(sc)                                                                                                          \
    if constexpr (N == 1) asm volatile(GLDS_HEAD GLDS_ONE(0, sc) GLDS_TAIL);                                                   \
    else if constexpr (N == 2) asm volatile(GLDS_HEAD GLDS_ONE(0, sc) GLDS_ONE(1024, sc) GLDS_TAIL);                           \
    else if constexpr (N == 3) asm volatile(GLDS_HEAD GLDS_ONE(0, sc) GLDS_ONE(1024, sc) GLDS_ONE(2048, sc) GLDS_TAIL);        \
    else asm volatile(GLDS_HEAD GLDS_ONE(0, sc) GLDS_ONE(1024, sc) GLDS_ONE(2048, sc) GLDS_ONE(3072, sc) GLDS_TAIL)
template <int N, bool DEV = false> DI void glds_s(const char* ubase, u32 voff, u32 lds_dst) {
    u32 keep;
    lds_dst = __builtin_amdgcn_readfirstlane(lds_dst);
    if constexpr (DEV) { GLDS_BODY(" sc1"); } else { GLDS_BODY(""); }
}
typedef __amdgpu_buffer_rsrc_t rsrc_t;
DI rsrc_t make_rsrc(void* base) { return __builtin_amdgcn_make_buffer_rsrc(base, 0, 0x7fffffff, 0x00020000); }
template <int AUX = 16> DI void st16(rsrc_t r, u32 voff, u32 soff, u32x4 v) { __builtin_amdgcn_raw_buffer_store_b128(v, r, voff + soff, 0, AUX); }
template <int AUX = 16> DI void st8(rsrc_t r, u32 voff, u32 soff, u32x2 v) { __builtin_amdgcn_raw_buffer_store_b64(v, r, voff, soff, AUX); }
#define H_AUX 16
#define MFMA16(a, b, c) __builtin_amdgcn_mfma_f32_16x16x32_bf16((a), (b), (c), 0, 0, 0)
#define MFMA32(a, b, c) __builtin_amdgcn_mfma_f32_32x32x16_bf16((a), (b), (c), 0, 0, 0)

template <class F, int... I> DI void static_for_impl(F&& f, std::integer_sequence<int, I...>) { (f(std::integral_constant<int, I>{}), ...); }
template <int N, class F> DI void static_for(F&& f) { static_for_impl(f, std::make_integer_sequence<int, N>{}); }

template <int CTRL, int RM = 0xf> DI float dppz(float v) { return __int_as_float(__builtin_amdgcn_update_dpp(0, __float_as_int(v), CTRL, RM, 0xf, true)); }
DI float wave_sum63(float v) {
    v += dppz<0x111>(v); v += dppz<0x112>(v); v += dppz<0x114>(v); v += dppz<0x118>(v);
    v += dppz<0x142, 0xa>(v); v += dppz<0x143, 0xc>(v);
    return v;
}
DI float wave_max63(float v) {
    v = fmaxf(v, dppz<0x111>(v)); v = fmaxf(v, dppz<0x112>(v)); v = fmaxf(v, dppz<0x114>(v)); v = fmaxf(v, dppz<0x118>(v));
    v = fmaxf(v, dppz<0x142, 0xa>(v)); v = fmaxf(v, dppz<0x143, 0xc>(v));
    return v;
}
DI float rdlane63(float v) { return __int_as_float(__builtin_amdgcn_readlane(__float_as_int(v), 63)); }
DI float half_sum2(float a, float b) {
    auto r = __builtin_amdgcn_permlane32_swap(__float_as_uint(a), __float_as_uint(b), false, false);
    return __uint_as_float(r[0]) + __uint_as_float(r[1]);
}
DI float row_pair_sum(float a) {
    auto r = __builtin_amdgcn_permlane16_swap(__float_as_uint(a), __float_as_uint(a), false, false);
    return __uint_as_float(r[0]) + __uint_as_float(r[1]);
}
DI void ln_scalars(float S, float Q, float& mb, float& sb) {
    const float mean = S * (1.f / DM);
    const float var = fmaxf(__builtin_fmaf(-mean, mean, Q * (1.f / DM)), 0.f);
    const u32 mv = pk_bf16(mean, var);
    mb = bflo(mv);
    sb = rbf(__builtin_amdgcn_sqrtf(rbf(bfhi(mv) + 1e-5f)));
}

struct cm { f32x2 b, r; };
DI cm mk(f32x2 b) { return {b, f32x2{-b[1], b[0]}}; }
DI f32x2 cmul(f32x2 a, const cm& m) {
    return __builtin_elementwise_fma(__builtin_shufflevector(a, a, 1, 1), m.r, __builtin_shufflevector(a, a, 0, 0) * m.b);
}
DI f32x2 cexp_p(float arl, float aiv, float p) {
    const float mag = __builtin_amdgcn_exp2f(arl * p);
    const float th = __builtin_amdgcn_fractf(aiv * p);
    return f32x2{mag * __builtin_amdgcn_cosf(th), mag * __builtin_amdgcn_sinf(th)};
}
DI float exp_bf(float x) {
    const float t = x * 1.44269504088896341f;
    const float r = __builtin_fmaf(x, 1.44269504088896341f, -t) + x * 1.92596299e-8f;
    const float e = __builtin_amdgcn_exp2f(t);
    return rbf(__builtin_fmaf(e * r, 0.693147181f, e));
}

constexpr int KG_ROW = 528;
constexpr int KG_TROW = 80;
constexpr int KG_B = 21 * KG_ROW, KG_TAB = 53 * KG_ROW, PREP_LDS = KG_TAB + 32 * KG_TROW;
constexpr int PB_WARM = 16, PB_KGEN = 512, PB_TR = 128, PB_ST = 128;
constexpr int PREP_BLOCKS = PB_WARM + PB_KGEN + PB_TR + PB_ST;

extern "C" __global__ __launch_bounds__(256, 2) void s4_prep_kernel(
    const u16* __restrict__ x0, const u16* __restrict__ logA, const u16* __restrict__ Aim,
    const u16* __restrict__ Cre, const u16* __restrict__ Cim, const u16* __restrict__ logdt, const u16* __restrict__ Dskip) {
    char* ws = g_ws;
    extern __shared__ __attribute__((aligned(16))) char smem_raw[];
    LAS char* L = (LAS char*)smem_raw;
    {
        unsigned long long pc;
        u32 off = (((blockIdx.x >> 3) << 8) + threadIdx.x) * 128u;
        asm volatile("s_cmp_ge_u32 %2, %3\n\ts_cbranch_scc1 2f\n\ts_getpc_b64 %0\n1:\n\t"
                     "v_min_u32 %1, s4_tail_kernel-1b, %1\n\tglobal_load_dword %1, %1, %0\n\ts_waitcnt vmcnt(0)\n\ts_endpgm\n2:"
                     : "=&s"(pc), "+v"(off) : "s"(blockIdx.x), "n"(PB_WARM) : "scc", "memory");
    }
    asm volatile("" ::"s"(x0), "s"(logA), "s"(Aim), "s"(Cre), "s"(Cim), "s"(logdt), "s"(Dskip));
    const rsrc_t WS = make_rsrc(ws);
    const int tid = threadIdx.x, lane = tid & 63;
    const int wave = __builtin_amdgcn_readfirstlane(tid >> 6);
    int b = blockIdx.x - PB_WARM;
    ST_DECL;
    if (__builtin_expect(b >= PB_KGEN, 0)) {
        b -= PB_KGEN;
        if (b < PB_TR) {
            const int w = b * 4 + wave, tau = w >> 6, lg = (w >> 4) & 3, cgg = w & 15;
            const int ln = lg * 16 + (lane & 15), cg = cgg * 4 + (lane >> 4);
            u32x4 in[4];
#pragma unroll
            for (int g = 0; g < 4; ++g) in[g] = *(const u32x4*)(x0 + (256 * tau + 64 * g + ln) * DM + 8 * cg);
            const u32 dst = 2 * xt_off(8 * cg, 256 * tau + 4 * ln);
#pragma unroll
            for (int q = 0; q < 4; ++q) {
                st8(WS, dst + (2 * q) * 256, WS_X, u32x2{perm_lo(in[1][q], in[0][q]), perm_lo(in[3][q], in[2][q])});
                st8(WS, dst + (2 * q + 1) * 256, WS_X, u32x2{perm_hi(in[1][q], in[0][q]), perm_hi(in[3][q], in[2][q])});
            }
            ST_FLUSH(0);
            return;
        }
        b -= PB_TR;
        {
            const int w = b * 4 + wave, tau = w >> 6, ln = w & 63;
            u32x4 in[4];
#pragma unroll
            for (int g = 0; g < 4; ++g) in[g] = *(const u32x4*)(x0 + (256 * tau + 64 * g + ln) * DM + 8 * lane);
            if ((b * 256 + tid) * 16 < ZERO_BYTES) st16(WS, (b * 256 + tid) * 16, WS_ZERO, u32x4{0u, 0u, 0u, 0u});
            if (b >= 96) st16(WS, ((b - 96) * 256 + tid) * 16, WS_W1 + NU * 32 * 1024, u32x4{0u, 0u, 0u, 0u});
#pragma unroll
            for (int g = 0; g < 4; ++g) {
                float S = 0.f, Q = 0.f;
#pragma unroll
                for (int q = 0; q < 4; ++q) { S = dot2(in[g][q], ONES, S); Q = dot2(in[g][q], in[g][q], Q); }
                S = wave_sum63(S); Q = wave_sum63(Q);
                const int pos = 256 * tau + 4 * ln + g;
                const u32 dst = lane == 63 ? 16 * st_slot(st_thread(pos), st_item(pos), 8) : 0x80000000u;
                const u32x4 v = __builtin_bit_cast(u32x4, f64x2{(double)S, (double)Q});
                st16(WS, dst, WS_ST64, v);
            }
            ST_FLUSH(0);
            return;
        }
    }
    {
        const int d = b, n = tid & 127;
        u32 h_dt = logdt[d], h_D = Dskip[d], h_la = logA[d * NST + n], h_ai = Aim[d * NST + n];
        u32 h_cr = Cre[d * NST + n], h_ci = Cim[d * NST + n];
        u32 ca = ((const u32*)(Cre + d * NST))[lane], cc = ((const u32*)(Cim + d * NST))[lane];
        const int rb = wave & 1, cb = wave >> 1, q = lane >> 4;
        const u32 a_pa = pinned((16 * rb + (lane & 15)) * KG_ROW + q * 16);
        const u32 a_pb = pinned(KG_B + (16 * cb + (lane & 15)) * KG_ROW + q * 16);
        const u32 a_tw = pinned(KG_TAB + (16 * cb + (lane & 15)) * KG_TROW + 32 * rb + 8 * q);
        const int S = tid >> 5, col = tid & 31;
        auto row0 = [](int sp) { return min(min(8 * sp, 8 + 2 * sp), 12 + sp); };
        const u32 a_t0 = pinned(KG_TAB + col * KG_TROW);
        const u32 a_ta = pinned(a_t0 + 2 * (row0(S) & ~1));
        const u32 a_tb = pinned(a_ta + (S == 1 ? 4 : 0));
        const u32 a_tn = pinned(KG_TAB + ((tid + 1) & 31) * KG_TROW + 2 * row0((tid + 1) >> 5));
        const u32 t0s = (S >= 4 && (S & 1)) ? 0x0302u : 0x0100u;
        const u32 s0 = pinned(t0s * 0x10001u);
        const u32 s1 = pinned(S == 1 ? 0x05040302u : S < 4 ? 0x07060100u : s0);
        const u32 s2 = pinned(S == 1 ? 0x01000100u : S < 4 ? 0x03020302u : (tid & 1) ? 0x05040504u : 0x05040000u | t0s);
        const u32 s3 = pinned(S == 1 ? 0x05040302u : 0x05040504u);
        asm volatile("" : "+v"(h_dt), "+v"(h_D), "+v"(h_la), "+v"(h_ai), "+v"(h_cr), "+v"(h_ci), "+v"(ca), "+v"(cc));
        const float dt = exp_bf(bflo(h_dt));
        const float arl = -exp_bf(bflo(h_la)) * dt * 1.44269504088896341f;
        float aiv = bflo(h_ai) * dt * 0.159154943091895336f;
        const float mx = wave_max63(fmaxf(fmaxf(fabsf(bflo(ca)), fabsf(bfhi(ca))), fmaxf(fabsf(bflo(cc)), fabsf(bfhi(cc)))));
        const u32 eb = min(max((u32)__builtin_amdgcn_readlane(__float_as_int(mx), 63) & 0x7f800000u, 32u << 23), 224u << 23);
        const float sc = __uint_as_float((261u << 23) - eb);
        const float back = dt * (__uint_as_float(eb) * (2.f / 32768.f));
        ST(1);
        if (wave < 2) {
            aiv = -aiv;
            const f32x2 t0 = {bflo(h_cr) * sc, -bflo(h_ci) * sc};
            const cm w1 = mk(cexp_p(arl, aiv, 1.f)), v1 = mk(cexp_p(arl, aiv, 256.f));
            const cm w2 = mk(cmul(w1.b, w1)), w4 = mk(cmul(w2.b, w2)), v2 = mk(cmul(v1.b, v1)), v4 = mk(cmul(v2.b, v2));
            LAS u32* A = (LAS u32*)(L + 4 * n);
            auto put = [&](int row, f32x2 z) { A[row * (KG_ROW / 4)] = pk_f16(z); };
            const f32x2 r1 = cmul(t0, w1), r2 = cmul(t0, w2), r3 = cmul(r1, w2);
            const f32x2 t1 = cmul(t0, v1), t2 = cmul(t0, v2), t3 = cmul(t1, v2), t4 = cmul(t0, v4), r9 = cmul(t1, w2);
            put(0, t0); put(1, r1); put(2, r2); put(3, r3);
            put(4, cmul(t0, w4)); put(5, cmul(r1, w4)); put(6, cmul(r2, w4)); put(7, cmul(r3, w4));
            put(8, t1); put(9, r9); put(10, cmul(t1, w4)); put(11, cmul(r9, w4));
            put(12, t2); put(13, cmul(t2, w4)); put(14, t3); put(15, cmul(t3, w4));
            put(16, t4); put(17, cmul(t1, v4)); put(18, cmul(t2, v4)); put(19, cmul(t3, v4)); put(20, cmul(t4, v4));
        } else {
            f32x2 e[16];
            const f32x2 b1 = cexp_p(arl, aiv, 8.f);
            const cm b8 = mk(cexp_p(arl, aiv, 64.f)), b2 = mk(cmul(b1, mk(b1))), b4 = mk(cmul(b2.b, b2)), b16 = mk(cmul(b8.b, b8));
            LAS u32* B = (LAS u32*)(L + KG_B + 4 * n);
            e[0] = f32x2{256.f, 0.f}; e[1] = b1 * 256.f; e[2] = b2.b * 256.f; e[3] = cmul(e[1], b2);
            e[4] = b4.b * 256.f; e[8] = b8.b * 256.f;
#pragma unroll
            for (int c = 1; c < 4; ++c) e[4 + c] = cmul(e[c], b4);
#pragma unroll
            for (int c = 1; c < 8; ++c) e[8 + c] = cmul(e[c], b8);
#pragma unroll
            for (int c = 0; c < 16; ++c) {
                B[c * (KG_ROW / 4)] = pk_f16(e[c]);
                B[(16 + c) * (KG_ROW / 4)] = pk_f16(c ? cmul(e[c], b16) : b16.b * 256.f);
            }
        }
        ST(2);
        __syncthreads();
        ST(3);
        {
            f16x8 fa[8], fb[8];
#pragma unroll
            for (int ks = 0; ks < 8; ++ks) { fa[ks] = *(LAS f16x8*)(L + a_pa + 64 * ks); fb[ks] = *(LAS f16x8*)(L + a_pb + 64 * ks); }
            sched_fence();
            f32x4 acc = {0.f, 0.f, 0.f, 0.f}, acc2 = {0.f, 0.f, 0.f, 0.f};
#pragma unroll
            for (int ks = 0; ks < 8; ks += 2) {
                acc = __builtin_amdgcn_mfma_f32_16x16x32_f16(fa[ks], fb[ks], acc, 0, 0, 0);
                acc2 = __builtin_amdgcn_mfma_f32_16x16x32_f16(fa[ks + 1], fb[ks + 1], acc2, 0, 0, 0);
            }
            acc += acc2;
            const float add0 = tid == 0 ? bflo(h_D) : 0.f;
            *(LAS u32x2*)(L + a_tw) = u32x2{pk_bf16(__builtin_fmaf(acc[0], back, add0), acc[1] * back), pk_bf16(acc[2] * back, acc[3] * back)};
        }
        ST(4);
        __syncthreads();
        ST(5);
        {
            const u32x4 w = *(LAS u32x4*)(L + a_t0);
            const u32 ta = *(LAS u32*)(L + a_ta), tb = *(LAS u32*)(L + a_tb), tn = *(LAS u16*)(L + a_tn);
            const u32x4 o = {__builtin_amdgcn_perm(ta, ta, s0), __builtin_amdgcn_perm(tb, ta, s1),
                             __builtin_amdgcn_perm(tn, tb, s2), __builtin_amdgcn_perm(tn, tb, s3)};
            st16(WS, 16 * tid, WS_K + d * SEQ * 2, S == 0 ? w : o);
        }
        ST(6);
        ST_FLUSH(0);
    }
}

constexpr int RT_W1 = 176, RT_W2 = 44;
DI void retile(LAS char* L, char* ws, const u16* Wg, const u16* Wu, const u16* Wd, int b, int tid, int wave, int lane) {
    const rsrc_t WS = make_rsrc(ws);
    const u32 lb = (u32)(uintptr_t)L + wave * 4096;
    const int g = lane >> 4, q = (lane & 15) >> 2, pp = lane & 3;
    u32x2 f[8];
    if (b < RT_W1) {
        const int ct = b >> 3, ty = (b >> 2) & 1, rt = b & 3;
        const u32 vo = (128 * rt + 32 * wave + (lane >> 3)) * (FF * 2) + ct * 128 + (((lane & 7) * 16) & (ct == 21 ? 63 : 127));
#pragma unroll
        for (int i = 0; i < 4; ++i) glds_s<1>((const char*)(ty ? Wu : Wg), vo + 8 * i * (FF * 2), lb + i * 1024);
        wait_vm<0>();
        const u32 a = lb + (8 * g + q) * 128 + pp * 8;
        asm volatile("ds_read_b64_tr_b16 %0, %8\n\tds_read_b64_tr_b16 %1, %8 offset:512\n\t"
                     "ds_read_b64_tr_b16 %2, %8 offset:32\n\tds_read_b64_tr_b16 %3, %8 offset:544\n\t"
                     "ds_read_b64_tr_b16 %4, %8 offset:64\n\tds_read_b64_tr_b16 %5, %8 offset:576\n\t"
                     "ds_read_b64_tr_b16 %6, %8 offset:96\n\tds_read_b64_tr_b16 %7, %8 offset:608\n\ts_waitcnt lgkmcnt(0)"
                     : "=&v"(f[0]), "=&v"(f[1]), "=&v"(f[2]), "=&v"(f[3]), "=&v"(f[4]), "=&v"(f[5]), "=&v"(f[6]), "=&v"(f[7])
                     : "v"(a) : "memory");
        LAS float* red = (LAS float*)(L + 32768);
#pragma unroll
        for (int i = 0; i < 4; ++i) {
            const int u = 4 * ct + i;
            if (u < NU) st16(WS, lane * 16, WS_W1 + ((u * 16 + 4 * rt + wave) * 2 + ty) * 1024, u32x4{f[2 * i][0], f[2 * i][1], f[2 * i + 1][0], f[2 * i + 1][1]});
        }
#pragma unroll
        for (int i = 0; i < 4; i += 2) {
            const float s0 = dot2(f[2 * i][0], ONES, dot2(f[2 * i][1], ONES, dot2(f[2 * i + 1][0], ONES, dot2(f[2 * i + 1][1], ONES, 0.f))));
            const float s1 = dot2(f[2 * i + 2][0], ONES, dot2(f[2 * i + 2][1], ONES, dot2(f[2 * i + 3][0], ONES, dot2(f[2 * i + 3][1], ONES, 0.f))));
            const float v = row_pair_sum(half_sum2(s0, s1));
            if ((lane & 16) == 0) red[wave * 64 + (i + (lane >> 5)) * 16 + (lane & 15)] = v;
        }
        __syncthreads();
        if (tid < 64 && 4 * ct + (tid >> 4) < NU)
            ((float*)(ws + WS_CSP))[((rt * 90 + 4 * ct + (tid >> 4)) * 2 + ty) * 16 + (tid & 15)] =
                (red[tid] + red[64 + tid]) + (red[128 + tid] + red[192 + tid]);
    } else {
        const int bb = b - RT_W1, rt = bb >> 2, cp = bb & 3;
#pragma unroll
        for (int i = 0; i < 8; ++i)
            glds_s<1>((const char*)Wd, min(128 * rt + 32 * wave + (lane >> 3) + 8 * (i & 3), FF - 1) * (DM * 2) + (2 * cp + (i >> 2)) * 128 + (lane & 7) * 16,
                      lb + (i >> 2) * 16384 + (i & 3) * 1024);
        wait_vm<0>();
#pragma unroll
        for (int h = 0; h < 2; ++h) {
            const u32 a = lb + h * 16384 + (8 * (g >> 1) + q) * 128 + pi_fwd(16 * (g & 1) + 4 * pp) * 2;
            asm volatile("ds_read_b64_tr_b16 %0, %8\n\tds_read_b64_tr_b16 %1, %8 offset:512\n\t"
                         "ds_read_b64_tr_b16 %2, %8 offset:64\n\tds_read_b64_tr_b16 %3, %8 offset:576\n\t"
                         "ds_read_b64_tr_b16 %4, %8 offset:2048\n\tds_read_b64_tr_b16 %5, %8 offset:2560\n\t"
                         "ds_read_b64_tr_b16 %6, %8 offset:2112\n\tds_read_b64_tr_b16 %7, %8 offset:2624\n\ts_waitcnt lgkmcnt(0)"
                         : "=&v"(f[0]), "=&v"(f[1]), "=&v"(f[2]), "=&v"(f[3]), "=&v"(f[4]), "=&v"(f[5]), "=&v"(f[6]), "=&v"(f[7])
                         : "v"(a) : "memory");
#pragma unroll
            for (int i = 0; i < 4; ++i) {
                const int u = 8 * rt + 2 * wave + (i >> 1), cb = 4 * cp + 2 * h + (i & 1);
                if (u < NU) st16(WS, lane * 16, WS_W2 + (cb * NU + u) * 1024, u32x4{f[2 * i][0], f[2 * i][1], f[2 * i + 1][0], f[2 * i + 1][1]});
            }
        }
    }
}

constexpr int CV_KROW = (64 + SEQ) * 2;
constexpr int CV_ZRS = 84 * 16;
constexpr int CV_Z = 2 * CV_KROW;
constexpr int CV_C = CV_Z + 2 * 4 * CV_ZRS;
constexpr int CV_DUMP = CV_C + 2 * 9 * 64;
constexpr int CV_X = CV_DUMP + 1024 + 9 * 64;
constexpr int CV_S = CV_X + 2 * SEQ * 2;
constexpr int CV_LDS = CV_S + SEQ * 16;
#define PF_SLEEP 40
#define CV_GRID 512

template <int HALF> struct TauSet { static constexpr int t(int i) { return HALF == 0 ? (i == 0 ? 0 : i == 1 ? 2 : i == 2 ? 5 : 7) : (i == 0 ? 1 : i == 1 ? 3 : i == 2 ? 4 : 6); } };

template <int HALF>
DI void conv_main(LAS char* L, rsrc_t xout, u32 xoff, u32 a_z, u32 a_k, u32 a_cw, u32 a_cr, u32 a_x, const bf16x8 (&kf0)[8]
) {
    using TS = TauSet<HALF>;
    constexpr int TMAX = TS::t(3);
    f32x16 acc[4];
#pragma unroll
    for (int i = 0; i < 4; ++i)
#pragma unroll
        for (int r = 0; r < 16; ++r) acc[i][r] = 0.f;
    LAS char* zb = L + a_z;
    LAS char* kb = L + a_k;
    u32x2 xm[4];
    bf16x8 kf[2][TMAX + 1], zf[2][TMAX + 1];
    auto load = [&](auto EI) {
        constexpr int ei = EI, e = 2 * ei + 1;
#pragma unroll
        for (int m = 0; m <= TMAX; ++m) {
            if constexpr (ei == 0) kf[0][m] = kf0[m];
            else kf[ei & 1][m] = tr_frag(kb + 32 + 64 * (8 * m + e), kb + 64 * (8 * m + e));
            zf[ei & 1][m] = *(LAS bf16x8*)(zb + 16 * (8 * m + 7 - e));
            if constexpr (ei == 0) sched_fence();
        }
    };
    load(std::integral_constant<int, 0>{});
    static_for<3>([&](auto EI) {
        constexpr int ei = EI;
        sched_fence();
        load(std::integral_constant<int, ei + 1>{});
        if constexpr (ei == 2) {
#pragma unroll
            for (int i = 0; i < 4; ++i) xm[i] = *(LAS u32x2*)(L + a_x + 512 * TS::t(i));
        }
#pragma unroll
        for (int i = 0; i < 4; ++i)
#pragma unroll
            for (int mu = 0; mu <= TS::t(i); ++mu) acc[i] = MFMA32(zf[ei & 1][TS::t(i) - mu], kf[ei & 1][mu], acc[i]);
        if constexpr (ei == 0) __builtin_amdgcn_sched_group_barrier(0x8, 2, 0);
#pragma unroll
        for (int k = 0; k < 13; ++k) { __builtin_amdgcn_sched_group_barrier(0x8, 1, 0); __builtin_amdgcn_sched_group_barrier(0x100, 2, 0); }
        PIN16(acc[0]); PIN16(acc[1]); PIN16(acc[2]); PIN16(acc[3]);
    });
    sched_fence();
    u32 a_c[4];
#pragma unroll
    for (int i = 0; i < 4; ++i) a_c[i] = pinned(a_cr ? a_cr + 64 * TS::t(i) : 0u);
    float y[4][4];
    auto post = [&](auto II) {
        constexpr int i = II;
        float s1[4], s2[4], v[4];
#pragma unroll
        for (int g = 0; g < 4; ++g) {
            v[g] = opaque(acc[i][4 * g + 2] + dppz<0x130>(acc[i][4 * g + 1]));
            s2[g] = opaque(acc[i][4 * g + 2] + dppz<0x138>(acc[i][4 * g + 3]));
        }
#pragma unroll
        for (int g = 0; g < 4; ++g) v[g] = opaque(acc[i][4 * g + 3] + dppz<0x130>(v[g]));
        *(LAS f32x4*)(L + a_cw + 64 * TS::t(i)) = f32x4{v[0], v[1], v[2], v[3]};
#pragma unroll
        for (int g = 0; g < 4; ++g) {
            s1[g] = opaque(acc[i][4 * g + 1] + dppz<0x138>(s2[g]));
            y[i][g] = opaque(acc[i][4 * g] + ((g & 1) ? bfhi(xm[i][g >> 1]) : bflo(xm[i][g >> 1])));
        }
#pragma unroll
        for (int g = 0; g < 4; ++g) y[i][g] = opaque(y[i][g] + dppz<0x138>(s1[g]));
    };
    static_for<4>([&](auto II) {
        constexpr int i = II;
#pragma unroll
        for (int mu = 0; mu <= TS::t(i); ++mu) acc[i] = MFMA32(zf[1][TS::t(i) - mu], kf[1][mu], acc[i]);
        if constexpr (i > 0) {
            post(std::integral_constant<int, i - 1>{});
            __builtin_amdgcn_sched_group_barrier(0x8, 2, 0);
#pragma unroll
            for (int k = 2; k <= TS::t(i); ++k) { __builtin_amdgcn_sched_group_barrier(0x202, 6, 0); __builtin_amdgcn_sched_group_barrier(0x8, 1, 0); }
        }
        PIN16(acc[i]);
        sched_fence();
    });
    ST(4);
    post(std::integral_constant<int, 3>{});
    ST(5);
    wait_lgkm0();
    raw_barrier();
    ST(6);
    f32x4 cv[4];
    float cp[4];
#pragma unroll
    for (int i = 0; i < 4; ++i) {
        cv[i] = *(LAS f32x4*)(L + a_c[i] + 64);
        cp[i] = *(LAS float*)(L + a_c[i] + 12);
    }
    static_for<4>([&](auto II) {
        constexpr int i = II;
        st8(xout, xoff, 4 * TS::t(i) * XTILE, u32x2{pk_bf16(opaque(TS::t(i) ? y[i][0] + cp[i] : y[i][0]), opaque(y[i][1] + cv[i][0])),
                                                    pk_bf16(opaque(y[i][2] + cv[i][1]), opaque(y[i][3] + cv[i][2]))});
    });
}

extern "C" __global__ __launch_bounds__(256, 2) void s4_conv_kernel(const u16* Wg, const u16* Wu, const u16* Wd) {
    char* ws = g_ws;
    extern __shared__ __attribute__((aligned(16))) char smem_raw[];
    LAS char* L = (LAS char*)smem_raw;
    ST_DECLN;
    const int layer = LAYER_;
    const int tid = TID_, lane = tid & 63, wave = __builtin_amdgcn_readfirstlane(tid >> 6);
    if (__builtin_expect(blockIdx.x >= 256, 0)) {
        if (layer == 0) {
            retile(L, ws, Wg, Wu, Wd, blockIdx.x - 256, tid, wave, lane);
            return;
        }
        const int xcd = blockIdx.x & 7, sl = ((blockIdx.x - 256) >> 3) * 4 + wave;
        constexpr int P1 = (96 / (8 / TG1)) * 32, N1 = (P1 + 127) / 128;
        const int k1 = (xcd / TG1) * P1 + sl * N1;
        const u32 dst = (u32)(uintptr_t)L + wave * 1024;
        __builtin_amdgcn_s_sleep(PF_SLEEP);
#pragma unroll 1
        for (int i = 0; i < N1; ++i) glds_s<1>(ws + WS_W1 + (size_t)min(k1 + i, 90 * 32 - 1) * 1024, lane * 16, dst);
        wait_vm<0>();
        return;
    }
    const u16* xin = (const u16*)(ws + WS_X + 2 * layer * WS_XSTEP);
    const rsrc_t xout = make_rsrc(ws + WS_X + (2 * layer + 1) * WS_XSTEP);
    const int ch = wave >> 1, half = wave & 1, d0 = 2 * blockIdx.x, d = d0 + ch;
    const int tau = tid >> 5, c = tid & 3, sp = (tid >> 2) & 7;
    const int p0 = 256 * tau + 32 * sp + 4 * c;
    const u32 l0 = (u32)(uintptr_t)L;
    {
        const int rot = (blockIdx.x >> 3) & 3;
        const char* sb = ws + WS_ST64 + layer * (SEQ * 16) + wave * 8192;
#pragma unroll
        for (int j = 0; j < 4; ++j) {
            const int k = (j + rot) & 3;
            glds_s<2>(sb + k * 2048, lane * 16, l0 + CV_S + wave * 8192 + k * 2048);
        }
    }
    glds_s<2>(ws + WS_K + (size_t)(d * SEQ + half * 1024) * 2, lane * 16, l0 + ch * CV_KROW + 128 + half * 2048);
    {
        const char* xb = (const char*)(xin + 4 * wave * XTILE + d0 * 128);
        const u32 vo = (lane >> 4) * (XTILE * 2) + (lane & 15) * 16;
        glds_s<1>(xb, vo, l0 + CV_X + wave * 1024);
        glds_s<1>(xb + 256, vo, l0 + CV_X + 4096 + wave * 1024);
    }
    const u32 xoff = 2 * xt_off(d, 4 * lane);
    if (wave == 0) *(LAS u32x4*)(L + CV_Z + (lane >> 3) * CV_ZRS + 16 * (lane & 7)) = u32x4{0u, 0u, 0u, 0u};
    if (wave == 1) *(LAS u32x4*)(L + ((lane >> 3) & 1) * CV_KROW + 16 * (lane & 7)) = u32x4{0u, 0u, 0u, 0u};
    const int hh = lane >> 5, cbase = CV_C + ch * 9 * 64;
    const u32 a_z = pinned(CV_Z + (ch * 4 + (lane & 3)) * CV_ZRS + 128 + 16 * (((lane & 31) >> 2) + hh) - 16 * 7);
    const u32 a_k = pinned(ch * CV_KROW + 128 + 2 * (16 * ((lane >> 4) & 1) - 32 * hh - 4 * ((lane & 15) >> 2) + 4 * (lane & 3)) - 32);
    const u32 a_cw = pinned((lane >= 61 ? cbase + 64 - 61 * 16 : CV_DUMP) + lane * 16);
    const u32 a_cr = pinned(lane < 3 ? cbase + 16 * lane : 0);
    const u32 a_x = pinned(CV_X + ch * 4096 + 8 * lane);
    STN(1);
    f32x2 rc[2][4];
    LAS char* sl = L + CV_S + wave * 8192 + lane * 16;
    wait_vm<4>();
    ST(8);
#pragma unroll
    for (int j = 0; j < 8; ++j) {
        const f64x2 sq = *(LAS f64x2*)(sl + j * 1024);
        const float nm = (float)sq[0] * (-1.f / DM);
        const float var = fmaxf(__builtin_fmaf(-nm, nm, (float)sq[1] * (1.f / DM)), 0.f);
        const float rs = __builtin_amdgcn_rsqf(var + 1e-5f);
        rc[j >> 2][j & 3] = f32x2{rs, nm * rs};
    }
    asm volatile("" : "+v"(rc[0][0]), "+v"(rc[0][1]), "+v"(rc[0][2]), "+v"(rc[0][3]), "+v"(rc[1][0]), "+v"(rc[1][1]), "+v"(rc[1][2]), "+v"(rc[1][3]));
    wait_vm<2>();
    raw_barrier();
    bf16x8 kf0[8];
#pragma unroll
    for (int m = 0; m < 8; ++m) kf0[m] = tr_frag(L + a_k + 32 + 64 * (8 * m + 1), L + a_k + 64 * (8 * m + 1));
    wait_vm<0>();
    u32x2 xz[2][2];
#pragma unroll
    for (int k = 0; k < 2; ++k)
#pragma unroll
        for (int s = 0; s < 2; ++s) xz[k][s] = *(LAS u32x2*)(L + CV_X + k * 4096 + (p0 + 16 * s) * 2);
    ST(9);
    {
        LAS char* zw = L + CV_Z + c * CV_ZRS + 128 + 2 * (64 * tau + 2 * sp);
#pragma unroll
        for (int k = 0; k < 2; ++k)
#pragma unroll
            for (int g = 0; g < 4; ++g) {
                const float a = (g & 1) ? bfhi(xz[k][0][g >> 1]) : bflo(xz[k][0][g >> 1]);
                const float e = (g & 1) ? bfhi(xz[k][1][g >> 1]) : bflo(xz[k][1][g >> 1]);
                *(LAS u32*)(zw + k * 4 * CV_ZRS + 32 * g) =
                    pk_bf16(__builtin_fmaf(a, rc[0][g][0], rc[0][g][1]), __builtin_fmaf(e, rc[1][g][0], rc[1][g][1]));
            }
    }
    ST(2);
    asm volatile("s_waitcnt vmcnt(0) lgkmcnt(0)" ::: "memory");
    raw_barrier();
    ST(3);
    if (half) conv_main<1>(L, xout, xoff, a_z, a_k, a_cw, a_cr, a_x, kf0); else conv_main<0>(L, xout, xoff, a_z, a_k, a_cw, a_cr, a_x, kf0);
    ST(7);
    ST_FLUSH(layer == 0 ? 1 : 4);
}

#define G1_R 6
#define MAP2 1
#define ROT2 0
#define RAMP2 4
#define W2_START 2
#define W2_PER 0
#define W2_TAIL 6
constexpr int G1_NS = 16, G1_SLOT = 20480;
constexpr int G2_NS = 11, G2_R = 7, G2_SLOT = 16384, KH = NU / 2;
constexpr int ML_RED = 131072, ML_OS = ML_RED, ML_XS = ML_RED + 8192, ML_EX = ML_XS + 8192, ML_LDS = ML_EX + 16384;
constexpr int ML_CTR = ML_EX;

constexpr int G1_LEAD = G1_R - 2;
constexpr int g1_nis(int s) { int a = 2 * s + 2, b = s + G1_LEAD; a = a < b ? a : b; return a < G1_NS ? a : G1_NS; }
constexpr int g1_wlo(int s) {
    int a = s <= W2_START ? 0 : s <= G1_NS - G1_LEAD ? W2_PER * (s - W2_START)
                              : W2_PER * (G1_NS - G1_LEAD - W2_START) + W2_TAIL * (s - (G1_NS - G1_LEAD));
    return a < KH ? a : KH;
}
constexpr int g1_younger(int s) {
    int t = -1;
    while (g1_nis(t + 1) <= s) ++t;
    return 5 * (g1_nis(s) - 1 - s) + g1_wlo(s) - g1_wlo(t < 0 ? 0 : t);
}

extern "C" __global__ __launch_bounds__(512) void s4_mlp_kernel(u16* out) {
    char* ws = g_ws;
    extern __shared__ __attribute__((aligned(16))) char smem_raw[];
    LAS char* L = (LAS char*)smem_raw;
    const int layer = LAYER_, tid = TID_;
    const u16* xT = (const u16*)(ws + WS_X + (2 * layer + 1) * WS_XSTEP);
    const int lane = tid & 63, wave = __builtin_amdgcn_readfirstlane(tid >> 6);
    const int xcd = blockIdx.x & 7, sl = blockIdx.x >> 3;
    constexpr int MPX = 16 / TG1;
    const int mt = (xcd % TG1) * MPX + (sl % MPX), fg = (xcd / TG1) * (32 / MPX) + (sl / MPX);
    const int m2 = 2 * mt + (fg & 1), nt = fg >> 1;
    const int rot = ROT2 == 0 ? 0 : ROT2 == 1 ? (nt & 1) * 5 : MAP2 ? (nt & 3) * 5 >> 1 : nt + (nt >> 2);
    auto blk_unit = [&](int s, int kh) { return KH * kh + 4 * (s == G2_NS - 1 ? s : s + rot < G2_NS - 1 ? s + rot : s + rot - (G2_NS - 1)); };
    const u32 l0 = (u32)(uintptr_t)L;
    ST_DECL;
    const int g = lane >> 4, q = (lane & 15) >> 2;
    auto xfrag_off = [&](int tb) { return (8 * g + q) * 256 + ((tb ^ (q | ((g & 1) << 2))) << 5) + (lane & 3) * 8; };
    auto xsrc_off = [&](int odd) { return (u32)(g * 256 + ((((lane & 15) >> 1) ^ (g | (odd << 2))) << 5) + (lane & 1) * 16); };
    const char* xtile = (const char*)(xT + mt * XTILE);
    const int u0 = fg * 6;
    using I0 = std::integral_constant<int, 0>; using I1 = std::integral_constant<int, 1>;
    if (wave == 0) { *(LAS u32*)(L + ML_CTR) = 0u; wait_lgkm0(); }

    if (__builtin_expect(fg == 15, 0)) {
        const u32 mine = l0 + wave * 16384;
        const u32 so0 = xsrc_off(0), so1 = xsrc_off(1);
#pragma unroll
        for (int i = 0; i < 8; ++i) glds_s<2>(xtile + (16 * wave + 2 * i) * 1024, (i & 1) ? so1 : so0, mine + i * 2048);
        float S[8], Q[8];
        u32 a[8];
#pragma unroll
        for (int tb = 0; tb < 8; ++tb) { a[tb] = mine + xfrag_off(tb); S[tb] = 0.f; Q[tb] = 0.f; }
        static_for<4>([&](auto KI) {
            constexpr int kk = KI >> 1, tq = (KI & 1) * 4;
            if constexpr (tq == 0) wait_vm<8 - 8 * kk>();
            u32x2 f[8];
            asm volatile("ds_read_b64_tr_b16 %0, %8 offset:%c12\n\tds_read_b64_tr_b16 %1, %8 offset:%c13\n\t"
                         "ds_read_b64_tr_b16 %2, %9 offset:%c12\n\tds_read_b64_tr_b16 %3, %9 offset:%c13\n\t"
                         "ds_read_b64_tr_b16 %4, %10 offset:%c12\n\tds_read_b64_tr_b16 %5, %10 offset:%c13\n\t"
                         "ds_read_b64_tr_b16 %6, %11 offset:%c12\n\tds_read_b64_tr_b16 %7, %11 offset:%c13\n\ts_waitcnt lgkmcnt(0)"
                         : "=&v"(f[0]), "=&v"(f[1]), "=&v"(f[2]), "=&v"(f[3]), "=&v"(f[4]), "=&v"(f[5]), "=&v"(f[6]), "=&v"(f[7])
                         : "v"(a[tq]), "v"(a[tq + 1]), "v"(a[tq + 2]), "v"(a[tq + 3]), "i"(kk * 8192), "i"(kk * 8192 + 1024) : "memory");
#pragma unroll
            for (int j = 0; j < 4; ++j)
#pragma unroll
                for (int tb = 0; tb < 4; ++tb) {
                    const u32 w = f[2 * tb + (j >> 1)][j & 1];
                    S[tq + tb] = dot2(w, ONES, S[tq + tb]); Q[tq + tb] = dot2(w, w, Q[tq + tb]);
                }
        });
        LAS float* red = (LAS float*)(L + ML_RED);
#pragma unroll
        for (int tb = 0; tb < 8; ++tb) {
            const float v = row_pair_sum(half_sum2(S[tb], Q[tb]));
            if ((lane & 16) == 0) red[wave * 256 + (lane >> 5) * 128 + 16 * tb + (lane & 15)] = v;
        }
        wait_lgkm0();
        raw_barrier();
        if (tid < 128) {
            float s = 0.f, qq = 0.f;
#pragma unroll
            for (int w = 0; w < 8; ++w) { s += red[w * 256 + tid]; qq += red[w * 256 + 128 + tid]; }
            float mb, sb;
            ln_scalars(s, qq, mb, sb);
            __hip_atomic_store((u32*)(ws + WS_LNW) + layer * SEQ + mt * 128 + tid, pk_bf16(mb, sb), __ATOMIC_RELAXED, __HIP_MEMORY_SCOPE_AGENT);
        }
    }
    if (__builtin_expect(wave >= 4, 1)) {
        const int lw = wave - 4;
        const int wn = lw & 1, kh = lw >> 1, hh = lane >> 5;
        bf16x8 w2[KH];
        const rsrc_t WR = make_rsrc(ws + WS_W2 + (2 * nt + wn) * NU * 1024);
        auto ld_w2 = [&](auto JI) {
            constexpr int j = JI;
            w2[j] = __builtin_bit_cast(bf16x8, __builtin_amdgcn_raw_buffer_load_b128(WR, lane * 16 + (j & 3) * 1024, blk_unit(j >> 2, kh) * 1024, 0));
        };
        auto ld_res = [&]() {
            const char* xb = (const char*)(xT + mt * XTILE + (64 * nt + 32 * wn) * 128 + (m2 & 1) * 64 + 32 * kh);
            const u32 vo = (lane >> 2) * 256 + (((lane & 3) ^ ((lane >> 4) & 3)) << 4);
            glds_s<1>(xb, vo, l0 + ML_XS + lw * 2048);
            glds_s<1>(xb + 4096, vo, l0 + ML_XS + lw * 2048 + 1024);
        };
        if (__builtin_expect(fg != 15, 1)) {
            const u32 xo = xsrc_off(lw & 1), wo = lane * 16;
            const char* xs = xtile + 2 * lw * 1024;
            const int up = lw + (lw >> 1) + (lw & 1), us = 1 + 3 * (lw >> 1);
            const char* wp = ws + WS_W1 + (u0 + up) * 32 * 1024;
            const char* w1 = ws + WS_W1 + ((u0 + us) * 32 + (lw & 1)) * 1024;
            auto issue = [&](auto SI) {
                constexpr int s = SI;
                const u32 dst = l0 + (s % G1_R) * G1_SLOT;
                glds_s<2>(xs + s * 8192, xo, dst + 2 * lw * 1024);
                glds_s<2>(wp + s * 2048, wo, dst + 8192 + up * 2048);
                glds_s<1>(w1 + s * 2048, wo, dst + 8192 + us * 2048 + (lw & 1) * 1024);
            };
            issue(I0{}); issue(I1{});
            ST(1);
            static_for<G1_NS>([&](auto SI) {
                constexpr int s = SI, n = g1_nis(s), n2 = g1_nis(s + 1);
                wait_vm<g1_younger(s)>();
                raw_barrier();
                if constexpr (n2 > n) issue(std::integral_constant<int, n>{});
                if constexpr (n2 > n + 1) issue(std::integral_constant<int, n + 1>{});
                constexpr int lo = g1_wlo(s), hi = s == G1_NS - 1 ? KH : g1_wlo(s + 1);
                static_for<hi - lo>([&](auto JI) { ld_w2(std::integral_constant<int, lo + JI>{}); });
                if constexpr (s == G1_NS - G1_LEAD) ST(2);
            });
        } else {
            static_for<KH>([&](auto JI) { ld_w2(JI); });
        }
        ST(3);
        ld_res();
        wait_vm<0>();
        ST(4);
        const int item = pi_fwd(lane & 31), row = 32 * wn + item;
        f32x16 acc[2];
        {
            LAS char* xs = L + ML_XS + lw * 2048 + item * 64;
            const u32x4 a = *(LAS u32x4*)(xs + (((2 * hh) ^ ((item >> 2) & 3)) << 4));
            const u32x4 c = *(LAS u32x4*)(xs + (((2 * hh + 1) ^ ((item >> 2) & 3)) << 4));
#pragma unroll
            for (int p = 0; p < 4; ++p) {
                acc[0][2 * p] = bflo(a[p]); acc[0][2 * p + 1] = bfhi(a[p]);
                acc[0][8 + 2 * p] = bflo(c[p]); acc[0][9 + 2 * p] = bfhi(c[p]);
            }
#pragma unroll
            for (int r = 0; r < 16; ++r) acc[1][r] = 0.f;
            PIN16(acc[0]); PIN16(acc[1]);
        }
        const u32 a_ex = pinned(ML_EX + lw * 4096 + lane * 16);
        const u32 a_os = pinned(layer == 0 ? ML_OS + row * 128 + (((4 * kh + 2 * hh) ^ (row & 7)) << 4) : ML_OS + (32 * kh + 16 * hh) * 128 + row * 2);
        const int rr = 4 * (lane >> 4) + ((lane & 15) >> 2), spos = 64 * m2 + 16 * lw + (lane & 15);
        const u32 a_rp = pinned(ML_OS + rr * 128 + (((2 * lw + ((lane & 3) >> 1)) ^ (rr & 7)) << 4) + 8 * (lane & 1));
        const u32 a_st = pinned((lane & 16) ? 0x80000000u : 16 * st_slot(st_thread(spos), st_item(spos), 8) + 8 * hh);
        ST(7);
        raw_barrier();
        ST(5);
        LAS char* hk = L + kh * 8192 + kh * 4096 + lane * 16;
        LAS char* hs = L + (kh ^ 1) * 8192 + kh * 4096 + lane * 16;
        bf16x8 hf[2][4];
        auto rd2 = [&](auto SI, auto HI) {
            constexpr int s = SI, hlf = HI;
#pragma unroll
            for (int j = 0; j < 2; ++j)
                if (4 * s + 2 * hlf + j < KH) {
                    hf[hlf][2 * j] = *(LAS bf16x8*)(hk + (s % G2_R) * G2_SLOT + (2 * hlf + j) * 1024);
                    hf[hlf][2 * j + 1] = *(LAS bf16x8*)(hs + (s % G2_R) * G2_SLOT + (2 * hlf + j) * 1024);
                }
        };
        auto mm2 = [&](auto SI, auto HI) {
            constexpr int s = SI, hlf = HI;
#pragma unroll
            for (int j = 0; j < 2; ++j)
                if (4 * s + 2 * hlf + j < KH) {
                    acc[0] = MFMA32(hf[hlf][2 * j], w2[4 * s + 2 * hlf + j], acc[0]);
                    acc[1] = MFMA32(hf[hlf][2 * j + 1], w2[4 * s + 2 * hlf + j], acc[1]);
                }
        };
        auto mix = [&]() {
#pragma unroll
            for (int k = 0; k < 4; ++k) { __builtin_amdgcn_sched_group_barrier(0x8, 1, 0); __builtin_amdgcn_sched_group_barrier(0x100, 1, 0); }
            PIN16(acc[0]); PIN16(acc[1]);
            sched_fence();
        };
        static_for<G2_NS>([&](auto SI) {
            constexpr int s = SI;
            raw_barrier();
            rd2(SI, I0{});
            if constexpr (s > 0) mm2(std::integral_constant<int, s - 1>{}, I1{});
            mix();
            rd2(SI, I1{});
            mm2(SI, I0{});
            mix();
        });
        mm2(std::integral_constant<int, G2_NS - 1>{}, I1{});
        ST(8);
#pragma unroll
        for (int k = 0; k < 4; ++k) *(LAS f32x4*)(L + a_ex + 1024 * k) = f32x4{acc[1][4 * k], acc[1][4 * k + 1], acc[1][4 * k + 2], acc[1][4 * k + 3]};
        wait_lgkm0();
        raw_barrier();
        u32 o[8];
#pragma unroll
        for (int k = 0; k < 4; ++k) {
            const f32x4 v = *(LAS f32x4*)(L + (a_ex ^ 8192) + 1024 * k);
            o[2 * k] = pk_bf16(acc[0][4 * k] + v[0], acc[0][4 * k + 1] + v[1]);
            o[2 * k + 1] = pk_bf16(acc[0][4 * k + 2] + v[2], acc[0][4 * k + 3] + v[3]);
        }
        if (layer != 0) {
            LAS u16* ow = (LAS u16*)(L + a_os);
#pragma unroll
            for (int p = 0; p < 8; ++p) { ow[(2 * p) * 64] = (u16)o[p]; ow[(2 * p + 1) * 64] = (u16)(o[p] >> 16); }
            wait_lgkm0();
            raw_barrier();
            ST_FLUSH(5);
            return;
        }
        *(LAS u32x4*)(L + a_os) = u32x4{o[0], o[1], o[2], o[3]};
        *(LAS u32x4*)(L + (a_os ^ 16)) = u32x4{o[4], o[5], o[6], o[7]};
        wait_lgkm0();
        raw_barrier();
        ST(9);
        float S = 0.f, Q = 0.f, S2 = 0.f, Q2 = 0.f;
#pragma unroll
        for (int i = 0; i < 4; ++i) {
            const u32x2 v = __builtin_bit_cast(u32x2, __builtin_amdgcn_ds_read_tr16_b64_v4bf16((LAS bf16x4*)(L + a_rp + i * 2048)));
            S = dot2(v[0], ONES, S); Q = dot2(v[0], v[0], Q);
            S2 = dot2(v[1], ONES, S2); Q2 = dot2(v[1], v[1], Q2);
        }
        const float v = row_pair_sum(half_sum2(S + S2, Q + Q2));
        if (a_st != 0x80000000u) atomicAdd((double*)(ws + WS_ST64 + SEQ * 16 + a_st), (double)v);
        ST(10);
        ST_FLUSH(2);
        return;
    }
    if (fg != 15) {
        const int wm = wave & 1, wn = wave >> 1;
        f32x4 acc[6][4];
        bf16x8 xf[2][4], wf[2][6];
#pragma unroll
        for (int i = 0; i < 6; ++i)
#pragma unroll
            for (int t = 0; t < 4; ++t) acc[i][t] = f32x4{0.f, 0.f, 0.f, 0.f};
        int xo[4];
#pragma unroll
        for (int t = 0; t < 4; ++t) xo[t] = xfrag_off(4 * wm + t);
        const int wo = 8192 + 6 * wn * 1024 + lane * 16;
        int slot = 0;
        auto rd = [&](auto CI) {
            constexpr int c = CI;
            LAS char* base = L + slot * G1_SLOT;
            slot = slot == G1_R - 1 ? 0 : slot + 1;
            wf[c][0] = *(LAS bf16x8*)(base + wo);
#pragma unroll
            for (int t = 0; t < 4; ++t) xf[c][t] = tr_frag(base + xo[t], base + xo[t] + 1024);
#pragma unroll
            for (int i = 1; i < 6; ++i) wf[c][i] = *(LAS bf16x8*)(base + wo + i * 1024);
        };
        auto mm = [&](auto CI) {
            constexpr int c = CI;
#pragma unroll
            for (int i = 0; i < 6; ++i)
#pragma unroll
                for (int t = 0; t < 4; ++t) acc[i][t] = MFMA16(wf[c][i], xf[c][t], acc[i][t]);
        };
        auto step = [&](auto CI) {
            constexpr int c = CI;
            raw_barrier();
            rd(std::integral_constant<int, c>{});
            mm(std::integral_constant<int, c ^ 1>{});
#pragma unroll
            for (int k = 0; k < 14; ++k) { __builtin_amdgcn_sched_group_barrier(0x8, 1, 0); __builtin_amdgcn_sched_group_barrier(0x100, 1, 0); }
            PIN16(acc[5][3]);
            sched_fence();
        };
        float cp[4];
#pragma unroll
        for (int k = 0; k < 4; ++k) cp[k] = ((const float*)(ws + WS_CSP))[(k * 90 + u0) * 32 + (tid < 192 ? tid : 0)];
        ST(1);
        raw_barrier();
        rd(I0{});
        sched_fence();
        ST(2);
#pragma unroll 1
        for (int s = 1; s < G1_NS - 3; s += 2) { step(I1{}); step(I0{}); }
        *(LAS float*)(L + ML_RED + tid * 4) = (cp[0] + cp[1]) + (cp[2] + cp[3]);
        wait_lgkm0();
        u32 st[4];
        f32x4 cs[6];
        u32* stp = (u32*)(ws + WS_LNW) + layer * SEQ + mt * 128 + 64 * wm + (lane & 15);
#pragma unroll
        for (int t = 0; t < 4; ++t) st[t] = __hip_atomic_load(stp + 16 * t, __ATOMIC_RELAXED, __HIP_MEMORY_SCOPE_AGENT);
        sched_fence();
        step(I1{}); step(I0{}); step(I1{});
#pragma unroll
        for (int i = 0; i < 6; ++i) cs[i] = *(LAS f32x4*)(L + ML_RED + (6 * wn + i) * 64 + g * 16);
        for (int spin = 0; spin < (1 << 22) && !__all(st[0] != 0u && st[1] != 0u && st[2] != 0u && st[3] != 0u); ++spin) {
            __builtin_amdgcn_s_sleep(2);
#pragma unroll
            for (int t = 0; t < 4; ++t) st[t] = __hip_atomic_load(stp + 16 * t, __ATOMIC_RELAXED, __HIP_MEMORY_SCOPE_AGENT);
        }
        ST(3);
        const rsrc_t HR = make_rsrc(ws + WS_H + (1 - layer) * WS_HSTEP + ((4 * mt + 2 * wm) * NU + u0 + 3 * wn) * 1024);
        const u32 hoff = (g >> 1) * 512 + pi_inv(16 * (g & 1) + (lane & 15)) * 16;
        f32x2 nm2[4], sb2[4], ce2[4];
#pragma unroll
        for (int t = 0; t < 4; ++t) {
            const float sb = bfhi(st[t]), nm = -bflo(st[t]), ce = -1.44269504088896341f * __builtin_amdgcn_rcpf(sb);
            nm2[t] = f32x2{nm, nm}; sb2[t] = f32x2{sb, sb}; ce2[t] = f32x2{ce, ce};
        }
        auto mmg = [&](auto GI) {
            constexpr int tp = GI / 3, i = GI % 3;
#pragma unroll
            for (int k = 0; k < 4; ++k) acc[2 * i + (k >> 1)][2 * tp + (k & 1)] = MFMA16(wf[1][2 * i + (k >> 1)], xf[1][2 * tp + (k & 1)], acc[2 * i + (k >> 1)][2 * tp + (k & 1)]);
        };
        auto pair = [](const f32x4& v, int j) { return f32x2{v[2 * j], v[2 * j + 1]}; };
        mmg(I0{}); mmg(I1{});
        sched_fence();
        static_for<6>([&](auto GI) {
            constexpr int tp = GI / 3, i = GI % 3;
            f32x2 a[4], u[4];
#pragma unroll
            for (int k = 0; k < 4; ++k) a[k] = __builtin_elementwise_fma(pair(cs[2 * i], k & 1), nm2[2 * tp + (k >> 1)], pair(acc[2 * i][2 * tp + (k >> 1)], k & 1));
#pragma unroll
            for (int k = 0; k < 4; ++k) { a[k] *= ce2[2 * tp + (k >> 1)]; asm volatile("" : "+v"(a[k])); }
            sched_fence();
            if constexpr (GI + 2 < 6) mmg(std::integral_constant<int, GI + 2>{});
#pragma unroll
            for (int k = 0; k < 4; ++k) { a[k] = f32x2{__builtin_amdgcn_exp2f(a[k][0]), __builtin_amdgcn_exp2f(a[k][1])}; asm volatile("" : "+v"(a[k])); }
#pragma unroll
            for (int k = 0; k < 4; ++k) { __builtin_amdgcn_sched_group_barrier(0x8, 1, 0); __builtin_amdgcn_sched_group_barrier(0x402, 2, 0); }
            if constexpr (GI + 2 < 6) { constexpr int t2 = (GI + 2) / 3, i2 = (GI + 2) % 3; PIN16(acc[2 * i2 + 1][2 * t2 + 1]); }
            sched_fence();
#pragma unroll
            for (int k = 0; k < 4; ++k) { a[k] = __builtin_elementwise_fma(a[k], sb2[2 * tp + (k >> 1)], sb2[2 * tp + (k >> 1)]); asm volatile("" : "+v"(a[k])); }
#pragma unroll
            for (int k = 0; k < 4; ++k) {
                u[k] = __builtin_elementwise_fma(pair(cs[2 * i + 1], k & 1), nm2[2 * tp + (k >> 1)], pair(acc[2 * i + 1][2 * tp + (k >> 1)], k & 1));
                asm volatile("" : "+v"(u[k]));
            }
            sched_fence();
#pragma unroll
            for (int k = 0; k < 4; ++k) { a[k] = f32x2{__builtin_amdgcn_rcpf(a[k][0]), __builtin_amdgcn_rcpf(a[k][1])}; asm volatile("" : "+v"(a[k])); }
            sched_fence();
#pragma unroll
            for (int k = 0; k < 4; ++k) { u[k] *= a[k]; asm volatile("" : "+v"(u[k])); }
            u32 o[4];
#pragma unroll
            for (int k = 0; k < 4; ++k) o[k] = pk_bf16(u[k]);
            const auto r0 = __builtin_amdgcn_permlane16_swap(o[0], o[2], false, false);
            const auto r1 = __builtin_amdgcn_permlane16_swap(o[1], o[3], false, false);
            st16(HR, hoff + i * 1024, (u0 + 3 * wn + i < NU ? 0u : 0x80000000u) + tp * NU * 1024, u32x4{r0[0], r1[0], r0[1], r1[1]});
        });
        ST(5);
    }
    const char* hF = ws + WS_H + (1 - layer) * WS_HSTEP;
    wait_vm<0>();
    ST(6);
    u32 order = 0;
    if (lane == 0) order = __hip_atomic_fetch_add((LAS u32*)(L + ML_CTR), 1u, __ATOMIC_RELAXED, __HIP_MEMORY_SCOPE_WORKGROUP);
    if (__builtin_amdgcn_readfirstlane(order) == 3u) {
        u32* cnt = (u32*)(ws + WS_CNT) + (layer * 16 + mt) * 512;
        u32 v = 0;
        if (fg == 15) __builtin_amdgcn_s_sleep(100);
        else if (lane < 16) v = __hip_atomic_fetch_add(cnt + lane * 32, 1u, __ATOMIC_RELAXED, __HIP_MEMORY_SCOPE_AGENT) + 1u;
        v = __builtin_amdgcn_readfirstlane(v);
#pragma unroll 1
        for (int spin = 0; spin < (1 << 22) && v < 15u; ++spin)
            v = __builtin_amdgcn_readfirstlane(__hip_atomic_load(cnt + fg * 32, __ATOMIC_RELAXED, __HIP_MEMORY_SCOPE_AGENT));
    }
    raw_barrier();
    ST(7);
    {
        const char* src = hF + (2 * m2 + (wave >> 1)) * NU * 1024;
        const u32 vo = lane * 16, dst = l0 + wave * 4096;
        auto issue = [&](auto SI) {
            constexpr int s = SI;
            glds_s<(s < G2_NS - 1 ? 4 : 3), true>(src + blk_unit(s, wave & 1) * 1024, vo, dst + (s % G2_R) * G2_SLOT);
        };
        constexpr int LEAD = G2_R - 2;
        auto nissued = [](int s) { int a = 2 * s + RAMP2, b = s + LEAD + (s < 2 ? 2 - s : 0); a = a < b ? a : b; return a < G2_NS ? a : G2_NS; };
        static_for<RAMP2>([&](auto SI) { issue(SI); });
        static_for<G2_NS>([&](auto SI) {
            constexpr int s = SI, n = nissued(s), n2 = nissued(s + 1);
            constexpr int fly = n - 1 - s;
            wait_vm<(fly > 0 ? 4 * fly - (n == G2_NS ? 1 : 0) : 0)>();
            if constexpr (s == 0) ST(9);
            if constexpr (s == 2) ST(10);
            if constexpr (s == 5) ST(11);
            if constexpr (s == 8) ST(12);
            raw_barrier();
            if constexpr (n2 > n) issue(std::integral_constant<int, n>{});
            if constexpr (n2 > n + 1) issue(std::integral_constant<int, n + 1>{});
        });
    }
    ST(8);
    {
        const rsrc_t xout = make_rsrc(layer ? (char*)out : ws + WS_X + 2 * WS_XSTEP);
        const int pos = 64 * m2 + 16 * wave + (lane >> 3);
        const int tok = (pos & ~255) | ((pos & 3) << 6) | ((pos >> 2) & 63);
        const u32 vo = pinned(layer ? tok * (DM * 2) + 128 * nt + ((lane & 7) << 4)
                                    : 2 * (mt * XTILE + (64 * nt + 16 * wave + (lane >> 3)) * 128 + (m2 & 1) * 64) + (((lane & 7) ^ (lane >> 3)) << 4));
        const u32 a_rd = pinned(ML_OS + wave * 2048 + lane * 16);
        raw_barrier();
        raw_barrier();
        st16(xout, vo, 0, *(LAS u32x4*)(L + a_rd));
        st16(xout, vo, 2048, *(LAS u32x4*)(L + a_rd + 1024));
    }
    ST_FLUSH(layer ? 5 : 2);
}

extern "C" __global__ void s4_tail_kernel() {}

extern "C" void kernel_launch(void* const* d_in, const int* in_sizes, int n_in, void* d_out, int out_size,
                              void* d_ws, size_t ws_size, hipStream_t stream) {
    (void)in_sizes; (void)n_in; (void)out_size; (void)ws_size;
    const u16* x0 = (const u16*)d_in[0];
    const u16* logA = (const u16*)d_in[1];
    const u16* Aim = (const u16*)d_in[2];
    const u16* Cre = (const u16*)d_in[3];
    const u16* Cim = (const u16*)d_in[4];
    const u16* logdt = (const u16*)d_in[5];
    const u16* Dsk = (const u16*)d_in[6];
    const u16* Wg = (const u16*)d_in[8];
    const u16* Wu = (const u16*)d_in[9];
    const u16* Wd = (const u16*)d_in[10];
    (void)d_ws;
    (void)hipFuncSetAttribute((const void*)s4_conv_kernel, hipFuncAttributeMaxDynamicSharedMemorySize, CV_LDS);
    (void)hipFuncSetAttribute((const void*)s4_mlp_kernel, hipFuncAttributeMaxDynamicSharedMemorySize, ML_LDS);

    s4_prep_kernel<<<PREP_BLOCKS, 256, PREP_LDS, stream>>>(x0, logA, Aim, Cre, Cim, logdt, Dsk);
    for (int layer = 0; layer < 2; ++layer) {
        s4_conv_kernel<<<layer ? CV_GRID : 256 + RT_W1 + RT_W2, layer ? dim3(1, 256) : dim3(256), CV_LDS, stream>>>(Wg, Wu, Wd);
        s4_mlp_kernel<<<256, layer ? dim3(1, 512) : dim3(512), ML_LDS, stream>>>((u16*)d_out);
    }
}
```

```cpp
#include <hip/hip_runtime.h>
#include <hip/hip_bf16.h>
#include <stdint.h>
#include <utility>

typedef unsigned short u16;
typedef unsigned int u32;
typedef __bf16 bf16x8 __attribute__((ext_vector_type(8)));
typedef __bf16 bf16x4 __attribute__((ext_vector_type(4)));
typedef __bf16 bf16x2 __attribute__((ext_vector_type(2)));
typedef _Float16 f16x8 __attribute__((ext_vector_type(8)));
typedef _Float16 f16x2 __attribute__((ext_vector_type(2)));
typedef float f32x16 __attribute__((ext_vector_type(16)));
typedef float f32x4 __attribute__((ext_vector_type(4)));
typedef float f32x2 __attribute__((ext_vector_type(2)));
typedef double f64x2 __attribute__((ext_vector_type(2)));
typedef u32 u32x4 __attribute__((ext_vector_type(4)));
typedef u32 u32x2 __attribute__((ext_vector_type(2)));

#define LAS __attribute__((address_space(3)))
#define GAS __attribute__((address_space(1)))
#define DI __device__ __forceinline__

constexpr int SEQ = 2048, DM = 512, NST = 128, FF = 1376, NU = FF / 16;
constexpr int XTILE = DM * 128;

constexpr size_t MB = 1 << 20;
constexpr size_t WS_X = 0, WS_XSTEP = 2 * MB;
constexpr size_t WS_K = 8 * MB, WS_W1 = 10 * MB, WS_W2 = 13 * MB, WS_MISC = 15 * MB - 262144, WS_H = 15 * MB, WS_HSTEP = 6 * MB;
constexpr size_t WS_BYTES = 27 * MB;
constexpr size_t WS_CSP = WS_MISC;
constexpr size_t WS_ST64 = WS_MISC + 65536;
constexpr size_t WS_ZERO = WS_ST64 + SEQ * 16;
constexpr size_t WS_LNW = WS_ZERO + SEQ * 16, WS_CNT = WS_LNW + 2 * SEQ * 4;
constexpr int ZERO_BYTES = SEQ * 16 + 2 * SEQ * 4 + 512 * 128;
__device__ __attribute__((aligned(4096))) char g_ws[WS_BYTES];

#define TG1 4

#define ST_DECL do {} while (0)
#define ST_DECLN do {} while (0)
#define STN(i) do {} while (0)
#define ST(i) do {} while (0)
#define ST_FLUSH(kid) do {} while (0)

#define TID_ (threadIdx.x + threadIdx.y)
#define LAYER_ (__builtin_amdgcn_readlane(threadIdx.y, 1) != 0u ? 1 : 0)

DI float bf2f(u16 h) { return __uint_as_float((u32)h << 16); }
DI float bflo(u32 w) { return __uint_as_float(w << 16); }
DI float bfhi(u32 w) { return __uint_as_float(w & 0xffff0000u); }
DI u32 pk_bf16(f32x2 v) { return __builtin_bit_cast(u32, __builtin_convertvector(v, bf16x2)); }
DI u32 pk_bf16(float lo, float hi) { return pk_bf16(f32x2{lo, hi}); }
DI float rbf(float x) { return bflo(pk_bf16(x, x)); }
DI u32 pk_f16(f32x2 v) { return __builtin_bit_cast(u32, __builtin_convertvector(v, f16x2)); }
DI float dot2(u32 a, u32 b, float c) { return __builtin_amdgcn_fdot2_f32_bf16(__builtin_bit_cast(bf16x2, a), __builtin_bit_cast(bf16x2, b), c, false); }
DI u32 perm_lo(u32 hi, u32 lo) { return __builtin_amdgcn_perm(hi, lo, 0x05040100u); }
DI u32 perm_hi(u32 hi, u32 lo) { return __builtin_amdgcn_perm(hi, lo, 0x07060302u); }
DI int pi_fwd(int r) { return (((r >> 2) & 1) << 4) | ((r >> 3) << 2) | (r & 3); }
DI int pi_inv(int p) { return (((p >> 2) & 3) << 3) | ((p >> 4) << 2) | (p & 3); }
DI int xt_off(int ch, int pos) { return (pos >> 7) * XTILE + ch * 128 + (pos & 127); }
DI int st_thread(int pos) { return ((pos >> 8) << 5) | (((pos >> 5) & 7) << 2) | ((pos >> 2) & 3); }
DI int st_item(int pos) { return (((pos >> 4) & 1) << 2) | (pos & 3); }
DI int st_slot(int thread, int item, int nitem) { return ((thread >> 6) * nitem + item) * 64 + (thread & 63); }
constexpr u32 ONES = 0x3f803f80u;

template <int N> DI void wait_vm() { asm volatile("s_waitcnt vmcnt(%0)" ::"n"(N) : "memory"); }
DI void wait_lgkm0() { asm volatile("s_waitcnt lgkmcnt(0)" ::: "memory"); }
DI void raw_barrier() { asm volatile("" ::: "memory"); __builtin_amdgcn_s_barrier(); asm volatile("" ::: "memory"); }
DI float opaque(float v) { asm("" : "+v"(v)); return v; }
DI u32 pinned(u32 v) { asm volatile("" : "+v"(v)); return v; }
#define PIN16(x) asm volatile("" : "+v"(x))
DI void sched_fence() { __builtin_amdgcn_sched_barrier(0); }
DI void glds16(const void* src, LAS char* dst) {
    __builtin_amdgcn_global_load_lds((const GAS void*)src, (LAS void*)dst, 16, 0, 0);
}
DI bf16x8 tr_frag(LAS char* a0, LAS char* a1) {
    bf16x4 lo = __builtin_amdgcn_ds_read_tr16_b64_v4bf16((LAS bf16x4*)a0);
    bf16x4 hi = __builtin_amdgcn_ds_read_tr16_b64_v4bf16((LAS bf16x4*)a1);
    return __builtin_shufflevector(lo, hi, 0, 1, 2, 3, 4, 5, 6, 7);
}
#define GLDS_HEAD "s_mov_b32 %0, m0\n\ts_mov_b32 m0, %3\n\ts_nop 0\n\t"
#define GLDS_ONE(off, sc) "global_load_lds_dwordx4 %1, %2 offset:" #off sc "\n\t"
#define GLDS_TAIL "s_mov_b32 m0, %0" : "=&s"(keep) : "v"(voff), "s"(ubase), "s"(lds_dst) : "memory"
#define GLDS_BODY(sc)                                                                                                          \
    if constexpr (N == 1) asm volatile(GLDS_HEAD GLDS_ONE(0, sc) GLDS_TAIL);                                                   \
    else if constexpr (N == 2) asm volatile(GLDS_HEAD GLDS_ONE(0, sc) GLDS_ONE(1024, sc) GLDS_TAIL);                           \
    else if constexpr (N == 3) asm volatile(GLDS_HEAD GLDS_ONE(0, sc) GLDS_ONE(1024, sc) GLDS_ONE(2048, sc) GLDS_TAIL);        \
    else asm volatile(GLDS_HEAD GLDS_ONE(0, sc) GLDS_ONE(1024, sc) GLDS_ONE(2048, sc) GLDS_ONE(3072, sc) GLDS_TAIL)
template <int N, bool DEV = false> DI void glds_s(const char* ubase, u32 voff, u32 lds_dst) {
    u32 keep;
    lds_dst = __builtin_amdgcn_readfirstlane(lds_dst);
    if constexpr (DEV) { GLDS_BODY(" sc1"); } else { GLDS_BODY(""); }
}
typedef __amdgpu_buffer_rsrc_t rsrc_t;
DI rsrc_t make_rsrc(void* base) { return __builtin_amdgcn_make_buffer_rsrc(base, 0, 0x7fffffff, 0x00020000); }
template <int AUX = 16> DI void st16(rsrc_t r, u32 voff, u32 soff, u32x4 v) { __builtin_amdgcn_raw_buffer_store_b128(v, r, voff + soff, 0, AUX); }
template <int AUX = 16> DI void st8(rsrc_t r, u32 voff, u32 soff, u32x2 v) { __builtin_amdgcn_raw_buffer_store_b64(v, r, voff, soff, AUX); }
#define H_AUX 16
#define MFMA16(a, b, c) __builtin_amdgcn_mfma_f32_16x16x32_bf16((a), (b), (c), 0, 0, 0)
#define MFMA32(a, b, c) __builtin_amdgcn_mfma_f32_32x32x16_bf16((a), (b), (c), 0, 0, 0)

template <class F, int... I> DI void static_for_impl(F&& f, std::integer_sequence<int, I...>) { (f(std::integral_constant<int, I>{}), ...); }
template <int N, class F> DI void static_for(F&& f) { static_for_impl(f, std::make_integer_sequence<int, N>{}); }

template <int CTRL, int RM = 0xf> DI float dppz(float v) { return __int_as_float(__builtin_amdgcn_update_dpp(0, __float_as_int(v), CTRL, RM, 0xf, true)); }
DI float wave_sum63(float v) {
    v += dppz<0x111>(v); v += dppz<0x112>(v); v += dppz<0x114>(v); v += dppz<0x118>(v);
    v += dppz<0x142, 0xa>(v); v += dppz<0x143, 0xc>(v);
    return v;
}
DI float wave_max63(float v) {
    v = fmaxf(v, dppz<0x111>(v)); v = fmaxf(v, dppz<0x112>(v)); v = fmaxf(v, dppz<0x114>(v)); v = fmaxf(v, dppz<0x118>(v));
    v = fmaxf(v, dppz<0x142, 0xa>(v)); v = fmaxf(v, dppz<0x143, 0xc>(v));
    return v;
}
DI float rdlane63(float v) { return __int_as_float(__builtin_amdgcn_readlane(__float_as_int(v), 63)); }
DI float half_sum2(float a, float b) {
    auto r = __builtin_amdgcn_permlane32_swap(__float_as_uint(a), __float_as_uint(b), false, false);
    return __uint_as_float(r[0]) + __uint_as_float(r[1]);
}
DI float row_pair_sum(float a) {
    auto r = __builtin_amdgcn_permlane16_swap(__float_as_uint(a), __float_as_uint(a), false, false);
    return __uint_as_float(r[0]) + __uint_as_float(r[1]);
}
DI void ln_scalars(float S, float Q, float& mb, float& sb) {
    const float mean = S * (1.f / DM);
    const float var = fmaxf(__builtin_fmaf(-mean, mean, Q * (1.f / DM)), 0.f);
    const u32 mv = pk_bf16(mean, var);
    mb = bflo(mv);
    sb = rbf(__builtin_amdgcn_sqrtf(rbf(bfhi(mv) + 1e-5f)));
}

struct cm { f32x2 b, r; };
DI cm mk(f32x2 b) { return {b, f32x2{-b[1], b[0]}}; }
DI f32x2 cmul(f32x2 a, const cm& m) {
    return __builtin_elementwise_fma(__builtin_shufflevector(a, a, 1, 1), m.r, __builtin_shufflevector(a, a, 0, 0) * m.b);
}
DI f32x2 cexp_p(float arl, float aiv, float p) {
    const float mag = __builtin_amdgcn_exp2f(arl * p);
    const float th = __builtin_amdgcn_fractf(aiv * p);
    return f32x2{mag * __builtin_amdgcn_cosf(th), mag * __builtin_amdgcn_sinf(th)};
}
DI float exp_bf(float x) {
    const float t = x * 1.44269504088896341f;
    const float r = __builtin_fmaf(x, 1.44269504088896341f, -t) + x * 1.92596299e-8f;
    const float e = __builtin_amdgcn_exp2f(t);
    return rbf(__builtin_fmaf(e * r, 0.693147181f, e));
}

constexpr int KG_ROW = 528;
constexpr int KG_TROW = 80;
constexpr int KG_B = 21 * KG_ROW, KG_TAB = 53 * KG_ROW, PREP_LDS = KG_TAB + 32 * KG_TROW;
constexpr int PB_WARM = 16, PB_KGEN = 512, PB_TR = 128, PB_ST = 128;
constexpr int PREP_BLOCKS = PB_WARM + PB_KGEN + PB_TR + PB_ST;

extern "C" __global__ __launch_bounds__(256, 2) void s4_prep_kernel(
    const u16* __restrict__ x0, const u16* __restrict__ logA, const u16* __restrict__ Aim,
    const u16* __restrict__ Cre, const u16* __restrict__ Cim, const u16* __restrict__ logdt, const u16* __restrict__ Dskip) {
    char* ws = g_ws;
    extern __shared__ __attribute__((aligned(16))) char smem_raw[];
    LAS char* L = (LAS char*)smem_raw;
    {
        unsigned long long pc;
        u32 off = (((blockIdx.x >> 3) << 8) + threadIdx.x) * 128u;
        asm volatile("s_cmp_ge_u32 %2, %3\n\ts_cbranch_scc1 2f\n\ts_getpc_b64 %0\n1:\n\t"
                     "v_min_u32 %1, s4_tail_kernel-1b, %1\n\tglobal_load_dword %1, %1, %0\n\ts_waitcnt vmcnt(0)\n\ts_endpgm\n2:"
                     : "=&s"(pc), "+v"(off) : "s"(blockIdx.x), "n"(PB_WARM) : "scc", "memory");
    }
    asm volatile("" ::"s"(x0), "s"(logA), "s"(Aim), "s"(Cre), "s"(Cim), "s"(logdt), "s"(Dskip));
    const rsrc_t WS = make_rsrc(ws);
    const int tid = threadIdx.x, lane = tid & 63;
    const int wave = __builtin_amdgcn_readfirstlane(tid >> 6);
    int b = blockIdx.x - PB_WARM;
    ST_DECL;
    if (__builtin_expect(b >= PB_KGEN, 0)) {
        b -= PB_KGEN;
        if (b < PB_TR) {
            const int w = b * 4 + wave, tau = w >> 6, lg = (w >> 4) & 3, cgg = w & 15;
            const int ln = lg * 16 + (lane & 15), cg = cgg * 4 + (lane >> 4);
            u32x4 in[4];
#pragma unroll
            for (int g = 0; g < 4; ++g) in[g] = *(const u32x4*)(x0 + (256 * tau + 64 * g + ln) * DM + 8 * cg);
            const u32 dst = 2 * xt_off(8 * cg, 256 * tau + 4 * ln);
#pragma unroll
            for (int q = 0; q < 4; ++q) {
                st8(WS, dst + (2 * q) * 256, WS_X, u32x2{perm_lo(in[1][q], in[0][q]), perm_lo(in[3][q], in[2][q])});
                st8(WS, dst + (2 * q + 1) * 256, WS_X, u32x2{perm_hi(in[1][q], in[0][q]), perm_hi(in[3][q], in[2][q])});
            }
            ST_FLUSH(0);
            return;
        }
        b -= PB_TR;
        {
            const int w = b * 4 + wave, tau = w >> 6, ln = w & 63;
            u32x4 in[4];
#pragma unroll
            for (int g = 0; g < 4; ++g) in[g] = *(const u32x4*)(x0 + (256 * tau + 64 * g + ln) * DM + 8 * lane);
            if ((b * 256 + tid) * 16 < ZERO_BYTES) st16(WS, (b * 256 + tid) * 16, WS_ZERO, u32x4{0u, 0u, 0u, 0u});
            if (b >= 96) st16(WS, ((b - 96) * 256 + tid) * 16, WS_W1 + NU * 32 * 1024, u32x4{0u, 0u, 0u, 0u});
#pragma unroll
            for (int g = 0; g < 4; ++g) {
                float S = 0.f, Q = 0.f;
#pragma unroll
                for (int q = 0; q < 4; ++q) { S = dot2(in[g][q], ONES, S); Q = dot2(in[g][q], in[g][q], Q); }
                S = wave_sum63(S); Q = wave_sum63(Q);
                const int pos = 256 * tau + 4 * ln + g;
                const u32 dst = lane == 63 ? 16 * st_slot(st_thread(pos), st_item(pos), 8) : 0x80000000u;
                const u32x4 v = __builtin_bit_cast(u32x4, f64x2{(double)S, (double)Q});
                st16(WS, dst, WS_ST64, v);
            }
            ST_FLUSH(0);
            return;
        }
    }
    {
        const int d = b, n = tid & 127;
        u32 h_dt = logdt[d], h_D = Dskip[d], h_la = logA[d * NST + n], h_ai = Aim[d * NST + n];
        u32 h_cr = Cre[d * NST + n], h_ci = Cim[d * NST + n];
        u32 ca = ((const u32*)(Cre + d * NST))[lane], cc = ((const u32*)(Cim + d * NST))[lane];
        const int rb = wave & 1, cb = wave >> 1, q = lane >> 4;
        const u32 a_pa = pinned((16 * rb + (lane & 15)) * KG_ROW + q * 16);
        const u32 a_pb = pinned(KG_B + (16 * cb + (lane & 15)) * KG_ROW + q * 16);
        const u32 a_tw = pinned(KG_TAB + (16 * cb + (lane & 15)) * KG_TROW + 32 * rb + 8 * q);
        const int S = tid >> 5, col = tid & 31;
        auto row0 = [](int sp) { return min(min(8 * sp, 8 + 2 * sp), 12 + sp); };
        const u32 a_t0 = pinned(KG_TAB + col * KG_TROW);
        const u32 a_ta = pinned(a_t0 + 2 * (row0(S) & ~1));
        const u32 a_tb = pinned(a_ta + (S == 1 ? 4 : 0));
        const u32 a_tn = pinned(KG_TAB + ((tid + 1) & 31) * KG_TROW + 2 * row0((tid + 1) >> 5));
        const u32 t0s = (S >= 4 && (S & 1)) ? 0x0302u : 0x0100u;
        const u32 s0 = pinned(t0s * 0x10001u);
        const u32 s1 = pinned(S == 1 ? 0x05040302u : S < 4 ? 0x07060100u : s0);
        const u32 s2 = pinned(S == 1 ? 0x01000100u : S < 4 ? 0x03020302u : (tid & 1) ? 0x05040504u : 0x05040000u | t0s);
        const u32 s3 = pinned(S == 1 ? 0x05040302u : 0x05040504u);
        asm volatile("" : "+v"(h_dt), "+v"(h_D), "+v"(h_la), "+v"(h_ai), "+v"(h_cr), "+v"(h_ci), "+v"(ca), "+v"(cc));
        const float dt = exp_bf(bflo(h_dt));
        const float arl = -exp_bf(bflo(h_la)) * dt * 1.44269504088896341f;
        float aiv = bflo(h_ai) * dt * 0.159154943091895336f;
        const float mx = wave_max63(fmaxf(fmaxf(fabsf(bflo(ca)), fabsf(bfhi(ca))), fmaxf(fabsf(bflo(cc)), fabsf(bfhi(cc)))));
        const u32 eb = min(max((u32)__builtin_amdgcn_readlane(__float_as_int(mx), 63) & 0x7f800000u, 32u << 23), 224u << 23);
        const float sc = __uint_as_float((261u << 23) - eb);
        const float back = dt * (__uint_as_float(eb) * (2.f / 32768.f));
        ST(1);
        if (wave < 2) {
            aiv = -aiv;
            const f32x2 t0 = {bflo(h_cr) * sc, -bflo(h_ci) * sc};
            const cm w1 = mk(cexp_p(arl, aiv, 1.f)), v1 = mk(cexp_p(arl, aiv, 256.f));
            const cm w2 = mk(cmul(w1.b, w1)), w4 = mk(cmul(w2.b, w2)), v2 = mk(cmul(v1.b, v1)), v4 = mk(cmul(v2.b, v2));
            LAS u32* A = (LAS u32*)(L + 4 * n);
            auto put = [&](int row, f32x2 z) { A[row * (KG_ROW / 4)] = pk_f16(z); };
            const f32x2 r1 = cmul(t0, w1), r2 = cmul(t0, w2), r3 = cmul(r1, w2);
            const f32x2 t1 = cmul(t0, v1), t2 = cmul(t0, v2), t3 = cmul(t1, v2), t4 = cmul(t0, v4), r9 = cmul(t1, w2);
            put(0, t0); put(1, r1); put(2, r2); put(3, r3);
            put(4, cmul(t0, w4)); put(5, cmul(r1, w4)); put(6, cmul(r2, w4)); put(7, cmul(r3, w4));
            put(8, t1); put(9, r9); put(10, cmul(t1, w4)); put(11, cmul(r9, w4));
            put(12, t2); put(13, cmul(t2, w4)); put(14, t3); put(15, cmul(t3, w4));
            put(16, t4); put(17, cmul(t1, v4)); put(18, cmul(t2, v4)); put(19, cmul(t3, v4)); put(20, cmul(t4, v4));
        } else {
            f32x2 e[16];
            const f32x2 b1 = cexp_p(arl, aiv, 8.f);
            const cm b8 = mk(cexp_p(arl, aiv, 64.f)), b2 = mk(cmul(b1, mk(b1))), b4 = mk(cmul(b2.b, b2)), b16 = mk(cmul(b8.b, b8));
            LAS u32* B = (LAS u32*)(L + KG_B + 4 * n);
            e[0] = f32x2{256.f, 0.f}; e[1] = b1 * 256.f; e[2] = b2.b * 256.f; e[3] = cmul(e[1], b2);
            e[4] = b4.b * 256.f; e[8] = b8.b * 256.f;
#pragma unroll
            for (int c = 1; c < 4; ++c) e[4 + c] = cmul(e[c], b4);
#pragma unroll
            for (int c = 1; c < 8; ++c) e[8 + c] = cmul(e[c], b8);
#pragma unroll
            for (int c = 0; c < 16; ++c) {
                B[c * (KG_ROW / 4)] = pk_f16(e[c]);
                B[(16 + c) * (KG_ROW / 4)] = pk_f16(c ? cmul(e[c], b16) : b16.b * 256.f);
            }
        }
        ST(2);
        __syncthreads();
        ST(3);
        {
            f16x8 fa[8], fb[8];
#pragma unroll
            for (int ks = 0; ks < 8; ++ks) { fa[ks] = *(LAS f16x8*)(L + a_pa + 64 * ks); fb[ks] = *(LAS f16x8*)(L + a_pb + 64 * ks); }
            sched_fence();
            f32x4 acc = {0.f, 0.f, 0.f, 0.f}, acc2 = {0.f, 0.f, 0.f, 0.f};
#pragma unroll
            for (int ks = 0; ks < 8; ks += 2) {
                acc = __builtin_amdgcn_mfma_f32_16x16x32_f16(fa[ks], fb[ks], acc, 0, 0, 0);
                acc2 = __builtin_amdgcn_mfma_f32_16x16x32_f16(fa[ks + 1], fb[ks + 1], acc2, 0, 0, 0);
            }
            acc += acc2;
            const float add0 = tid == 0 ? bflo(h_D) : 0.f;
            *(LAS u32x2*)(L + a_tw) = u32x2{pk_bf16(__builtin_fmaf(acc[0], back, add0), acc[1] * back), pk_bf16(acc[2] * back, acc[3] * back)};
        }
        ST(4);
        __syncthreads();
        ST(5);
        {
            const u32x4 w = *(LAS u32x4*)(L + a_t0);
            const u32 ta = *(LAS u32*)(L + a_ta), tb = *(LAS u32*)(L + a_tb), tn = *(LAS u16*)(L + a_tn);
            const u32x4 o = {__builtin_amdgcn_perm(ta, ta, s0), __builtin_amdgcn_perm(tb, ta, s1),
                             __builtin_amdgcn_perm(tn, tb, s2), __builtin_amdgcn_perm(tn, tb, s3)};
            st16(WS, 16 * tid, WS_K + d * SEQ * 2, S == 0 ? w : o);
        }
        ST(6);
        ST_FLUSH(0);
    }
}

constexpr int RT_W1 = 176, RT_W2 = 44;
DI void retile(LAS char* L, char* ws, const u16* Wg, const u16* Wu, const u16* Wd, int b, int tid, int wave, int lane) {
    const rsrc_t WS = make_rsrc(ws);
    const u32 lb = (u32)(uintptr_t)L + wave * 4096;
    const int g = lane >> 4, q = (lane & 15) >> 2, pp = lane & 3;
    u32x2 f[8];
    if (b < RT_W1) {
        const int ct = b >> 3, ty = (b >> 2) & 1, rt = b & 3;
        const u32 vo = (128 * rt + 32 * wave + (lane >> 3)) * (FF * 2) + ct * 128 + (((lane & 7) * 16) & (ct == 21 ? 63 : 127));
#pragma unroll
        for (int i = 0; i < 4; ++i) glds_s<1>((const char*)(ty ? Wu : Wg), vo + 8 * i * (FF * 2), lb + i * 1024);
        wait_vm<0>();
        const u32 a = lb + (8 * g + q) * 128 + pp * 8;
        asm volatile("ds_read_b64_tr_b16 %0, %8\n\tds_read_b64_tr_b16 %1, %8 offset:512\n\t"
                     "ds_read_b64_tr_b16 %2, %8 offset:32\n\tds_read_b64_tr_b16 %3, %8 offset:544\n\t"
                     "ds_read_b64_tr_b16 %4, %8 offset:64\n\tds_read_b64_tr_b16 %5, %8 offset:576\n\t"
                     "ds_read_b64_tr_b16 %6, %8 offset:96\n\tds_read_b64_tr_b16 %7, %8 offset:608\n\ts_waitcnt lgkmcnt(0)"
                     : "=&v"(f[0]), "=&v"(f[1]), "=&v"(f[2]), "=&v"(f[3]), "=&v"(f[4]), "=&v"(f[5]), "=&v"(f[6]), "=&v"(f[7])
                     : "v"(a) : "memory");
        LAS float* red = (LAS float*)(L + 32768);
#pragma unroll
        for (int i = 0; i < 4; ++i) {
            const int u = 4 * ct + i;
            if (u < NU) st16(WS, lane * 16, WS_W1 + ((u * 16 + 4 * rt + wave) * 2 + ty) * 1024, u32x4{f[2 * i][0], f[2 * i][1], f[2 * i + 1][0], f[2 * i + 1][1]});
        }
#pragma unroll
        for (int i = 0; i < 4; i += 2) {
            const float s0 = dot2(f[2 * i][0], ONES, dot2(f[2 * i][1], ONES, dot2(f[2 * i + 1][0], ONES, dot2(f[2 * i + 1][1], ONES, 0.f))));
            const float s1 = dot2(f[2 * i + 2][0], ONES, dot2(f[2 * i + 2][1], ONES, dot2(f[2 * i + 3][0], ONES, dot2(f[2 * i + 3][1], ONES, 0.f))));
            const float v = row_pair_sum(half_sum2(s0, s1));
            if ((lane & 16) == 0) red[wave * 64 + (i + (lane >> 5)) * 16 + (lane & 15)] = v;
        }
        __syncthreads();
        if (tid < 64 && 4 * ct + (tid >> 4) < NU)
            ((float*)(ws + WS_CSP))[((rt * 90 + 4 * ct + (tid >> 4)) * 2 + ty) * 16 + (tid & 15)] =
                (red[tid] + red[64 + tid]) + (red[128 + tid] + red[192 + tid]);
    } else {
        const int bb = b - RT_W1, rt = bb >> 2, cp = bb & 3;
#pragma unroll
        for (int i = 0; i < 8; ++i)
            glds_s<1>((const char*)Wd, min(128 * rt + 32 * wave + (lane >> 3) + 8 * (i & 3), FF - 1) * (DM * 2) + (2 * cp + (i >> 2)) * 128 + (lane & 7) * 16,
                      lb + (i >> 2) * 16384 + (i & 3) * 1024);
        wait_vm<0>();
#pragma unroll
        for (int h = 0; h < 2; ++h) {
            const u32 a = lb + h * 16384 + (8 * (g >> 1) + q) * 128 + pi_fwd(16 * (g & 1) + 4 * pp) * 2;
            asm volatile("ds_read_b64_tr_b16 %0, %8\n\tds_read_b64_tr_b16 %1, %8 offset:512\n\t"
                         "ds_read_b64_tr_b16 %2, %8 offset:64\n\tds_read_b64_tr_b16 %3, %8 offset:576\n\t"
                         "ds_read_b64_tr_b16 %4, %8 offset:2048\n\tds_read_b64_tr_b16 %5, %8 offset:2560\n\t"
                         "ds_read_b64_tr_b16 %6, %8 offset:2112\n\tds_read_b64_tr_b16 %7, %8 offset:2624\n\ts_waitcnt lgkmcnt(0)"
                         : "=&v"(f[0]), "=&v"(f[1]), "=&v"(f[2]), "=&v"(f[3]), "=&v"(f[4]), "=&v"(f[5]), "=&v"(f[6]), "=&v"(f[7])
                         : "v"(a) : "memory");
#pragma unroll
            for (int i = 0; i < 4; ++i) {
                const int u = 8 * rt + 2 * wave + (i >> 1), cb = 4 * cp + 2 * h + (i & 1);
                if (u < NU) st16(WS, lane * 16, WS_W2 + (cb * NU + u) * 1024, u32x4{f[2 * i][0], f[2 * i][1], f[2 * i + 1][0], f[2 * i + 1][1]});
            }
        }
    }
}

constexpr int CV_KROW = (64 + SEQ) * 2;
constexpr int CV_ZRS = 84 * 16;
constexpr int CV_Z = 2 * CV_KROW;
constexpr int CV_C = CV_Z + 2 * 4 * CV_ZRS;
constexpr int CV_DUMP = CV_C + 2 * 9 * 64;
constexpr int CV_X = CV_DUMP + 1024 + 9 * 64;
constexpr int CV_S = CV_X + 2 * SEQ * 2;
constexpr int CV_LDS = CV_S + SEQ * 16;
#define PF_SLEEP 40
#define CV_GRID 512

template <int HALF> struct TauSet { static constexpr int t(int i) { return HALF == 0 ? (i == 0 ? 0 : i == 1 ? 2 : i == 2 ? 5 : 7) : (i == 0 ? 1 : i == 1 ? 3 : i == 2 ? 4 : 6); } };

template <int HALF>
DI void conv_main(LAS char* L, rsrc_t xout, u32 xoff, u32 a_z, u32 a_k, u32 a_cw, u32 a_cr, u32 a_x, const bf16x8 (&kf0)[8]
) {
    using TS = TauSet<HALF>;
    constexpr int TMAX = TS::t(3);
    f32x16 acc[4];
#pragma unroll
    for (int i = 0; i < 4; ++i)
#pragma unroll
        for (int r = 0; r < 16; ++r) acc[i][r] = 0.f;
    LAS char* zb = L + a_z;
    LAS char* kb = L + a_k;
    u32x2 xm[4];
    bf16x8 kf[2][TMAX + 1], zf[2][TMAX + 1];
    auto load = [&](auto EI) {
        constexpr int ei = EI, e = 2 * ei + 1;
#pragma unroll
        for (int m = 0; m <= TMAX; ++m) {
            if constexpr (ei == 0) kf[0][m] = kf0[m];
            else kf[ei & 1][m] = tr_frag(kb + 32 + 64 * (8 * m + e), kb + 64 * (8 * m + e));
            zf[ei & 1][m] = *(LAS bf16x8*)(zb + 16 * (8 * m + 7 - e));
            if constexpr (ei == 0) sched_fence();
        }
    };
    load(std::integral_constant<int, 0>{});
    static_for<3>([&](auto EI) {
        constexpr int ei = EI;
        sched_fence();
        load(std::integral_constant<int, ei + 1>{});
        if constexpr (ei == 2) {
#pragma unroll
            for (int i = 0; i < 4; ++i) xm[i] = *(LAS u32x2*)(L + a_x + 512 * TS::t(i));
        }
#pragma unroll
        for (int i = 0; i < 4; ++i)
#pragma unroll
            for (int mu = 0; mu <= TS::t(i); ++mu) acc[i] = MFMA32(zf[ei & 1][TS::t(i) - mu], kf[ei & 1][mu], acc[i]);
        if constexpr (ei == 0) __builtin_amdgcn_sched_group_barrier(0x8, 2, 0);
#pragma unroll
        for (int k = 0; k < 13; ++k) { __builtin_amdgcn_sched_group_barrier(0x8, 1, 0); __builtin_amdgcn_sched_group_barrier(0x100, 2, 0); }
        PIN16(acc[0]); PIN16(acc[1]); PIN16(acc[2]); PIN16(acc[3]);
    });
    sched_fence();
    u32 a_c[4];
#pragma unroll
    for (int i = 0; i < 4; ++i) a_c[i] = pinned(a_cr ? a_cr + 64 * TS::t(i) : 0u);
    float y[4][4];
    auto post = [&](auto II) {
        constexpr int i = II;
        float s1[4], s2[4], v[4];
#pragma unroll
        for (int g = 0; g < 4; ++g) {
            v[g] = opaque(acc[i][4 * g + 2] + dppz<0x130>(acc[i][4 * g + 1]));
            s2[g] = opaque(acc[i][4 * g + 2] + dppz<0x138>(acc[i][4 * g + 3]));
        }
#pragma unroll
        for (int g = 0; g < 4; ++g) v[g] = opaque(acc[i][4 * g + 3] + dppz<0x130>(v[g]));
        *(LAS f32x4*)(L + a_cw + 64 * TS::t(i)) = f32x4{v[0], v[1], v[2], v[3]};
#pragma unroll
        for (int g = 0; g < 4; ++g) {
            s1[g] = opaque(acc[i][4 * g + 1] + dppz<0x138>(s2[g]));
            y[i][g] = opaque(acc[i][4 * g] + ((g & 1) ? bfhi(xm[i][g >> 1]) : bflo(xm[i][g >> 1])));
        }
#pragma unroll
        for (int g = 0; g < 4; ++g) y[i][g] = opaque(y[i][g] + dppz<0x138>(s1[g]));
    };
    static_for<4>([&](auto II) {
        constexpr int i = II;
#pragma unroll
        for (int mu = 0; mu <= TS::t(i); ++mu) acc[i] = MFMA32(zf[1][TS::t(i) - mu], kf[1][mu], acc[i]);
        if constexpr (i > 0) {
            post(std::integral_constant<int, i - 1>{});
            __builtin_amdgcn_sched_group_barrier(0x8, 2, 0);
#pragma unroll
            for (int k = 2; k <= TS::t(i); ++k) { __builtin_amdgcn_sched_group_barrier(0x202, 6, 0); __builtin_amdgcn_sched_group_barrier(0x8, 1, 0); }
        }
        PIN16(acc[i]);
        sched_fence();
    });
    ST(4);
    post(std::integral_constant<int, 3>{});
    ST(5);
    wait_lgkm0();
    raw_barrier();
    ST(6);
    f32x4 cv[4];
    float cp[4];
#pragma unroll
    for (int i = 0; i < 4; ++i) {
        cv[i] = *(LAS f32x4*)(L + a_c[i] + 64);
        cp[i] = *(LAS float*)(L + a_c[i] + 12);
    }
    static_for<4>([&](auto II) {
        constexpr int i = II;
        st8(xout, xoff, 4 * TS::t(i) * XTILE, u32x2{pk_bf16(opaque(TS::t(i) ? y[i][0] + cp[i] : y[i][0]), opaque(y[i][1] + cv[i][0])),
                                                    pk_bf16(opaque(y[i][2] + cv[i][1]), opaque(y[i][3] + cv[i][2]))});
    });
}

extern "C" __global__ __launch_bounds__(256, 2) void s4_conv_kernel(const u16* Wg, const u16* Wu, const u16* Wd) {
    char* ws = g_ws;
    extern __shared__ __attribute__((aligned(16))) char smem_raw[];
    LAS char* L = (LAS char*)smem_raw;
    ST_DECLN;
    const int layer = LAYER_;
    const int tid = TID_, lane = tid & 63, wave = __builtin_amdgcn_readfirstlane(tid >> 6);
    if (__builtin_expect(blockIdx.x >= 256, 0)) {
        if (layer == 0) {
            retile(L, ws, Wg, Wu, Wd, blockIdx.x - 256, tid, wave, lane);
            return;
        }
        const int xcd = blockIdx.x & 7, sl = ((blockIdx.x - 256) >> 3) * 4 + wave;
        constexpr int P1 = (96 / (8 / TG1)) * 32, N1 = (P1 + 127) / 128;
        const int k1 = (xcd / TG1) * P1 + sl * N1;
        const u32 dst = (u32)(uintptr_t)L + wave * 1024;
        __builtin_amdgcn_s_sleep(PF_SLEEP);
#pragma unroll 1
        for (int i = 0; i < N1; ++i) glds_s<1>(ws + WS_W1 + (size_t)min(k1 + i, 90 * 32 - 1) * 1024, lane * 16, dst);
        wait_vm<0>();
        return;
    }
    const u16* xin = (const u16*)(ws + WS_X + 2 * layer * WS_XSTEP);
    const rsrc_t xout = make_rsrc(ws + WS_X + (2 * layer + 1) * WS_XSTEP);
    const int ch = wave >> 1, half = wave & 1, d0 = 2 * blockIdx.x, d = d0 + ch;
    const int tau = tid >> 5, c = tid & 3, sp = (tid >> 2) & 7;
    const int p0 = 256 * tau + 32 * sp + 4 * c;
    const u32 l0 = (u32)(uintptr_t)L;
    {
        const int rot = (blockIdx.x >> 3) & 3;
        const char* sb = ws + WS_ST64 + layer * (SEQ * 16) + wave * 8192;
#pragma unroll
        for (int j = 0; j < 4; ++j) {
            const int k = (j + rot) & 3;
            glds_s<2>(sb + k * 2048, lane * 16, l0 + CV_S + wave * 8192 + k * 2048);
        }
    }
    glds_s<2>(ws + WS_K + (size_t)(d * SEQ + half * 1024) * 2, lane * 16, l0 + ch * CV_KROW + 128 + half * 2048);
    {
        const char* xb = (const char*)(xin + 4 * wave * XTILE + d0 * 128);
        const u32 vo = (lane >> 4) * (XTILE * 2) + (lane & 15) * 16;
        glds_s<1>(xb, vo, l0 + CV_X + wave * 1024);
        glds_s<1>(xb + 256, vo, l0 + CV_X + 4096 + wave * 1024);
    }
    const u32 xoff = 2 * xt_off(d, 4 * lane);
    if (wave == 0) *(LAS u32x4*)(L + CV_Z + (lane >> 3) * CV_ZRS + 16 * (lane & 7)) = u32x4{0u, 0u, 0u, 0u};
    if (wave == 1) *(LAS u32x4*)(L + ((lane >> 3) & 1) * CV_KROW + 16 * (lane & 7)) = u32x4{0u, 0u, 0u, 0u};
    const int hh = lane >> 5, cbase = CV_C + ch * 9 * 64;
    const u32 a_z = pinned(CV_Z + (ch * 4 + (lane & 3)) * CV_ZRS + 128 + 16 * (((lane & 31) >> 2) + hh) - 16 * 7);
    const u32 a_k = pinned(ch * CV_KROW + 128 + 2 * (16 * ((lane >> 4) & 1) - 32 * hh - 4 * ((lane & 15) >> 2) + 4 * (lane & 3)) - 32);
    const u32 a_cw = pinned((lane >= 61 ? cbase + 64 - 61 * 16 : CV_DUMP) + lane * 16);
    const u32 a_cr = pinned(lane < 3 ? cbase + 16 * lane : 0);
    const u32 a_x = pinned(CV_X + ch * 4096 + 8 * lane);
    STN(1);
    f32x2 rc[2][4];
    LAS char* sl = L + CV_S + wave * 8192 + lane * 16;
    wait_vm<4>();
    ST(8);
#pragma unroll
    for (int j = 0; j < 8; ++j) {
        const f64x2 sq = *(LAS f64x2*)(sl + j * 1024);
        const float nm = (float)sq[0] * (-1.f / DM);
        const float var = fmaxf(__builtin_fmaf(-nm, nm, (float)sq[1] * (1.f / DM)), 0.f);
        const float rs = __builtin_amdgcn_rsqf(var + 1e-5f);
        rc[j >> 2][j & 3] = f32x2{rs, nm * rs};
    }
    asm volatile("" : "+v"(rc[0][0]), "+v"(rc[0][1]), "+v"(rc[0][2]), "+v"(rc[0][3]), "+v"(rc[1][0]), "+v"(rc[1][1]), "+v"(rc[1][2]), "+v"(rc[1][3]));
    wait_vm<2>();
    raw_barrier();
    bf16x8 kf0[8];
#pragma unroll
    for (int m = 0; m < 8; ++m) kf0[m] = tr_frag(L + a_k + 32 + 64 * (8 * m + 1), L + a_k + 64 * (8 * m + 1));
    wait_vm<0>();
    u32x2 xz[2][2];
#pragma unroll
    for (int k = 0; k < 2; ++k)
#pragma unroll
        for (int s = 0; s < 2; ++s) xz[k][s] = *(LAS u32x2*)(L + CV_X + k * 4096 + (p0 + 16 * s) * 2);
    ST(9);
    {
        LAS char* zw = L + CV_Z + c * CV_ZRS + 128 + 2 * (64 * tau + 2 * sp);
#pragma unroll
        for (int k = 0; k < 2; ++k)
#pragma unroll
            for (int g = 0; g < 4; ++g) {
                const float a = (g & 1) ? bfhi(xz[k][0][g >> 1]) : bflo(xz[k][0][g >> 1]);
                const float e = (g & 1) ? bfhi(xz[k][1][g >> 1]) : bflo(xz[k][1][g >> 1]);
                *(LAS u32*)(zw + k * 4 * CV_ZRS + 32 * g) =
                    pk_bf16(__builtin_fmaf(a, rc[0][g][0], rc[0][g][1]), __builtin_fmaf(e, rc[1][g][0], rc[1][g][1]));
            }
    }
    ST(2);
    asm volatile("s_waitcnt vmcnt(0) lgkmcnt(0)" ::: "memory");
    raw_barrier();
    ST(3);
    if (half) conv_main<1>(L, xout, xoff, a_z, a_k, a_cw, a_cr, a_x, kf0); else conv_main<0>(L, xout, xoff, a_z, a_k, a_cw, a_cr, a_x, kf0);
    ST(7);
    ST_FLUSH(layer == 0 ? 1 : 4);
}

#define G1_R 6
#define MAP2 1
#define ROT2 0
#define RAMP2 4
#define W2_START 2
#define W2_PER 0
#define W2_TAIL 6
constexpr int G1_NS = 16, G1_SLOT = 20480;
constexpr int G2_NS = 11, G2_R = 7, G2_SLOT = 16384, KH = NU / 2;
constexpr int ML_RED = 131072, ML_OS = ML_RED, ML_XS = ML_RED + 8192, ML_EX = ML_XS + 8192, ML_LDS = ML_EX + 16384;
constexpr int ML_CTR = ML_EX;

constexpr int G1_LEAD = G1_R - 2;
constexpr int g1_nis(int s) { int a = 2 * s + 2, b = s + G1_LEAD; a = a < b ? a : b; return a < G1_NS ? a : G1_NS; }
constexpr int g1_wlo(int s) {
    int a = s <= W2_START ? 0 : s <= G1_NS - G1_LEAD ? W2_PER * (s - W2_START)
                              : W2_PER * (G1_NS - G1_LEAD - W2_START) + W2_TAIL * (s - (G1_NS - G1_LEAD));
    return a < KH ? a : KH;
}
constexpr int g1_younger(int s) {
    int t = -1;
    while (g1_nis(t + 1) <= s) ++t;
    return 5 * (g1_nis(s) - 1 - s) + g1_wlo(s) - g1_wlo(t < 0 ? 0 : t);
}

extern "C" __global__ __launch_bounds__(512) void s4_mlp_kernel(u16* out) {
    char* ws = g_ws;
    extern __shared__ __attribute__((aligned(16))) char smem_raw[];
    LAS char* L = (LAS char*)smem_raw;
    const int layer = LAYER_, tid = TID_;
    const u16* xT = (const u16*)(ws + WS_X + (2 * layer + 1) * WS_XSTEP);
    const int lane = tid & 63, wave = __builtin_amdgcn_readfirstlane(tid >> 6);
    const int xcd = blockIdx.x & 7, sl = blockIdx.x >> 3;
    constexpr int MPX = 16 / TG1;
    const int mt = (xcd % TG1) * MPX + (sl % MPX), fg = (xcd / TG1) * (32 / MPX) + (sl / MPX);
    const int m2 = 2 * mt + (fg & 1), nt = fg >> 1;
    const int rot = ROT2 == 0 ? 0 : ROT2 == 1 ? (nt & 1) * 5 : MAP2 ? (nt & 3) * 5 >> 1 : nt + (nt >> 2);
    auto blk_unit = [&](int s, int kh) { return KH * kh + 4 * (s == G2_NS - 1 ? s : s + rot < G2_NS - 1 ? s + rot : s + rot - (G2_NS - 1)); };
    const u32 l0 = (u32)(uintptr_t)L;
    ST_DECL;
    const int g = lane >> 4, q = (lane & 15) >> 2;
    auto xfrag_off = [&](int tb) { return (8 * g + q) * 256 + ((tb ^ (q | ((g & 1) << 2))) << 5) + (lane & 3) * 8; };
    auto xsrc_off = [&](int odd) { return (u32)(g * 256 + ((((lane & 15) >> 1) ^ (g | (odd << 2))) << 5) + (lane & 1) * 16); };
    const char* xtile = (const char*)(xT + mt * XTILE);
    const int u0 = fg * 6;
    using I0 = std::integral_constant<int, 0>; using I1 = std::integral_constant<int, 1>;
    if (wave == 0) { *(LAS u32*)(L + ML_CTR) = 0u; wait_lgkm0(); }

    if (__builtin_expect(fg == 15, 0)) {
        const u32 mine = l0 + wave * 16384;
        const u32 so0 = xsrc_off(0), so1 = xsrc_off(1);
#pragma unroll
        for (int i = 0; i < 8; ++i) glds_s<2>(xtile + (16 * wave + 2 * i) * 1024, (i & 1) ? so1 : so0, mine + i * 2048);
        float S[8], Q[8];
        u32 a[8];
#pragma unroll
        for (int tb = 0; tb < 8; ++tb) { a[tb] = mine + xfrag_off(tb); S[tb] = 0.f; Q[tb] = 0.f; }
        static_for<4>([&](auto KI) {
            constexpr int kk = KI >> 1, tq = (KI & 1) * 4;
            if constexpr (tq == 0) wait_vm<8 - 8 * kk>();
            u32x2 f[8];
            asm volatile("ds_read_b64_tr_b16 %0, %8 offset:%c12\n\tds_read_b64_tr_b16 %1, %8 offset:%c13\n\t"
                         "ds_read_b64_tr_b16 %2, %9 offset:%c12\n\tds_read_b64_tr_b16 %3, %9 offset:%c13\n\t"
                         "ds_read_b64_tr_b16 %4, %10 offset:%c12\n\tds_read_b64_tr_b16 %5, %10 offset:%c13\n\t"
                         "ds_read_b64_tr_b16 %6, %11 offset:%c12\n\tds_read_b64_tr_b16 %7, %11 offset:%c13\n\ts_waitcnt lgkmcnt(0)"
                         : "=&v"(f[0]), "=&v"(f[1]), "=&v"(f[2]), "=&v"(f[3]), "=&v"(f[4]), "=&v"(f[5]), "=&v"(f[6]), "=&v"(f[7])
                         : "v"(a[tq]), "v"(a[tq + 1]), "v"(a[tq + 2]), "v"(a[tq + 3]), "i"(kk * 8192), "i"(kk * 8192 + 1024) : "memory");
#pragma unroll
            for (int j = 0; j < 4; ++j)
#pragma unroll
                for (int tb = 0; tb < 4; ++tb) {
                    const u32 w = f[2 * tb + (j >> 1)][j & 1];
                    S[tq + tb] = dot2(w, ONES, S[tq + tb]); Q[tq + tb] = dot2(w, w, Q[tq + tb]);
                }
        });
        LAS float* red = (LAS float*)(L + ML_RED);
#pragma unroll
        for (int tb = 0; tb < 8; ++tb) {
            const float v = row_pair_sum(half_sum2(S[tb], Q[tb]));
            if ((lane & 16) == 0) red[wave * 256 + (lane >> 5) * 128 + 16 * tb + (lane & 15)] = v;
        }
        wait_lgkm0();
        raw_barrier();
        if (tid < 128) {
            float s = 0.f, qq = 0.f;
#pragma unroll
            for (int w = 0; w < 8; ++w) { s += red[w * 256 + tid]; qq += red[w * 256 + 128 + tid]; }
            float mb, sb;
            ln_scalars(s, qq, mb, sb);
            __hip_atomic_store((u32*)(ws + WS_LNW) + layer * SEQ + mt * 128 + tid, pk_bf16(mb, sb), __ATOMIC_RELAXED, __HIP_MEMORY_SCOPE_AGENT);
        }
    }
    if (__builtin_expect(wave >= 4, 1)) {
        const int lw = wave - 4;
        const int wn = lw & 1, kh = lw >> 1, hh = lane >> 5;
        bf16x8 w2[KH];
        const rsrc_t WR = make_rsrc(ws + WS_W2 + (2 * nt + wn) * NU * 1024);
        auto ld_w2 = [&](auto JI) {
            constexpr int j = JI;
            w2[j] = __builtin_bit_cast(bf16x8, __builtin_amdgcn_raw_buffer_load_b128(WR, lane * 16 + (j & 3) * 1024, blk_unit(j >> 2, kh) * 1024, 0));
        };
        auto ld_res = [&]() {
            const char* xb = (const char*)(xT + mt * XTILE + (64 * nt + 32 * wn) * 128 + (m2 & 1) * 64 + 32 * kh);
            const u32 vo = (lane >> 2) * 256 + (((lane & 3) ^ ((lane >> 4) & 3)) << 4);
            glds_s<1>(xb, vo, l0 + ML_XS + lw * 2048);
            glds_s<1>(xb + 4096, vo, l0 + ML_XS + lw * 2048 + 1024);
        };
        if (__builtin_expect(fg != 15, 1)) {
            const u32 xo = xsrc_off(lw & 1), wo = lane * 16;
            const char* xs = xtile + 2 * lw * 1024;
            const int up = lw + (lw >> 1) + (lw & 1), us = 1 + 3 * (lw >> 1);
            const char* wp = ws + WS_W1 + (u0 + up) * 32 * 1024;
            const char* w1 = ws + WS_W1 + ((u0 + us) * 32 + (lw & 1)) * 1024;
            auto issue = [&](auto SI) {
                constexpr int s = SI;
                const u32 dst = l0 + (s % G1_R) * G1_SLOT;
                glds_s<2>(xs + s * 8192, xo, dst + 2 * lw * 1024);
                glds_s<2>(wp + s * 2048, wo, dst + 8192 + up * 2048);
                glds_s<1>(w1 + s * 2048, wo, dst + 8192 + us * 2048 + (lw & 1) * 1024);
            };
            issue(I0{}); issue(I1{});
            ST(1);
            static_for<G1_NS>([&](auto SI) {
                constexpr int s = SI, n = g1_nis(s), n2 = g1_nis(s + 1);
                wait_vm<g1_younger(s)>();
                raw_barrier();
                if constexpr (n2 > n) issue(std::integral_constant<int, n>{});
                if constexpr (n2 > n + 1) issue(std::integral_constant<int, n + 1>{});
                constexpr int lo = g1_wlo(s), hi = s == G1_NS - 1 ? KH : g1_wlo(s + 1);
                static_for<hi - lo>([&](auto JI) { ld_w2(std::integral_constant<int, lo + JI>{}); });
                if constexpr (s == G1_NS - G1_LEAD) ST(2);
            });
        } else {
            static_for<KH>([&](auto JI) { ld_w2(JI); });
        }
        ST(3);
        ld_res();
        wait_vm<0>();
        ST(4);
        const int item = pi_fwd(lane & 31), row = 32 * wn + item;
        f32x16 acc[2];
        {
            LAS char* xs = L + ML_XS + lw * 2048 + item * 64;
            const u32x4 a = *(LAS u32x4*)(xs + (((2 * hh) ^ ((item >> 2) & 3)) << 4));
            const u32x4 c = *(LAS u32x4*)(xs + (((2 * hh + 1) ^ ((item >> 2) & 3)) << 4));
#pragma unroll
            for (int p = 0; p < 4; ++p) {
                acc[0][2 * p] = bflo(a[p]); acc[0][2 * p + 1] = bfhi(a[p]);
                acc[0][8 + 2 * p] = bflo(c[p]); acc[0][9 + 2 * p] = bfhi(c[p]);
            }
#pragma unroll
            for (int r = 0; r < 16; ++r) acc[1][r] = 0.f;
            PIN16(acc[0]); PIN16(acc[1]);
        }
        const u32 a_ex = pinned(ML_EX + lw * 4096 + lane * 16);
        const u32 a_os = pinned(layer == 0 ? ML_OS + row * 128 + (((4 * kh + 2 * hh) ^ (row & 7)) << 4) : ML_OS + (32 * kh + 16 * hh) * 128 + row * 2);
        const int rr = 4 * (lane >> 4) + ((lane & 15) >> 2), spos = 64 * m2 + 16 * lw + (lane & 15);
        const u32 a_rp = pinned(ML_OS + rr * 128 + (((2 * lw + ((lane & 3) >> 1)) ^ (rr & 7)) << 4) + 8 * (lane & 1));
        const u32 a_st = pinned((lane & 16) ? 0x80000000u : 16 * st_slot(st_thread(spos), st_item(spos), 8) + 8 * hh);
        ST(7);
        raw_barrier();
        ST(5);
        LAS char* hk = L + kh * 8192 + kh * 4096 + lane * 16;
        LAS char* hs = L + (kh ^ 1) * 8192 + kh * 4096 + lane * 16;
        bf16x8 hf[2][4];
        auto rd2 = [&](auto SI, auto HI) {
            constexpr int s = SI, hlf = HI;
#pragma unroll
            for (int j = 0; j < 2; ++j)
                if (4 * s + 2 * hlf + j < KH) {
                    hf[hlf][2 * j] = *(LAS bf16x8*)(hk + (s % G2_R) * G2_SLOT + (2 * hlf + j) * 1024);
                    hf[hlf][2 * j + 1] = *(LAS bf16x8*)(hs + (s % G2_R) * G2_SLOT + (2 * hlf + j) * 1024);
                }
        };
        auto mm2 = [&](auto SI, auto HI) {
            constexpr int s = SI, hlf = HI;
#pragma unroll
            for (int j = 0; j < 2; ++j)
                if (4 * s + 2 * hlf + j < KH) {
                    acc[0] = MFMA32(hf[hlf][2 * j], w2[4 * s + 2 * hlf + j], acc[0]);
                    acc[1] = MFMA32(hf[hlf][2 * j + 1], w2[4 * s + 2 * hlf + j], acc[1]);
                }
        };
        auto mix = [&]() {
#pragma unroll
            for (int k = 0; k < 4; ++k) { __builtin_amdgcn_sched_group_barrier(0x8, 1, 0); __builtin_amdgcn_sched_group_barrier(0x100, 1, 0); }
            PIN16(acc[0]); PIN16(acc[1]);
            sched_fence();
        };
        static_for<G2_NS>([&](auto SI) {
            constexpr int s = SI;
            raw_barrier();
            rd2(SI, I0{});
            if constexpr (s > 0) mm2(std::integral_constant<int, s - 1>{}, I1{});
            mix();
            rd2(SI, I1{});
            mm2(SI, I0{});
            mix();
        });
        mm2(std::integral_constant<int, G2_NS - 1>{}, I1{});
        ST(8);
#pragma unroll
        for (int k = 0; k < 4; ++k) *(LAS f32x4*)(L + a_ex + 1024 * k) = f32x4{acc[1][4 * k], acc[1][4 * k + 1], acc[1][4 * k + 2], acc[1][4 * k + 3]};
        wait_lgkm0();
        raw_barrier();
        u32 o[8];
#pragma unroll
        for (int k = 0; k < 4; ++k) {
            const f32x4 v = *(LAS f32x4*)(L + (a_ex ^ 8192) + 1024 * k);
            o[2 * k] = pk_bf16(acc[0][4 * k] + v[0], acc[0][4 * k + 1] + v[1]);
            o[2 * k + 1] = pk_bf16(acc[0][4 * k + 2] + v[2], acc[0][4 * k + 3] + v[3]);
        }
        if (layer != 0) {
            LAS u16* ow = (LAS u16*)(L + a_os);
#pragma unroll
            for (int p = 0; p < 8; ++p) { ow[(2 * p) * 64] = (u16)o[p]; ow[(2 * p + 1) * 64] = (u16)(o[p] >> 16); }
            wait_lgkm0();
            raw_barrier();
            ST_FLUSH(5);
            return;
        }
        *(LAS u32x4*)(L + a_os) = u32x4{o[0], o[1], o[2], o[3]};
        *(LAS u32x4*)(L + (a_os ^ 16)) = u32x4{o[4], o[5], o[6], o[7]};
        wait_lgkm0();
        raw_barrier();
        ST(9);
        float S = 0.f, Q = 0.f, S2 = 0.f, Q2 = 0.f;
#pragma unroll
        for (int i = 0; i < 4; ++i) {
            const u32x2 v = __builtin_bit_cast(u32x2, __builtin_amdgcn_ds_read_tr16_b64_v4bf16((LAS bf16x4*)(L + a_rp + i * 2048)));
            S = dot2(v[0], ONES, S); Q = dot2(v[0], v[0], Q);
            S2 = dot2(v[1], ONES, S2); Q2 = dot2(v[1], v[1], Q2);
        }
        const float v = row_pair_sum(half_sum2(S + S2, Q + Q2));
        if (a_st != 0x80000000u) atomicAdd((double*)(ws + WS_ST64 + SEQ * 16 + a_st), (double)v);
        ST(10);
        ST_FLUSH(2);
        return;
    }
    if (fg != 15) {
        const int wm = wave & 1, wn = wave >> 1;
        f32x4 acc[6][4];
        bf16x8 xf[2][4], wf[2][6];
#pragma unroll
        for (int i = 0; i < 6; ++i)
#pragma unroll
            for (int t = 0; t < 4; ++t) acc[i][t] = f32x4{0.f, 0.f, 0.f, 0.f};
        int xo[4];
#pragma unroll
        for (int t = 0; t < 4; ++t) xo[t] = xfrag_off(4 * wm + t);
        const int wo = 8192 + 6 * wn * 1024 + lane * 16;
        int slot = 0;
        auto rd = [&](auto CI) {
            constexpr int c = CI;
            LAS char* base = L + slot * G1_SLOT;
            slot = slot == G1_R - 1 ? 0 : slot + 1;
            wf[c][0] = *(LAS bf16x8*)(base + wo);
#pragma unroll
            for (int t = 0; t < 4; ++t) xf[c][t] = tr_frag(base + xo[t], base + xo[t] + 1024);
#pragma unroll
            for (int i = 1; i < 6; ++i) wf[c][i] = *(LAS bf16x8*)(base + wo + i * 1024);
        };
        auto mm = [&](auto CI) {
            constexpr int c = CI;
#pragma unroll
            for (int i = 0; i < 6; ++i)
#pragma unroll
                for (int t = 0; t < 4; ++t) acc[i][t] = MFMA16(wf[c][i], xf[c][t], acc[i][t]);
        };
        auto step = [&](auto CI) {
            constexpr int c = CI;
            raw_barrier();
            rd(std::integral_constant<int, c>{});
            mm(std::integral_constant<int, c ^ 1>{});
#pragma unroll
            for (int k = 0; k < 14; ++k) { __builtin_amdgcn_sched_group_barrier(0x8, 1, 0); __builtin_amdgcn_sched_group_barrier(0x100, 1, 0); }
            PIN16(acc[5][3]);
            sched_fence();
        };
        float cp[4];
#pragma unroll
        for (int k = 0; k < 4; ++k) cp[k] = ((const float*)(ws + WS_CSP))[(k * 90 + u0) * 32 + (tid < 192 ? tid : 0)];
        ST(1);
        raw_barrier();
        rd(I0{});
        sched_fence();
        ST(2);
#pragma unroll 1
        for (int s = 1; s < G1_NS - 3; s += 2) { step(I1{}); step(I0{}); }
        *(LAS float*)(L + ML_RED + tid * 4) = (cp[0] + cp[1]) + (cp[2] + cp[3]);
        wait_lgkm0();
        u32 st[4];
        f32x4 cs[6];
        u32* stp = (u32*)(ws + WS_LNW) + layer * SEQ + mt * 128 + 64 * wm + (lane & 15);
#pragma unroll
        for (int t = 0; t < 4; ++t) st[t] = __hip_atomic_load(stp + 16 * t, __ATOMIC_RELAXED, __HIP_MEMORY_SCOPE_AGENT);
        sched_fence();
        step(I1{}); step(I0{}); step(I1{});
#pragma unroll
        for (int i = 0; i < 6; ++i) cs[i] = *(LAS f32x4*)(L + ML_RED + (6 * wn + i) * 64 + g * 16);
        for (int spin = 0; spin < (1 << 22) && !__all(st[0] != 0u && st[1] != 0u && st[2] != 0u && st[3] != 0u); ++spin) {
            __builtin_amdgcn_s_sleep(2);
#pragma unroll
            for (int t = 0; t < 4; ++t) st[t] = __hip_atomic_load(stp + 16 * t, __ATOMIC_RELAXED, __HIP_MEMORY_SCOPE_AGENT);
        }
        ST(3);
        mm(I1{});
        ST(4);
        const rsrc_t HR = make_rsrc(ws + WS_H + (1 - layer) * WS_HSTEP + ((4 * mt + 2 * wm) * NU + u0 + 3 * wn) * 1024);
        const u32 hoff = (g >> 1) * 512 + pi_inv(16 * (g & 1) + (lane & 15)) * 16;
        auto glu = [&](int i, int t) {
            const float sb = bfhi(st[t]), nm = -bflo(st[t]);
            const float ce = -1.44269504088896341f * __builtin_amdgcn_rcpf(sb);
            const f32x2 nm2 = {nm, nm}, sb2 = {sb, sb};
            const f32x4 ga = acc[2 * i][t], ua = acc[2 * i + 1][t], cg = cs[2 * i], cu = cs[2 * i + 1];
            u32x2 o;
#pragma unroll
            for (int j = 0; j < 2; ++j) {
                const f32x2 arg = __builtin_elementwise_fma(f32x2{cg[2 * j], cg[2 * j + 1]}, nm2, f32x2{ga[2 * j], ga[2 * j + 1]}) * ce;
                const f32x2 e = {__builtin_amdgcn_exp2f(arg[0]), __builtin_amdgcn_exp2f(arg[1])};
                const f32x2 den = __builtin_elementwise_fma(e, sb2, sb2);
                const f32x2 r = {__builtin_amdgcn_rcpf(den[0]), __builtin_amdgcn_rcpf(den[1])};
                o[j] = pk_bf16(__builtin_elementwise_fma(f32x2{cu[2 * j], cu[2 * j + 1]}, nm2, f32x2{ua[2 * j], ua[2 * j + 1]}) * r);
            }
            return o;
        };
#pragma unroll
        for (int tp = 0; tp < 2; ++tp)
#pragma unroll
            for (int i = 0; i < 3; ++i) {
                const u32x2 a = glu(i, 2 * tp), b = glu(i, 2 * tp + 1);
                const auto r0 = __builtin_amdgcn_permlane16_swap(a[0], b[0], false, false);
                const auto r1 = __builtin_amdgcn_permlane16_swap(a[1], b[1], false, false);
                st16(HR, hoff + i * 1024, (u0 + 3 * wn + i < NU ? 0u : 0x80000000u) + tp * NU * 1024, u32x4{r0[0], r1[0], r0[1], r1[1]});
            }
        ST(5);
    }
    const char* hF = ws + WS_H + (1 - layer) * WS_HSTEP;
    wait_vm<0>();
    ST(6);
    u32 order = 0;
    if (lane == 0) order = __hip_atomic_fetch_add((LAS u32*)(L + ML_CTR), 1u, __ATOMIC_RELAXED, __HIP_MEMORY_SCOPE_WORKGROUP);
    if (__builtin_amdgcn_readfirstlane(order) == 3u) {
        u32* cnt = (u32*)(ws + WS_CNT) + (layer * 16 + mt) * 512;
        u32 v = 0;
        if (fg == 15) __builtin_amdgcn_s_sleep(100);
        else if (lane < 16) v = __hip_atomic_fetch_add(cnt + lane * 32, 1u, __ATOMIC_RELAXED, __HIP_MEMORY_SCOPE_AGENT) + 1u;
        v = __builtin_amdgcn_readfirstlane(v);
#pragma unroll 1
        for (int spin = 0; spin < (1 << 22) && v < 15u; ++spin)
            v = __builtin_amdgcn_readfirstlane(__hip_atomic_load(cnt + fg * 32, __ATOMIC_RELAXED, __HIP_MEMORY_SCOPE_AGENT));
    }
    raw_barrier();
    ST(7);
    {
        const char* src = hF + (2 * m2 + (wave >> 1)) * NU * 1024;
        const u32 vo = lane * 16, dst = l0 + wave * 4096;
        auto issue = [&](auto SI) {
            constexpr int s = SI;
            glds_s<(s < G2_NS - 1 ? 4 : 3), true>(src + blk_unit(s, wave & 1) * 1024, vo, dst + (s % G2_R) * G2_SLOT);
        };
        constexpr int LEAD = G2_R - 2;
        auto nissued = [](int s) { int a = 2 * s + RAMP2, b = s + LEAD + (s < 2 ? 2 - s : 0); a = a < b ? a : b; return a < G2_NS ? a : G2_NS; };
        static_for<RAMP2>([&](auto SI) { issue(SI); });
        static_for<G2_NS>([&](auto SI) {
            constexpr int s = SI, n = nissued(s), n2 = nissued(s + 1);
            constexpr int fly = n - 1 - s;
            wait_vm<(fly > 0 ? 4 * fly - (n == G2_NS ? 1 : 0) : 0)>();
            if constexpr (s == 0) ST(9);
            if constexpr (s == 2) ST(10);
            if constexpr (s == 5) ST(11);
            if constexpr (s == 8) ST(12);
            raw_barrier();
            if constexpr (n2 > n) issue(std::integral_constant<int, n>{});
            if constexpr (n2 > n + 1) issue(std::integral_constant<int, n + 1>{});
        });
    }
    ST(8);
    {
        const rsrc_t xout = make_rsrc(layer ? (char*)out : ws + WS_X + 2 * WS_XSTEP);
        const int pos = 64 * m2 + 16 * wave + (lane >> 3);
        const int tok = (pos & ~255) | ((pos & 3) << 6) | ((pos >> 2) & 63);
        const u32 vo = pinned(layer ? tok * (DM * 2) + 128 * nt + ((lane & 7) << 4)
                                    : 2 * (mt * XTILE + (64 * nt + 16 * wave + (lane >> 3)) * 128 + (m2 & 1) * 64) + (((lane & 7) ^ (lane >> 3)) << 4));
        const u32 a_rd = pinned(ML_OS + wave * 2048 + lane * 16);
        raw_barrier();
        raw_barrier();
        st16(xout, vo, 0, *(LAS u32x4*)(L + a_rd));
        st16(xout, vo, 2048, *(LAS u32x4*)(L + a_rd + 1024));
    }
    ST_FLUSH(layer ? 5 : 2);
}

extern "C" __global__ void s4_tail_kernel() {}

extern "C" void kernel_launch(void* const* d_in, const int* in_sizes, int n_in, void* d_out, int out_size,
                              void* d_ws, size_t ws_size, hipStream_t stream) {
    (void)in_sizes; (void)n_in; (void)out_size; (void)ws_size;
    const u16* x0 = (const u16*)d_in[0];
    const u16* logA = (const u16*)d_in[1];
    const u16* Aim = (const u16*)d_in[2];
    const u16* Cre = (const u16*)d_in[3];
    const u16* Cim = (const u16*)d_in[4];
    const u16* logdt = (const u16*)d_in[5];
    const u16* Dsk = (const u16*)d_in[6];
    const u16* Wg = (const u16*)d_in[8];
    const u16* Wu = (const u16*)d_in[9];
    const u16* Wd = (const u16*)d_in[10];
    (void)d_ws;
    (void)hipFuncSetAttribute((const void*)s4_conv_kernel, hipFuncAttributeMaxDynamicSharedMemorySize, CV_LDS);
    (void)hipFuncSetAttribute((const void*)s4_mlp_kernel, hipFuncAttributeMaxDynamicSharedMemorySize, ML_LDS);

    s4_prep_kernel<<<PREP_BLOCKS, 256, PREP_LDS, stream>>>(x0, logA, Aim, Cre, Cim, logdt, Dsk);
    for (int layer = 0; layer < 2; ++layer) {
        s4_conv_kernel<<<layer ? CV_GRID : 256 + RT_W1 + RT_W2, layer ? dim3(1, 256) : dim3(256), CV_LDS, stream>>>(Wg, Wu, Wd);
        s4_mlp_kernel<<<256, layer ? dim3(1, 512) : dim3(512), ML_LDS, stream>>>((u16*)d_out);
    }
}
```

```cpp
#include <hip/hip_runtime.h>
#include <hip/hip_bf16.h>
#include <stdint.h>
#include <utility>

typedef unsigned short u16;
typedef unsigned int u32;
typedef __bf16 bf16x8 __attribute__((ext_vector_type(8)));
typedef __bf16 bf16x4 __attribute__((ext_vector_type(4)));
typedef __bf16 bf16x2 __attribute__((ext_vector_type(2)));
typedef _Float16 f16x8 __attribute__((ext_vector_type(8)));
typedef _Float16 f16x2 __attribute__((ext_vector_type(2)));
typedef float f32x16 __attribute__((ext_vector_type(16)));
typedef float f32x4 __attribute__((ext_vector_type(4)));
typedef float f32x2 __attribute__((ext_vector_type(2)));
typedef double f64x2 __attribute__((ext_vector_type(2)));
typedef u32 u32x4 __attribute__((ext_vector_type(4)));
typedef u32 u32x2 __attribute__((ext_vector_type(2)));

#define LAS __attribute__((address_space(3)))
#define GAS __attribute__((address_space(1)))
#define DI __device__ __forceinline__

constexpr int SEQ = 2048, DM = 512, NST = 128, FF = 1376, NU = FF / 16;
constexpr int XTILE = DM * 128;

constexpr size_t MB = 1 << 20;
constexpr size_t WS_X = 0, WS_XSTEP = 2 * MB;
constexpr size_t WS_K = 8 * MB, WS_W1 = 10 * MB, WS_W2 = 13 * MB, WS_MISC = 15 * MB - 262144, WS_H = 15 * MB, WS_HSTEP = 6 * MB;
constexpr size_t WS_BYTES = 27 * MB;
constexpr size_t WS_CSP = WS_MISC;
constexpr size_t WS_ST64 = WS_MISC + 65536;
constexpr size_t WS_ZERO = WS_ST64 + SEQ * 16;
constexpr size_t WS_LNW = WS_ZERO + SEQ * 16, WS_CNT = WS_LNW + 2 * SEQ * 4;
constexpr int ZERO_BYTES = SEQ * 16 + 2 * SEQ * 4 + 512 * 128;
__device__ __attribute__((aligned(4096))) char g_ws[WS_BYTES];

constexpr int TG1 = 4;

#define TID_ (threadIdx.x + threadIdx.y)
#define LAYER_ (__builtin_amdgcn_readlane(threadIdx.y, 1) != 0u ? 1 : 0)

DI float bflo(u32 w) { return __uint_as_float(w << 16); }
DI float bfhi(u32 w) { return __uint_as_float(w & 0xffff0000u); }
DI u32 pk_bf16(f32x2 v) { return __builtin_bit_cast(u32, __builtin_convertvector(v, bf16x2)); }
DI u32 pk_bf16(float lo, float hi) { return pk_bf16(f32x2{lo, hi}); }
DI float rbf(float x) { return bflo(pk_bf16(x, x)); }
DI u32 pk_f16(f32x2 v) { return __builtin_bit_cast(u32, __builtin_convertvector(v, f16x2)); }
DI float dot2(u32 a, u32 b, float c) { return __builtin_amdgcn_fdot2_f32_bf16(__builtin_bit_cast(bf16x2, a), __builtin_bit_cast(bf16x2, b), c, false); }
DI u32 perm_lo(u32 hi, u32 lo) { return __builtin_amdgcn_perm(hi, lo, 0x05040100u); }
DI u32 perm_hi(u32 hi, u32 lo) { return __builtin_amdgcn_perm(hi, lo, 0x07060302u); }
DI int pi_fwd(int r) { return (((r >> 2) & 1) << 4) | ((r >> 3) << 2) | (r & 3); }
DI int pi_inv(int p) { return (((p >> 2) & 3) << 3) | ((p >> 4) << 2) | (p & 3); }
DI int xt_off(int ch, int pos) { return (pos >> 7) * XTILE + ch * 128 + (pos & 127); }
DI int st_thread(int pos) { return ((pos >> 8) << 5) | (((pos >> 5) & 7) << 2) | ((pos >> 2) & 3); }
DI int st_item(int pos) { return (((pos >> 4) & 1) << 2) | (pos & 3); }
DI int st_slot(int thread, int item, int nitem) { return ((thread >> 6) * nitem + item) * 64 + (thread & 63); }
constexpr u32 ONES = 0x3f803f80u;

template <int N> DI void wait_vm() { asm volatile("s_waitcnt vmcnt(%0)" ::"n"(N) : "memory"); }
DI void wait_lgkm0() { asm volatile("s_waitcnt lgkmcnt(0)" ::: "memory"); }
DI void raw_barrier() { asm volatile("" ::: "memory"); __builtin_amdgcn_s_barrier(); asm volatile("" ::: "memory"); }
DI float opaque(float v) { asm("" : "+v"(v)); return v; }
DI u32 pinned(u32 v) { asm volatile("" : "+v"(v)); return v; }
#define PIN16(x) asm volatile("" : "+v"(x))
DI void sched_fence() { __builtin_amdgcn_sched_barrier(0); }
DI bf16x8 tr_frag(LAS char* a0, LAS char* a1) {
    bf16x4 lo = __builtin_amdgcn_ds_read_tr16_b64_v4bf16((LAS bf16x4*)a0);
    bf16x4 hi = __builtin_amdgcn_ds_read_tr16_b64_v4bf16((LAS bf16x4*)a1);
    return __builtin_shufflevector(lo, hi, 0, 1, 2, 3, 4, 5, 6, 7);
}
#define GLDS_HEAD "s_mov_b32 %0, m0\n\ts_mov_b32 m0, %3\n\ts_nop 0\n\t"
#define GLDS_ONE(off, sc) "global_load_lds_dwordx4 %1, %2 offset:" #off sc "\n\t"
#define GLDS_TAIL "s_mov_b32 m0, %0" : "=&s"(keep) : "v"(voff), "s"(ubase), "s"(lds_dst) : "memory"
#define GLDS_BODY(sc)                                                                                                          \
    if constexpr (N == 1) asm volatile(GLDS_HEAD GLDS_ONE(0, sc) GLDS_TAIL);                                                   \
    else if constexpr (N == 2) asm volatile(GLDS_HEAD GLDS_ONE(0, sc) GLDS_ONE(1024, sc) GLDS_TAIL);                           \
    else if constexpr (N == 3) asm volatile(GLDS_HEAD GLDS_ONE(0, sc) GLDS_ONE(1024, sc) GLDS_ONE(2048, sc) GLDS_TAIL);        \
    else asm volatile(GLDS_HEAD GLDS_ONE(0, sc) GLDS_ONE(1024, sc) GLDS_ONE(2048, sc) GLDS_ONE(3072, sc) GLDS_TAIL)
template <int N, bool DEV = false> DI void glds_s(const char* ubase, u32 voff, u32 lds_dst) {
    u32 keep;
    lds_dst = __builtin_amdgcn_readfirstlane(lds_dst);
    if constexpr (DEV) { GLDS_BODY(" sc1"); } else { GLDS_BODY(""); }
}
typedef __amdgpu_buffer_rsrc_t rsrc_t;
DI rsrc_t make_rsrc(void* base) { return __builtin_amdgcn_make_buffer_rsrc(base, 0, 0x7fffffff, 0x00020000); }
DI void st16(rsrc_t r, u32 voff, u32 soff, u32x4 v) { __builtin_amdgcn_raw_buffer_store_b128(v, r, voff + soff, 0, 16); }
DI void st8(rsrc_t r, u32 voff, u32 soff, u32x2 v) { __builtin_amdgcn_raw_buffer_store_b64(v, r, voff, soff, 16); }
#define MFMA16(a, b, c) __builtin_amdgcn_mfma_f32_16x16x32_bf16((a), (b), (c), 0, 0, 0)
#define MFMA32(a, b, c) __builtin_amdgcn_mfma_f32_32x32x16_bf16((a), (b), (c), 0, 0, 0)

template <class F, int... I> DI void static_for_impl(F&& f, std::integer_sequence<int, I...>) { (f(std::integral_constant<int, I>{}), ...); }
template <int N, class F> DI void static_for(F&& f) { static_for_impl(f, std::make_integer_sequence<int, N>{}); }

template <int CTRL, int RM = 0xf> DI float dppz(float v) { return __int_as_float(__builtin_amdgcn_update_dpp(0, __float_as_int(v), CTRL, RM, 0xf, true)); }
DI float wave_sum63(float v) {
    v += dppz<0x111>(v); v += dppz<0x112>(v); v += dppz<0x114>(v); v += dppz<0x118>(v);
    v += dppz<0x142, 0xa>(v); v += dppz<0x143, 0xc>(v);
    return v;
}
DI float wave_max63(float v) {
    v = fmaxf(v, dppz<0x111>(v)); v = fmaxf(v, dppz<0x112>(v)); v = fmaxf(v, dppz<0x114>(v)); v = fmaxf(v, dppz<0x118>(v));
    v = fmaxf(v, dppz<0x142, 0xa>(v)); v = fmaxf(v, dppz<0x143, 0xc>(v));
    return v;
}
DI float half_sum2(float a, float b) {
    auto r = __builtin_amdgcn_permlane32_swap(__float_as_uint(a), __float_as_uint(b), false, false);
    return __uint_as_float(r[0]) + __uint_as_float(r[1]);
}
DI float row_pair_sum(float a) {
    auto r = __builtin_amdgcn_permlane16_swap(__float_as_uint(a), __float_as_uint(a), false, false);
    return __uint_as_float(r[0]) + __uint_as_float(r[1]);
}
DI void ln_scalars(float S, float Q, float& mb, float& sb) {
    const float mean = S * (1.f / DM);
    const float var = fmaxf(__builtin_fmaf(-mean, mean, Q * (1.f / DM)), 0.f);
    const u32 mv = pk_bf16(mean, var);
    mb = bflo(mv);
    sb = rbf(__builtin_amdgcn_sqrtf(rbf(bfhi(mv) + 1e-5f)));
}

struct cm { f32x2 b, r; };
DI cm mk(f32x2 b) { return {b, f32x2{-b[1], b[0]}}; }
DI f32x2 cmul(f32x2 a, const cm& m) {
    return __builtin_elementwise_fma(__builtin_shufflevector(a, a, 1, 1), m.r, __builtin_shufflevector(a, a, 0, 0) * m.b);
}
DI f32x2 cexp_p(float arl, float aiv, float p) {
    const float mag = __builtin_amdgcn_exp2f(arl * p);
    const float th = __builtin_amdgcn_fractf(aiv * p);
    return f32x2{mag * __builtin_amdgcn_cosf(th), mag * __builtin_amdgcn_sinf(th)};
}
DI float exp_bf(float x) {
    const float t = x * 1.44269504088896341f;
    const float r = __builtin_fmaf(x, 1.44269504088896341f, -t) + x * 1.92596299e-8f;
    const float e = __builtin_amdgcn_exp2f(t);
    return rbf(__builtin_fmaf(e * r, 0.693147181f, e));
}

constexpr int KG_ROW = 528;
constexpr int KG_TROW = 80;
constexpr int KG_B = 21 * KG_ROW, KG_TAB = 53 * KG_ROW, PREP_LDS = KG_TAB + 32 * KG_TROW;
constexpr int PB_WARM = 16, PB_KGEN = 512, PB_TR = 128, PB_ST = 128;
constexpr int PREP_BLOCKS = PB_WARM + PB_KGEN + PB_TR + PB_ST;

extern "C" __global__ __launch_bounds__(256, 2) void s4_prep_kernel(
    const u16* __restrict__ x0, const u16* __restrict__ logA, const u16* __restrict__ Aim,
    const u16* __restrict__ Cre, const u16* __restrict__ Cim, const u16* __restrict__ logdt, const u16* __restrict__ Dskip) {
    char* ws = g_ws;
    extern __shared__ __attribute__((aligned(16))) char smem_raw[];
    LAS char* L = (LAS char*)smem_raw;
    {
        unsigned long long pc;
        u32 off = (((blockIdx.x >> 3) << 8) + threadIdx.x) * 128u;
        asm volatile("s_cmp_ge_u32 %2, %3\n\ts_cbranch_scc1 2f\n\ts_getpc_b64 %0\n1:\n\t"
                     "v_min_u32 %1, s4_tail_kernel-1b, %1\n\tglobal_load_dword %1, %1, %0\n\ts_waitcnt vmcnt(0)\n\ts_endpgm\n2:"
                     : "=&s"(pc), "+v"(off) : "s"(blockIdx.x), "n"(PB_WARM) : "scc", "memory");
    }
    asm volatile("" ::"s"(x0), "s"(logA), "s"(Aim), "s"(Cre), "s"(Cim), "s"(logdt), "s"(Dskip));
    const rsrc_t WS = make_rsrc(ws);
    const int tid = threadIdx.x, lane = tid & 63;
    const int wave = __builtin_amdgcn_readfirstlane(tid >> 6);
    int b = blockIdx.x - PB_WARM;
    if (__builtin_expect(b >= PB_KGEN, 0)) {
        b -= PB_KGEN;
        if (b < PB_TR) {
            const int w = b * 4 + wave, tau = w >> 6, lg = (w >> 4) & 3, cgg = w & 15;
            const int ln = lg * 16 + (lane & 15), cg = cgg * 4 + (lane >> 4);
            u32x4 in[4];
#pragma unroll
            for (int g = 0; g < 4; ++g) in[g] = *(const u32x4*)(x0 + (256 * tau + 64 * g + ln) * DM + 8 * cg);
            const u32 dst = 2 * xt_off(8 * cg, 256 * tau + 4 * ln);
#pragma unroll
            for (int q = 0; q < 4; ++q) {
                st8(WS, dst + (2 * q) * 256, WS_X, u32x2{perm_lo(in[1][q], in[0][q]), perm_lo(in[3][q], in[2][q])});
                st8(WS, dst + (2 * q + 1) * 256, WS_X, u32x2{perm_hi(in[1][q], in[0][q]), perm_hi(in[3][q], in[2][q])});
            }
            return;
        }
        b -= PB_TR;
        {
            const int w = b * 4 + wave, tau = w >> 6, ln = w & 63;
            u32x4 in[4];
#pragma unroll
            for (int g = 0; g < 4; ++g) in[g] = *(const u32x4*)(x0 + (256 * tau + 64 * g + ln) * DM + 8 * lane);
            if ((b * 256 + tid) * 16 < ZERO_BYTES) st16(WS, (b * 256 + tid) * 16, WS_ZERO, u32x4{0u, 0u, 0u, 0u});
            if (b >= 96) st16(WS, ((b - 96) * 256 + tid) * 16, WS_W1 + NU * 32 * 1024, u32x4{0u, 0u, 0u, 0u});
#pragma unroll
            for (int g = 0; g < 4; ++g) {
                float S = 0.f, Q = 0.f;
#pragma unroll
                for (int q = 0; q < 4; ++q) { S = dot2(in[g][q], ONES, S); Q = dot2(in[g][q], in[g][q], Q); }
                S = wave_sum63(S); Q = wave_sum63(Q);
                const int pos = 256 * tau + 4 * ln + g;
                const u32 dst = lane == 63 ? 16 * st_slot(st_thread(pos), st_item(pos), 8) : 0x80000000u;
                const u32x4 v = __builtin_bit_cast(u32x4, f64x2{(double)S, (double)Q});
                st16(WS, dst, WS_ST64, v);
            }
            return;
        }
    }
    {
        const int d = b, n = tid & 127;
        u32 h_dt = logdt[d], h_D = Dskip[d], h_la = logA[d * NST + n], h_ai = Aim[d * NST + n];
        u32 h_cr = Cre[d * NST + n], h_ci = Cim[d * NST + n];
        u32 ca = ((const u32*)(Cre + d * NST))[lane], cc = ((const u32*)(Cim + d * NST))[lane];
        const int rb = wave & 1, cb = wave >> 1, q = lane >> 4;
        const u32 a_pa = pinned((16 * rb + (lane & 15)) * KG_ROW + q * 16);
        const u32 a_pb = pinned(KG_B + (16 * cb + (lane & 15)) * KG_ROW + q * 16);
        const u32 a_tw = pinned(KG_TAB + (16 * cb + (lane & 15)) * KG_TROW + 32 * rb + 8 * q);
        const int S = tid >> 5, col = tid & 31;
        auto row0 = [](int sp) { return min(min(8 * sp, 8 + 2 * sp), 12 + sp); };
        const u32 a_t0 = pinned(KG_TAB + col * KG_TROW);
        const u32 a_ta = pinned(a_t0 + 2 * (row0(S) & ~1));
        const u32 a_tb = pinned(a_ta + (S == 1 ? 4 : 0));
        const u32 a_tn = pinned(KG_TAB + ((tid + 1) & 31) * KG_TROW + 2 * row0((tid + 1) >> 5));
        const u32 t0s = (S >= 4 && (S & 1)) ? 0x0302u : 0x0100u;
        const u32 s0 = pinned(t0s * 0x10001u);
        const u32 s1 = pinned(S == 1 ? 0x05040302u : S < 4 ? 0x07060100u : s0);
        const u32 s2 = pinned(S == 1 ? 0x01000100u : S < 4 ? 0x03020302u : (tid & 1) ? 0x05040504u : 0x05040000u | t0s);
        const u32 s3 = pinned(S == 1 ? 0x05040302u : 0x05040504u);
        asm volatile("" : "+v"(h_dt), "+v"(h_D), "+v"(h_la), "+v"(h_ai), "+v"(h_cr), "+v"(h_ci), "+v"(ca), "+v"(cc));
        const float dt = exp_bf(bflo(h_dt));
        const float arl = -exp_bf(bflo(h_la)) * dt * 1.44269504088896341f;
        float aiv = bflo(h_ai) * dt * 0.159154943091895336f;
        const float mx = wave_max63(fmaxf(fmaxf(fabsf(bflo(ca)), fabsf(bfhi(ca))), fmaxf(fabsf(bflo(cc)), fabsf(bfhi(cc)))));
        const u32 eb = min(max((u32)__builtin_amdgcn_readlane(__float_as_int(mx), 63) & 0x7f800000u, 32u << 23), 224u << 23);
        const float sc = __uint_as_float((261u << 23) - eb);
        const float back = dt * (__uint_as_float(eb) * (2.f / 32768.f));
        if (wave < 2) {
            aiv = -aiv;
            const f32x2 t0 = {bflo(h_cr) * sc, -bflo(h_ci) * sc};
            const cm w1 = mk(cexp_p(arl, aiv, 1.f)), v1 = mk(cexp_p(arl, aiv, 256.f));
            const cm w2 = mk(cmul(w1.b, w1)), w4 = mk(cmul(w2.b, w2)), v2 = mk(cmul(v1.b, v1)), v4 = mk(cmul(v2.b, v2));
            LAS u32* A = (LAS u32*)(L + 4 * n);
            auto put = [&](int row, f32x2 z) { A[row * (KG_ROW / 4)] = pk_f16(z); };
            const f32x2 r1 = cmul(t0, w1), r2 = cmul(t0, w2), r3 = cmul(r1, w2);
            const f32x2 t1 = cmul(t0, v1), t2 = cmul(t0, v2), t3 = cmul(t1, v2), t4 = cmul(t0, v4), r9 = cmul(t1, w2);
            put(0, t0); put(1, r1); put(2, r2); put(3, r3);
            put(4, cmul(t0, w4)); put(5, cmul(r1, w4)); put(6, cmul(r2, w4)); put(7, cmul(r3, w4));
            put(8, t1); put(9, r9); put(10, cmul(t1, w4)); put(11, cmul(r9, w4));
            put(12, t2); put(13, cmul(t2, w4)); put(14, t3); put(15, cmul(t3, w4));
            put(16, t4); put(17, cmul(t1, v4)); put(18, cmul(t2, v4)); put(19, cmul(t3, v4)); put(20, cmul(t4, v4));
        } else {
            f32x2 e[16];
            const f32x2 b1 = cexp_p(arl, aiv, 8.f);
            const cm b8 = mk(cexp_p(arl, aiv, 64.f)), b2 = mk(cmul(b1, mk(b1))), b4 = mk(cmul(b2.b, b2)), b16 = mk(cmul(b8.b, b8));
            LAS u32* B = (LAS u32*)(L + KG_B + 4 * n);
            e[0] = f32x2{256.f, 0.f}; e[1] = b1 * 256.f; e[2] = b2.b * 256.f; e[3] = cmul(e[1], b2);
            e[4] = b4.b * 256.f; e[8] = b8.b * 256.f;
#pragma unroll
            for (int c = 1; c < 4; ++c) e[4 + c] = cmul(e[c], b4);
#pragma unroll
            for (int c = 1; c < 8; ++c) e[8 + c] = cmul(e[c], b8);
#pragma unroll
            for (int c = 0; c < 16; ++c) {
                B[c * (KG_ROW / 4)] = pk_f16(e[c]);
                B[(16 + c) * (KG_ROW / 4)] = pk_f16(c ? cmul(e[c], b16) : b16.b * 256.f);
            }
        }
        __syncthreads();
        {
            f16x8 fa[8], fb[8];
#pragma unroll
            for (int ks = 0; ks < 8; ++ks) { fa[ks] = *(LAS f16x8*)(L + a_pa + 64 * ks); fb[ks] = *(LAS f16x8*)(L + a_pb + 64 * ks); }
            sched_fence();
            f32x4 acc = {0.f, 0.f, 0.f, 0.f}, acc2 = {0.f, 0.f, 0.f, 0.f};
#pragma unroll
            for (int ks = 0; ks < 8; ks += 2) {
                acc = __builtin_amdgcn_mfma_f32_16x16x32_f16(fa[ks], fb[ks], acc, 0, 0, 0);
                acc2 = __builtin_amdgcn_mfma_f32_16x16x32_f16(fa[ks + 1], fb[ks + 1], acc2, 0, 0, 0);
            }
            acc += acc2;
            const float add0 = tid == 0 ? bflo(h_D) : 0.f;
            *(LAS u32x2*)(L + a_tw) = u32x2{pk_bf16(__builtin_fmaf(acc[0], back, add0), acc[1] * back), pk_bf16(acc[2] * back, acc[3] * back)};
        }
        __syncthreads();
        {
            const u32x4 w = *(LAS u32x4*)(L + a_t0);
            const u32 ta = *(LAS u32*)(L + a_ta), tb = *(LAS u32*)(L + a_tb), tn = *(LAS u16*)(L + a_tn);
            const u32x4 o = {__builtin_amdgcn_perm(ta, ta, s0), __builtin_amdgcn_perm(tb, ta, s1),
                             __builtin_amdgcn_perm(tn, tb, s2), __builtin_amdgcn_perm(tn, tb, s3)};
            st16(WS, 16 * tid, WS_K + d * SEQ * 2, S == 0 ? w : o);
        }
    }
}

DI bool rt_is_w1(int t) { return t < 168 || t >= 256; }
DI void retile_load(LAS char* L, const u16* Wg, const u16* Wu, const u16* Wd, int t, int wave, int lane) {
    const u32 lb = (u32)(uintptr_t)L + wave * 4096;
    const bool w1 = rt_is_w1(t);
    const int ct = w1 ? (t < 168 ? t >> 3 : 21) : (t & 7), rt = w1 ? (t & 3) : (t - 168) >> 3, pitch = w1 ? FF * 2 : DM * 2;
    const char* W = (const char*)(w1 ? ((t & 4) ? Wu : Wg) : Wd);
    const int r0 = 128 * rt + 32 * wave + (lane >> 3), co = ct * 128 + (((lane & 7) * 16) & (t >= 256 ? 63 : 127));
#pragma unroll
    for (int i = 0; i < 4; ++i) glds_s<1>(W, min(r0 + 8 * i, FF - 1) * pitch + co, lb + i * 1024);
}
DI void retile_store(LAS char* L, char* ws, int t, int tid, int wave, int lane) {
    const rsrc_t WS = make_rsrc(ws);
    const u32 lb = (u32)(uintptr_t)L + wave * 4096;
    const int g = lane >> 4, q = (lane & 15) >> 2, pp = lane & 3;
    u32x2 f[8];
    if (rt_is_w1(t)) {
        const int ct = t < 168 ? t >> 3 : 21, ty = (t >> 2) & 1, rt = t & 3;
        const u32 a = lb + (8 * g + q) * 128 + pp * 8;
        asm volatile("ds_read_b64_tr_b16 %0, %8\n\tds_read_b64_tr_b16 %1, %8 offset:512\n\t"
                     "ds_read_b64_tr_b16 %2, %8 offset:32\n\tds_read_b64_tr_b16 %3, %8 offset:544\n\t"
                     "ds_read_b64_tr_b16 %4, %8 offset:64\n\tds_read_b64_tr_b16 %5, %8 offset:576\n\t"
                     "ds_read_b64_tr_b16 %6, %8 offset:96\n\tds_read_b64_tr_b16 %7, %8 offset:608\n\ts_waitcnt lgkmcnt(0)"
                     : "=&v"(f[0]), "=&v"(f[1]), "=&v"(f[2]), "=&v"(f[3]), "=&v"(f[4]), "=&v"(f[5]), "=&v"(f[6]), "=&v"(f[7])
                     : "v"(a) : "memory");
        LAS float* red = (LAS float*)(L + 32768);
#pragma unroll
        for (int i = 0; i < 4; ++i) {
            const int u = 4 * ct + i;
            if (u < NU) st16(WS, lane * 16, WS_W1 + ((u * 16 + 4 * rt + wave) * 2 + ty) * 1024, u32x4{f[2 * i][0], f[2 * i][1], f[2 * i + 1][0], f[2 * i + 1][1]});
        }
#pragma unroll
        for (int i = 0; i < 4; i += 2) {
            const float s0 = dot2(f[2 * i][0], ONES, dot2(f[2 * i][1], ONES, dot2(f[2 * i + 1][0], ONES, dot2(f[2 * i + 1][1], ONES, 0.f))));
            const float s1 = dot2(f[2 * i + 2][0], ONES, dot2(f[2 * i + 2][1], ONES, dot2(f[2 * i + 3][0], ONES, dot2(f[2 * i + 3][1], ONES, 0.f))));
            const float v = row_pair_sum(half_sum2(s0, s1));
            if ((lane & 16) == 0) red[wave * 64 + (i + (lane >> 5)) * 16 + (lane & 15)] = v;
        }
        wait_lgkm0();
        raw_barrier();
        if (tid < 64 && 4 * ct + (tid >> 4) < NU)
            ((float*)(ws + WS_CSP))[((rt * 90 + 4 * ct + (tid >> 4)) * 2 + ty) * 16 + (tid & 15)] =
                (red[tid] + red[64 + tid]) + (red[128 + tid] + red[192 + tid]);
    } else {
        const int rt = (t - 168) >> 3, ct = t & 7;
        const u32 a = lb + (8 * (g >> 1) + q) * 128 + pi_fwd(16 * (g & 1) + 4 * pp) * 2;
        asm volatile("ds_read_b64_tr_b16 %0, %8\n\tds_read_b64_tr_b16 %1, %8 offset:512\n\t"
                     "ds_read_b64_tr_b16 %2, %8 offset:64\n\tds_read_b64_tr_b16 %3, %8 offset:576\n\t"
                     "ds_read_b64_tr_b16 %4, %8 offset:2048\n\tds_read_b64_tr_b16 %5, %8 offset:2560\n\t"
                     "ds_read_b64_tr_b16 %6, %8 offset:2112\n\tds_read_b64_tr_b16 %7, %8 offset:2624\n\ts_waitcnt lgkmcnt(0)"
                     : "=&v"(f[0]), "=&v"(f[1]), "=&v"(f[2]), "=&v"(f[3]), "=&v"(f[4]), "=&v"(f[5]), "=&v"(f[6]), "=&v"(f[7])
                     : "v"(a) : "memory");
#pragma unroll
        for (int i = 0; i < 4; ++i) {
            const int u = 8 * rt + 2 * wave + (i >> 1), cb = 2 * ct + (i & 1);
            if (u < NU) st16(WS, lane * 16, WS_W2 + (cb * NU + u) * 1024, u32x4{f[2 * i][0], f[2 * i][1], f[2 * i + 1][0], f[2 * i + 1][1]});
        }
    }
}

constexpr int CV_KROW = (64 + SEQ) * 2;
constexpr int CV_ZRS = 84 * 16;
constexpr int CV_Z = 2 * CV_KROW;
constexpr int CV_C = CV_Z + 2 * 4 * CV_ZRS;
constexpr int CV_DUMP = CV_C + 2 * 9 * 64;
constexpr int CV_X = CV_DUMP + 1024 + 9 * 64;
constexpr int CV_S = CV_X + 2 * SEQ * 2;
constexpr int CV_LDS = CV_S + SEQ * 16;
constexpr int PF_SLEEP = 40;

template <int HALF> struct TauSet { static constexpr int t(int i) { return HALF == 0 ? (i == 0 ? 0 : i == 1 ? 2 : i == 2 ? 5 : 7) : (i == 0 ? 1 : i == 1 ? 3 : i == 2 ? 4 : 6); } };

template <int HALF>
DI void conv_main(LAS char* L, rsrc_t xout, u32 xoff, u32 a_z, u32 a_k, u32 a_cw, u32 a_cr, u32 a_x, const bf16x8 (&kf0)[8]) {
    using TS = TauSet<HALF>;
    constexpr int TMAX = TS::t(3);
    f32x16 acc[4];
#pragma unroll
    for (int i = 0; i < 4; ++i)
#pragma unroll
        for (int r = 0; r < 16; ++r) acc[i][r] = 0.f;
    LAS char* zb = L + a_z;
    LAS char* kb = L + a_k;
    u32x2 xm[4];
    bf16x8 kf[2][TMAX + 1], zf[2][TMAX + 1];
    auto load = [&](auto EI) {
        constexpr int ei = EI, e = 2 * ei + 1;
#pragma unroll
        for (int m = 0; m <= TMAX; ++m) {
            if constexpr (ei == 0) kf[0][m] = kf0[m];
            else kf[ei & 1][m] = tr_frag(kb + 32 + 64 * (8 * m + e), kb + 64 * (8 * m + e));
            zf[ei & 1][m] = *(LAS bf16x8*)(zb + 16 * (8 * m + 7 - e));
            if constexpr (ei == 0) sched_fence();
        }
    };
    load(std::integral_constant<int, 0>{});
    static_for<3>([&](auto EI) {
        constexpr int ei = EI;
        sched_fence();
        load(std::integral_constant<int, ei + 1>{});
        if constexpr (ei == 2) {
#pragma unroll
            for (int i = 0; i < 4; ++i) xm[i] = *(LAS u32x2*)(L + a_x + 512 * TS::t(i));
        }
#pragma unroll
        for (int i = 0; i < 4; ++i)
#pragma unroll
            for (int mu = 0; mu <= TS::t(i); ++mu) acc[i] = MFMA32(zf[ei & 1][TS::t(i) - mu], kf[ei & 1][mu], acc[i]);
        if constexpr (ei == 0) __builtin_amdgcn_sched_group_barrier(0x8, 2, 0);
#pragma unroll
        for (int k = 0; k < 13; ++k) { __builtin_amdgcn_sched_group_barrier(0x8, 1, 0); __builtin_amdgcn_sched_group_barrier(0x100, 2, 0); }
        PIN16(acc[0]); PIN16(acc[1]); PIN16(acc[2]); PIN16(acc[3]);
    });
    sched_fence();
    u32 a_c[4];
#pragma unroll
    for (int i = 0; i < 4; ++i) a_c[i] = pinned(a_cr ? a_cr + 64 * TS::t(i) : 0u);
    float y[4][4];
    auto post = [&](auto II) {
        constexpr int i = II;
        float s1[4], s2[4], v[4];
#pragma unroll
        for (int g = 0; g < 4; ++g) {
            v[g] = opaque(acc[i][4 * g + 2] + dppz<0x130>(acc[i][4 * g + 1]));
            s2[g] = opaque(acc[i][4 * g + 2] + dppz<0x138>(acc[i][4 * g + 3]));
        }
#pragma unroll
        for (int g = 0; g < 4; ++g) v[g] = opaque(acc[i][4 * g + 3] + dppz<0x130>(v[g]));
        *(LAS f32x4*)(L + a_cw + 64 * TS::t(i)) = f32x4{v[0], v[1], v[2], v[3]};
#pragma unroll
        for (int g = 0; g < 4; ++g) {
            s1[g] = opaque(acc[i][4 * g + 1] + dppz<0x138>(s2[g]));
            y[i][g] = opaque(acc[i][4 * g] + ((g & 1) ? bfhi(xm[i][g >> 1]) : bflo(xm[i][g >> 1])));
        }
#pragma unroll
        for (int g = 0; g < 4; ++g) y[i][g] = opaque(y[i][g] + dppz<0x138>(s1[g]));
    };
    static_for<4>([&](auto II) {
        constexpr int i = II;
#pragma unroll
        for (int mu = 0; mu <= TS::t(i); ++mu) acc[i] = MFMA32(zf[1][TS::t(i) - mu], kf[1][mu], acc[i]);
        if constexpr (i > 0) {
            post(std::integral_constant<int, i - 1>{});
            __builtin_amdgcn_sched_group_barrier(0x8, 2, 0);
#pragma unroll
            for (int k = 2; k <= TS::t(i); ++k) { __builtin_amdgcn_sched_group_barrier(0x202, 6, 0); __builtin_amdgcn_sched_group_barrier(0x8, 1, 0); }
        }
        PIN16(acc[i]);
        sched_fence();
    });
    post(std::integral_constant<int, 3>{});
    wait_lgkm0();
    raw_barrier();
    f32x4 cv[4];
    float cp[4];
#pragma unroll
    for (int i = 0; i < 4; ++i) {
        cv[i] = *(LAS f32x4*)(L + a_c[i] + 64);
        cp[i] = *(LAS float*)(L + a_c[i] + 12);
    }
    static_for<4>([&](auto II) {
        constexpr int i = II;
        st8(xout, xoff, 4 * TS::t(i) * XTILE, u32x2{pk_bf16(opaque(TS::t(i) ? y[i][0] + cp[i] : y[i][0]), opaque(y[i][1] + cv[i][0])),
                                                    pk_bf16(opaque(y[i][2] + cv[i][1]), opaque(y[i][3] + cv[i][2]))});
    });
}

extern "C" __global__ __launch_bounds__(256, 2) void s4_conv_kernel(const u16* Wg, const u16* Wu, const u16* Wd) {
    char* ws = g_ws;
    extern __shared__ __attribute__((aligned(16))) char smem_raw[];
    LAS char* L = (LAS char*)smem_raw;
    const int layer = LAYER_;
    const int tid = TID_, lane = tid & 63, wave = __builtin_amdgcn_readfirstlane(tid >> 6);
    if (__builtin_expect(blockIdx.x >= 256, 0)) {
        if (layer == 0) {
            const int e = blockIdx.x - 256;
            const bool two = e >= 248;
            retile_load(L, Wg, Wu, Wd, e, wave, lane);
            if (two) retile_load(L + 16384, Wg, Wu, Wd, e + 8, wave, lane);
            wait_vm<0>();
            retile_store(L, ws, e, tid, wave, lane);
            if (two) retile_store(L + 16384, ws, e + 8, tid, wave, lane);
            return;
        }
        const int xcd = blockIdx.x & 7, sl = ((blockIdx.x - 256) >> 3) * 4 + wave;
        constexpr int P1 = (96 / (8 / TG1)) * 32, N1 = (P1 + 127) / 128;
        const int k1 = (xcd / TG1) * P1 + sl * N1;
        const u32 dst = (u32)(uintptr_t)L + wave * 1024;
        __builtin_amdgcn_s_sleep(PF_SLEEP);
#pragma unroll 1
        for (int i = 0; i < N1; ++i) glds_s<1>(ws + WS_W1 + (size_t)min(k1 + i, 90 * 32 - 1) * 1024, lane * 16, dst);
        wait_vm<0>();
        return;
    }
    const u16* xin = (const u16*)(ws + WS_X + 2 * layer * WS_XSTEP);
    const rsrc_t xout = make_rsrc(ws + WS_X + (2 * layer + 1) * WS_XSTEP);
    const int ch = wave >> 1, half = wave & 1, d0 = 2 * blockIdx.x, d = d0 + ch;
    const int tau = tid >> 5, c = tid & 3, sp = (tid >> 2) & 7;
    const int p0 = 256 * tau + 32 * sp + 4 * c;
    const u32 l0 = (u32)(uintptr_t)L;
    {
        const int rot = (blockIdx.x >> 3) & 3;
        const char* sb = ws + WS_ST64 + layer * (SEQ * 16) + wave * 8192;
#pragma unroll
        for (int j = 0; j < 4; ++j) {
            const int k = (j + rot) & 3;
            glds_s<2>(sb + k * 2048, lane * 16, l0 + CV_S + wave * 8192 + k * 2048);
        }
    }
    glds_s<2>(ws + WS_K + (size_t)(d * SEQ + half * 1024) * 2, lane * 16, l0 + ch * CV_KROW + 128 + half * 2048);
    {
        const char* xb = (const char*)(xin + 4 * wave * XTILE + d0 * 128);
        const u32 vo = (lane >> 4) * (XTILE * 2) + (lane & 15) * 16;
        glds_s<1>(xb, vo, l0 + CV_X + wave * 1024);
        glds_s<1>(xb + 256, vo, l0 + CV_X + 4096 + wave * 1024);
    }
    const u32 xoff = 2 * xt_off(d, 4 * lane);
    if (wave == 0) *(LAS u32x4*)(L + CV_Z + (lane >> 3) * CV_ZRS + 16 * (lane & 7)) = u32x4{0u, 0u, 0u, 0u};
    if (wave == 1) *(LAS u32x4*)(L + ((lane >> 3) & 1) * CV_KROW + 16 * (lane & 7)) = u32x4{0u, 0u, 0u, 0u};
    const int hh = lane >> 5, cbase = CV_C + ch * 9 * 64;
    const u32 a_z = pinned(CV_Z + (ch * 4 + (lane & 3)) * CV_ZRS + 128 + 16 * (((lane & 31) >> 2) + hh) - 16 * 7);
    const u32 a_k = pinned(ch * CV_KROW + 128 + 2 * (16 * ((lane >> 4) & 1) - 32 * hh - 4 * ((lane & 15) >> 2) + 4 * (lane & 3)) - 32);
    const u32 a_cw = pinned((lane >= 61 ? cbase + 64 - 61 * 16 : CV_DUMP) + lane * 16);
    const u32 a_cr = pinned(lane < 3 ? cbase + 16 * lane : 0);
    const u32 a_x = pinned(CV_X + ch * 4096 + 8 * lane);
    f32x2 rc[2][4];
    LAS char* sl = L + CV_S + wave * 8192 + lane * 16;
    wait_vm<4>();
#pragma unroll
    for (int j = 0; j < 4; ++j) {
        float nm[2], rs[2];
#pragma unroll
        for (int s = 0; s < 2; ++s) {
            const f64x2 sq = *(LAS f64x2*)(sl + (4 * s + j) * 1024);
            nm[s] = (float)sq[0] * (-1.f / DM);
            rs[s] = __builtin_amdgcn_rsqf(fmaxf(__builtin_fmaf(-nm[s], nm[s], (float)sq[1] * (1.f / DM)), 0.f) + 1e-5f);
        }
        const u32 mb = pk_bf16(nm[0], nm[1]);
        rc[0][j] = f32x2{rs[0], bflo(mb) * rs[0]};
        rc[1][j] = f32x2{rs[1], bfhi(mb) * rs[1]};
    }
    asm volatile("" : "+v"(rc[0][0]), "+v"(rc[0][1]), "+v"(rc[0][2]), "+v"(rc[0][3]), "+v"(rc[1][0]), "+v"(rc[1][1]), "+v"(rc[1][2]), "+v"(rc[1][3]));
    wait_vm<2>();
    raw_barrier();
    bf16x8 kf0[8];
#pragma unroll
    for (int m = 0; m < 8; ++m) kf0[m] = tr_frag(L + a_k + 32 + 64 * (8 * m + 1), L + a_k + 64 * (8 * m + 1));
    wait_vm<0>();
    u32x2 xz[2][2];
#pragma unroll
    for (int k = 0; k < 2; ++k)
#pragma unroll
        for (int s = 0; s < 2; ++s) xz[k][s] = *(LAS u32x2*)(L + CV_X + k * 4096 + (p0 + 16 * s) * 2);
    {
        LAS char* zw = L + CV_Z + c * CV_ZRS + 128 + 2 * (64 * tau + 2 * sp);
#pragma unroll
        for (int k = 0; k < 2; ++k)
#pragma unroll
            for (int g = 0; g < 4; ++g) {
                const float a = (g & 1) ? bfhi(xz[k][0][g >> 1]) : bflo(xz[k][0][g >> 1]);
                const float e = (g & 1) ? bfhi(xz[k][1][g >> 1]) : bflo(xz[k][1][g >> 1]);
                *(LAS u32*)(zw + k * 4 * CV_ZRS + 32 * g) =
                    pk_bf16(__builtin_fmaf(a, rc[0][g][0], rc[0][g][1]), __builtin_fmaf(e, rc[1][g][0], rc[1][g][1]));
            }
    }
    asm volatile("s_waitcnt vmcnt(0) lgkmcnt(0)" ::: "memory");
    raw_barrier();
    if (half) conv_main<1>(L, xout, xoff, a_z, a_k, a_cw, a_cr, a_x, kf0); else conv_main<0>(L, xout, xoff, a_z, a_k, a_cw, a_cr, a_x, kf0);
}

constexpr int G1_R = 6;
constexpr int RAMP2 = 4;
constexpr int W2_TAIL = 6;
constexpr int G1_NS = 16, G1_SLOT = 20480;
constexpr int G2_NS = 11, G2_R = 7, G2_SLOT = 16384, KH = NU / 2;
constexpr int ML_RED = 131072, ML_OS = ML_RED, ML_XS = ML_RED + 8192, ML_EX = ML_XS + 8192, ML_LDS = ML_EX + 16384;
constexpr int ML_CTR = ML_EX;

constexpr int G1_LEAD = G1_R - 2;
constexpr int g1_nis(int s) { int a = 2 * s + 2, b = s + G1_LEAD; a = a < b ? a : b; return a < G1_NS ? a : G1_NS; }
constexpr int g1_wlo(int s) {
    int a = s <= G1_NS - G1_LEAD ? 0 : W2_TAIL * (s - (G1_NS - G1_LEAD));
    return a < KH ? a : KH;
}
constexpr int g1_younger(int s) {
    int t = -1;
    while (g1_nis(t + 1) <= s) ++t;
    return 5 * (g1_nis(s) - 1 - s) + g1_wlo(s) - g1_wlo(t < 0 ? 0 : t);
}

extern "C" __global__ __launch_bounds__(512, 2) void s4_mlp_kernel(u16* out) {
    char* ws = g_ws;
    extern __shared__ __attribute__((aligned(16))) char smem_raw[];
    LAS char* L = (LAS char*)smem_raw;
    const int layer = LAYER_, tid = TID_;
    const u16* xT = (const u16*)(ws + WS_X + (2 * layer + 1) * WS_XSTEP);
    const int lane = tid & 63, wave = __builtin_amdgcn_readfirstlane(tid >> 6);
    const int xcd = blockIdx.x & 7, sl = blockIdx.x >> 3;
    constexpr int MPX = 16 / TG1;
    const int mt = (xcd % TG1) * MPX + (sl % MPX), fg = (xcd / TG1) * (32 / MPX) + (sl / MPX);
    const int m2 = 2 * mt + (fg & 1), nt = fg >> 1;
    auto blk_unit = [&](int s, int kh) { return KH * kh + 4 * s; };
    const u32 l0 = (u32)(uintptr_t)L;
    const int g = lane >> 4, q = (lane & 15) >> 2;
    auto xfrag_off = [&](int tb) { return (8 * g + q) * 256 + ((tb ^ (q | ((g & 1) << 2))) << 5) + (lane & 3) * 8; };
    auto xsrc_off = [&](int odd) { return (u32)(g * 256 + ((((lane & 15) >> 1) ^ (g | (odd << 2))) << 5) + (lane & 1) * 16); };
    const char* xtile = (const char*)(xT + mt * XTILE);
    glds_s<1>(xtile + wave * 1024, xsrc_off((wave >> 1) & 1), l0 + wave * 1024);
    const int u0 = fg * 6;
    using I0 = std::integral_constant<int, 0>; using I1 = std::integral_constant<int, 1>;

    if (__builtin_expect(fg == 15, 0)) {
        const u32 mine = l0 + wave * 16384;
        const u32 so0 = xsrc_off(0), so1 = xsrc_off(1);
#pragma unroll
        for (int i = 0; i < 8; ++i) glds_s<2>(xtile + (16 * wave + 2 * i) * 1024, (i & 1) ? so1 : so0, mine + i * 2048);
        float S[8], Q[8];
        u32 a[8];
#pragma unroll
        for (int tb = 0; tb < 8; ++tb) { a[tb] = mine + xfrag_off(tb); S[tb] = 0.f; Q[tb] = 0.f; }
        static_for<4>([&](auto KI) {
            constexpr int kk = KI >> 1, tq = (KI & 1) * 4;
            if constexpr (tq == 0) wait_vm<8 - 8 * kk>();
            u32x2 f[8];
            asm volatile("ds_read_b64_tr_b16 %0, %8 offset:%c12\n\tds_read_b64_tr_b16 %1, %8 offset:%c13\n\t"
                         "ds_read_b64_tr_b16 %2, %9 offset:%c12\n\tds_read_b64_tr_b16 %3, %9 offset:%c13\n\t"
                         "ds_read_b64_tr_b16 %4, %10 offset:%c12\n\tds_read_b64_tr_b16 %5, %10 offset:%c13\n\t"
                         "ds_read_b64_tr_b16 %6, %11 offset:%c12\n\tds_read_b64_tr_b16 %7, %11 offset:%c13\n\ts_waitcnt lgkmcnt(0)"
                         : "=&v"(f[0]), "=&v"(f[1]), "=&v"(f[2]), "=&v"(f[3]), "=&v"(f[4]), "=&v"(f[5]), "=&v"(f[6]), "=&v"(f[7])
                         : "v"(a[tq]), "v"(a[tq + 1]), "v"(a[tq + 2]), "v"(a[tq + 3]), "i"(kk * 8192), "i"(kk * 8192 + 1024) : "memory");
#pragma unroll
            for (int j = 0; j < 4; ++j)
#pragma unroll
                for (int tb = 0; tb < 4; ++tb) {
                    const u32 w = f[2 * tb + (j >> 1)][j & 1];
                    S[tq + tb] = dot2(w, ONES, S[tq + tb]); Q[tq + tb] = dot2(w, w, Q[tq + tb]);
                }
        });
        LAS float* red = (LAS float*)(L + ML_RED);
        *(LAS u32*)(L + ML_CTR) = 0u;
#pragma unroll
        for (int tb = 0; tb < 8; ++tb) {
            const float v = row_pair_sum(half_sum2(S[tb], Q[tb]));
            if ((lane & 16) == 0) red[wave * 256 + (lane >> 5) * 128 + 16 * tb + (lane & 15)] = v;
        }
        wait_lgkm0();
        raw_barrier();
        if (tid < 128) {
            float s = 0.f, qq = 0.f;
#pragma unroll
            for (int w = 0; w < 8; ++w) { s += red[w * 256 + tid]; qq += red[w * 256 + 128 + tid]; }
            float mb, sb;
            ln_scalars(s, qq, mb, sb);
            __hip_atomic_store((u32*)(ws + WS_LNW) + layer * SEQ + mt * 128 + tid, pk_bf16(mb, sb), __ATOMIC_RELAXED, __HIP_MEMORY_SCOPE_AGENT);
        }
    }
    if (__builtin_expect(wave >= 4, 1)) {
        const int lw = wave - 4;
        const int wn = lw & 1, kh = lw >> 1, hh = lane >> 5;
        bf16x8 w2[KH];
        const rsrc_t WR = make_rsrc(ws + WS_W2 + (2 * nt + wn) * NU * 1024);
        auto ld_w2 = [&](auto JI) {
            constexpr int j = JI;
            w2[j] = __builtin_bit_cast(bf16x8, __builtin_amdgcn_raw_buffer_load_b128(WR, lane * 16 + (j & 3) * 1024, blk_unit(j >> 2, kh) * 1024, 0));
        };
        auto ld_res = [&]() {
            const char* xb = (const char*)(xT + mt * XTILE + (64 * nt + 32 * wn) * 128 + (m2 & 1) * 64 + 32 * kh);
            const u32 vo = (lane >> 2) * 256 + (((lane & 3) ^ ((lane >> 4) & 3)) << 4);
            glds_s<1>(xb, vo, l0 + ML_XS + lw * 2048);
            glds_s<1>(xb + 4096, vo, l0 + ML_XS + lw * 2048 + 1024);
        };
        if (__builtin_expect(fg != 15, 1)) {
            const u32 xo = xsrc_off(lw & 1), wo = lane * 16;
            const char* xs = xtile + 2 * lw * 1024;
            const int up = lw + (lw >> 1) + (lw & 1), us = 1 + 3 * (lw >> 1);
            const char* wp = ws + WS_W1 + (u0 + up) * 32 * 1024;
            const char* w1 = ws + WS_W1 + ((u0 + us) * 32 + (lw & 1)) * 1024;
            auto issue = [&](auto SI) {
                constexpr int s = SI;
                const u32 dst = l0 + (s % G1_R) * G1_SLOT;
                if constexpr (s > 0) glds_s<2>(xs + s * 8192, xo, dst + 2 * lw * 1024);
                glds_s<2>(wp + s * 2048, wo, dst + 8192 + up * 2048);
                glds_s<1>(w1 + s * 2048, wo, dst + 8192 + us * 2048 + (lw & 1) * 1024);
            };
            issue(I0{}); issue(I1{});
            static_for<G1_NS>([&](auto SI) {
                constexpr int s = SI, n = g1_nis(s), n2 = g1_nis(s + 1);
                wait_vm<g1_younger(s)>();
                raw_barrier();
                if constexpr (n2 > n) issue(std::integral_constant<int, n>{});
                if constexpr (n2 > n + 1) issue(std::integral_constant<int, n + 1>{});
                constexpr int lo = g1_wlo(s), hi = s == G1_NS - 1 ? KH : g1_wlo(s + 1);
                static_for<hi - lo>([&](auto JI) { ld_w2(std::integral_constant<int, lo + JI>{}); });
            });
        } else {
            static_for<KH>([&](auto JI) { ld_w2(JI); });
        }
        ld_res();
        asm volatile("" ::"s"(out));
        wait_vm<0>();
        const int item = pi_fwd(lane & 31), row = 32 * wn + item;
        f32x16 acc[2];
        {
            LAS char* xs = L + ML_XS + lw * 2048 + item * 64;
            const u32x4 a = *(LAS u32x4*)(xs + (((2 * hh) ^ ((item >> 2) & 3)) << 4));
            const u32x4 c = *(LAS u32x4*)(xs + (((2 * hh + 1) ^ ((item >> 2) & 3)) << 4));
#pragma unroll
            for (int p = 0; p < 4; ++p) {
                acc[0][2 * p] = bflo(a[p]); acc[0][2 * p + 1] = bfhi(a[p]);
                acc[0][8 + 2 * p] = bflo(c[p]); acc[0][9 + 2 * p] = bfhi(c[p]);
            }
#pragma unroll
            for (int r = 0; r < 16; ++r) acc[1][r] = 0.f;
            PIN16(acc[0]); PIN16(acc[1]);
        }
        const u32 a_ex = pinned(ML_EX + lw * 4096 + lane * 16);
        const u32 a_os = pinned(layer == 0 ? ML_OS + row * 128 + (((4 * kh + 2 * hh) ^ (row & 7)) << 4) : ML_OS + (32 * kh + 16 * hh) * 128 + row * 2);
        const int rr = 4 * (lane >> 4) + ((lane & 15) >> 2), spos = 64 * m2 + 16 * lw + (lane & 15);
        const u32 a_rp = pinned(ML_OS + rr * 128 + (((2 * lw + ((lane & 3) >> 1)) ^ (rr & 7)) << 4) + 8 * (lane & 1));
        const u32 a_st = pinned((lane & 16) ? 0x80000000u : 16 * st_slot(st_thread(spos), st_item(spos), 8) + 8 * hh);
        raw_barrier();
        LAS char* hk = L + kh * 8192 + kh * 4096 + lane * 16;
        LAS char* hs = L + (kh ^ 1) * 8192 + kh * 4096 + lane * 16;
        bf16x8 hf[2][4];
        auto rd2 = [&](auto SI, auto HI) {
            constexpr int s = SI, hlf = HI;
#pragma unroll
            for (int j = 0; j < 2; ++j)
                if (4 * s + 2 * hlf + j < KH) {
                    hf[hlf][2 * j] = *(LAS bf16x8*)(hk + (s % G2_R) * G2_SLOT + (2 * hlf + j) * 1024);
                    hf[hlf][2 * j + 1] = *(LAS bf16x8*)(hs + (s % G2_R) * G2_SLOT + (2 * hlf + j) * 1024);
                }
        };
        auto mm2 = [&](auto SI, auto HI) {
            constexpr int s = SI, hlf = HI;
#pragma unroll
            for (int j = 0; j < 2; ++j)
                if (4 * s + 2 * hlf + j < KH) {
                    acc[0] = MFMA32(hf[hlf][2 * j], w2[4 * s + 2 * hlf + j], acc[0]);
                    acc[1] = MFMA32(hf[hlf][2 * j + 1], w2[4 * s + 2 * hlf + j], acc[1]);
                }
        };
        auto mix = [&]() {
#pragma unroll
            for (int k = 0; k < 4; ++k) { __builtin_amdgcn_sched_group_barrier(0x8, 1, 0); __builtin_amdgcn_sched_group_barrier(0x100, 1, 0); }
            PIN16(acc[0]); PIN16(acc[1]);
            sched_fence();
        };
        static_for<G2_NS>([&](auto SI) {
            constexpr int s = SI;
            raw_barrier();
            rd2(SI, I0{});
            if constexpr (s > 0) mm2(std::integral_constant<int, s - 1>{}, I1{});
            mix();
            rd2(SI, I1{});
            mm2(SI, I0{});
            mix();
        });
        mm2(std::integral_constant<int, G2_NS - 1>{}, I1{});
#pragma unroll
        for (int k = 0; k < 4; ++k) *(LAS f32x4*)(L + a_ex + 1024 * k) = f32x4{acc[1][4 * k], acc[1][4 * k + 1], acc[1][4 * k + 2], acc[1][4 * k + 3]};
        wait_lgkm0();
        raw_barrier();
        u32 o[8];
#pragma unroll
        for (int k = 0; k < 4; ++k) {
            const f32x4 v = *(LAS f32x4*)(L + (a_ex ^ 8192) + 1024 * k);
            o[2 * k] = pk_bf16(acc[0][4 * k] + v[0], acc[0][4 * k + 1] + v[1]);
            o[2 * k + 1] = pk_bf16(acc[0][4 * k + 2] + v[2], acc[0][4 * k + 3] + v[3]);
        }
        if (layer != 0) {
            LAS u16* ow = (LAS u16*)(L + a_os);
#pragma unroll
            for (int p = 0; p < 8; ++p) { ow[(2 * p) * 64] = (u16)o[p]; ow[(2 * p + 1) * 64] = (u16)(o[p] >> 16); }
            wait_lgkm0();
            raw_barrier();
            return;
        }
        *(LAS u32x4*)(L + a_os) = u32x4{o[0], o[1], o[2], o[3]};
        *(LAS u32x4*)(L + (a_os ^ 16)) = u32x4{o[4], o[5], o[6], o[7]};
        wait_lgkm0();
        raw_barrier();
        float S = 0.f, Q = 0.f, S2 = 0.f, Q2 = 0.f;
#pragma unroll
        for (int i = 0; i < 4; ++i) {
            const u32x2 v = __builtin_bit_cast(u32x2, __builtin_amdgcn_ds_read_tr16_b64_v4bf16((LAS bf16x4*)(L + a_rp + i * 2048)));
            S = dot2(v[0], ONES, S); Q = dot2(v[0], v[0], Q);
            S2 = dot2(v[1], ONES, S2); Q2 = dot2(v[1], v[1], Q2);
        }
        const float v = row_pair_sum(half_sum2(S + S2, Q + Q2));
        if (a_st != 0x80000000u) atomicAdd((double*)(ws + WS_ST64 + SEQ * 16 + a_st), (double)v);
        return;
    }
    if (fg != 15) {
        const int wm = wave & 1, wn = wave >> 1;
        f32x4 acc[6][4];
        bf16x8 xf[2][4], wf[2][6];
#pragma unroll
        for (int i = 0; i < 6; ++i)
#pragma unroll
            for (int t = 0; t < 4; ++t) acc[i][t] = f32x4{0.f, 0.f, 0.f, 0.f};
        int xo[4];
#pragma unroll
        for (int t = 0; t < 4; ++t) xo[t] = xfrag_off(4 * wm + t);
        const int wo = 8192 + 6 * wn * 1024 + lane * 16;
        int slot = 0;
        auto rd = [&](auto CI) {
            constexpr int c = CI;
            LAS char* base = L + slot * G1_SLOT;
            slot = slot == G1_R - 1 ? 0 : slot + 1;
            wf[c][0] = *(LAS bf16x8*)(base + wo);
#pragma unroll
            for (int t = 0; t < 4; ++t) xf[c][t] = tr_frag(base + xo[t], base + xo[t] + 1024);
#pragma unroll
            for (int i = 1; i < 6; ++i) wf[c][i] = *(LAS bf16x8*)(base + wo + i * 1024);
        };
        auto mm = [&](auto CI) {
            constexpr int c = CI;
#pragma unroll
            for (int i = 0; i < 6; ++i)
#pragma unroll
                for (int t = 0; t < 4; ++t) acc[i][t] = MFMA16(wf[c][i], xf[c][t], acc[i][t]);
        };
        auto step = [&](auto CI) {
            constexpr int c = CI;
            raw_barrier();
            rd(std::integral_constant<int, c>{});
            mm(std::integral_constant<int, c ^ 1>{});
#pragma unroll
            for (int k = 0; k < 14; ++k) { __builtin_amdgcn_sched_group_barrier(0x8, 1, 0); __builtin_amdgcn_sched_group_barrier(0x100, 1, 0); }
            PIN16(acc[5][3]);
            sched_fence();
        };
        float cp[4];
#pragma unroll
        for (int k = 0; k < 4; ++k) cp[k] = ((const float*)(ws + WS_CSP))[k * 90 * 32 + min(u0 * 32 + tid, NU * 32 - 1)];
        *(LAS u32*)(L + ML_CTR) = 0u;
        wait_vm<4>();
        raw_barrier();
        rd(I0{});
        sched_fence();
#pragma unroll 1
        for (int s = 1; s < G1_NS - 3; s += 2) { step(I1{}); step(I0{}); }
        *(LAS float*)(L + ML_RED + tid * 4) = (cp[0] + cp[1]) + (cp[2] + cp[3]);
        wait_lgkm0();
        u32 st[4];
        f32x4 cs[6];
        u32* stp = (u32*)(ws + WS_LNW) + layer * SEQ + mt * 128 + 64 * wm + (lane & 15);
#pragma unroll
        for (int t = 0; t < 4; ++t) st[t] = __hip_atomic_load(stp + 16 * t, __ATOMIC_RELAXED, __HIP_MEMORY_SCOPE_AGENT);
        sched_fence();
        step(I1{}); step(I0{}); step(I1{});
#pragma unroll
        for (int i = 0; i < 6; ++i) cs[i] = *(LAS f32x4*)(L + ML_RED + (6 * wn + i) * 64 + g * 16);
        for (int spin = 0; spin < (1 << 22) && !__all(st[0] != 0u && st[1] != 0u && st[2] != 0u && st[3] != 0u); ++spin) {
            __builtin_amdgcn_s_sleep(2);
#pragma unroll
            for (int t = 0; t < 4; ++t) st[t] = __hip_atomic_load(stp + 16 * t, __ATOMIC_RELAXED, __HIP_MEMORY_SCOPE_AGENT);
        }
        const rsrc_t HR = make_rsrc(ws + WS_H + (1 - layer) * WS_HSTEP + ((4 * mt + 2 * wm) * NU + u0 + 3 * wn) * 1024);
        const u32 hoff = (g >> 1) * 512 + pi_inv(16 * (g & 1) + (lane & 15)) * 16;
        f32x2 nm2[4], sb2[4], ce2[4];
#pragma unroll
        for (int t = 0; t < 4; ++t) {
            const float sb = bfhi(st[t]), nm = -bflo(st[t]), ce = -1.44269504088896341f * __builtin_amdgcn_rcpf(sb);
            nm2[t] = f32x2{nm, nm}; sb2[t] = f32x2{sb, sb}; ce2[t] = f32x2{ce, ce};
        }
        auto mmg = [&](auto GI) {
            constexpr int tp = GI / 3, i = GI % 3;
#pragma unroll
            for (int k = 0; k < 4; ++k) acc[2 * i + (k >> 1)][2 * tp + (k & 1)] = MFMA16(wf[1][2 * i + (k >> 1)], xf[1][2 * tp + (k & 1)], acc[2 * i + (k >> 1)][2 * tp + (k & 1)]);
        };
        auto pair = [](const f32x4& v, int j) { return f32x2{v[2 * j], v[2 * j + 1]}; };
        mmg(I0{}); mmg(I1{});
        sched_fence();
        static_for<6>([&](auto GI) {
            constexpr int tp = GI / 3, i = GI % 3;
            f32x2 a[4], u[4];
#pragma unroll
            for (int k = 0; k < 4; ++k) a[k] = __builtin_elementwise_fma(pair(cs[2 * i], k & 1), nm2[2 * tp + (k >> 1)], pair(acc[2 * i][2 * tp + (k >> 1)], k & 1));
#pragma unroll
            for (int k = 0; k < 4; ++k) { a[k] *= ce2[2 * tp + (k >> 1)]; asm volatile("" : "+v"(a[k])); }
            sched_fence();
            if constexpr (GI + 2 < 6) mmg(std::integral_constant<int, GI + 2>{});
#pragma unroll
            for (int k = 0; k < 4; ++k) { a[k] = f32x2{__builtin_amdgcn_exp2f(a[k][0]), __builtin_amdgcn_exp2f(a[k][1])}; asm volatile("" : "+v"(a[k])); }
#pragma unroll
            for (int k = 0; k < 4; ++k) { __builtin_amdgcn_sched_group_barrier(0x8, 1, 0); __builtin_amdgcn_sched_group_barrier(0x402, 2, 0); }
            if constexpr (GI + 2 < 6) { constexpr int t2 = (GI + 2) / 3, i2 = (GI + 2) % 3; PIN16(acc[2 * i2 + 1][2 * t2 + 1]); }
            sched_fence();
#pragma unroll
            for (int k = 0; k < 4; ++k) { a[k] = __builtin_elementwise_fma(a[k], sb2[2 * tp + (k >> 1)], sb2[2 * tp + (k >> 1)]); asm volatile("" : "+v"(a[k])); }
#pragma unroll
            for (int k = 0; k < 4; ++k) {
                u[k] = __builtin_elementwise_fma(pair(cs[2 * i + 1], k & 1), nm2[2 * tp + (k >> 1)], pair(acc[2 * i + 1][2 * tp + (k >> 1)], k & 1));
                asm volatile("" : "+v"(u[k]));
            }
            sched_fence();
#pragma unroll
            for (int k = 0; k < 4; ++k) { a[k] = f32x2{__builtin_amdgcn_rcpf(a[k][0]), __builtin_amdgcn_rcpf(a[k][1])}; asm volatile("" : "+v"(a[k])); }
            sched_fence();
#pragma unroll
            for (int k = 0; k < 4; ++k) { u[k] *= a[k]; asm volatile("" : "+v"(u[k])); }
            u32 o[4];
#pragma unroll
            for (int k = 0; k < 4; ++k) o[k] = pk_bf16(u[k]);
            const auto r0 = __builtin_amdgcn_permlane16_swap(o[0], o[2], false, false);
            const auto r1 = __builtin_amdgcn_permlane16_swap(o[1], o[3], false, false);
            st16(HR, hoff + i * 1024, (u0 + 3 * wn + i < NU ? 0u : 0x80000000u) + tp * NU * 1024, u32x4{r0[0], r1[0], r0[1], r1[1]});
        });
    }
    const char* hF = ws + WS_H + (1 - layer) * WS_HSTEP;
    char* obase = layer ? (char*)out : ws + WS_X + 2 * WS_XSTEP;
    asm volatile("" : "+s"(obase));
    wait_vm<0>();
    u32 order = 0;
    if (lane == 0) order = __hip_atomic_fetch_add((LAS u32*)(L + ML_CTR), 1u, __ATOMIC_RELAXED, __HIP_MEMORY_SCOPE_WORKGROUP);
    if (__builtin_amdgcn_readfirstlane(order) == 3u) {
        u32* cnt = (u32*)(ws + WS_CNT) + (layer * 16 + mt) * 512;
        u32 v = 0;
        if (fg == 15) __builtin_amdgcn_s_sleep(100);
        else if (lane < 16) v = __hip_atomic_fetch_add(cnt + lane * 32, 1u, __ATOMIC_RELAXED, __HIP_MEMORY_SCOPE_AGENT) + 1u;
        v = __builtin_amdgcn_readfirstlane(v);
#pragma unroll 1
        for (int spin = 0; spin < (1 << 22) && v < 15u; ++spin)
            v = __builtin_amdgcn_readfirstlane(__hip_atomic_load(cnt + fg * 32, __ATOMIC_RELAXED, __HIP_MEMORY_SCOPE_AGENT));
    }
    raw_barrier();
    {
        const char* src = hF + (2 * m2 + (wave >> 1)) * NU * 1024;
        const u32 vo = lane * 16, dst = l0 + wave * 4096;
        auto issue = [&](auto SI) {
            constexpr int s = SI;
            glds_s<(s < G2_NS - 1 ? 4 : 3), true>(src + blk_unit(s, wave & 1) * 1024, vo, dst + (s % G2_R) * G2_SLOT);
        };
        constexpr int LEAD = G2_R - 2;
        auto nissued = [](int s) { int a = 2 * s + RAMP2, b = s + LEAD + (s < 2 ? 2 - s : 0); a = a < b ? a : b; return a < G2_NS ? a : G2_NS; };
        static_for<RAMP2>([&](auto SI) { issue(SI); });
        static_for<G2_NS>([&](auto SI) {
            constexpr int s = SI, n = nissued(s), n2 = nissued(s + 1);
            constexpr int fly = n - 1 - s;
            wait_vm<(fly > 0 ? 4 * fly - (n == G2_NS ? 1 : 0) : 0)>();
            raw_barrier();
            if constexpr (n2 > n) issue(std::integral_constant<int, n>{});
            if constexpr (n2 > n + 1) issue(std::integral_constant<int, n + 1>{});
        });
    }
    {
        const rsrc_t xout = make_rsrc(obase);
        const int pos = 64 * m2 + 16 * wave + (lane >> 3);
        const int tok = (pos & ~255) | ((pos & 3) << 6) | ((pos >> 2) & 63);
        const u32 vo = pinned(layer ? tok * (DM * 2) + 128 * nt + ((lane & 7) << 4)
                                    : 2 * (mt * XTILE + (64 * nt + 16 * wave + (lane >> 3)) * 128 + (m2 & 1) * 64) + (((lane & 7) ^ (lane >> 3)) << 4));
        const u32 a_rd = pinned(ML_OS + wave * 2048 + lane * 16);
        raw_barrier();
        raw_barrier();
        st16(xout, vo, 0, *(LAS u32x4*)(L + a_rd));
        st16(xout, vo, 2048, *(LAS u32x4*)(L + a_rd + 1024));
    }
}

extern "C" __global__ void s4_tail_kernel() {}

extern "C" void kernel_launch(void* const* d_in, const int* in_sizes, int n_in, void* d_out, int out_size,
                              void* d_ws, size_t ws_size, hipStream_t stream) {
    (void)in_sizes; (void)n_in; (void)out_size; (void)ws_size;
    const u16* x0 = (const u16*)d_in[0];
    const u16* logA = (const u16*)d_in[1];
    const u16* Aim = (const u16*)d_in[2];
    const u16* Cre = (const u16*)d_in[3];
    const u16* Cim = (const u16*)d_in[4];
    const u16* logdt = (const u16*)d_in[5];
    const u16* Dsk = (const u16*)d_in[6];
    const u16* Wg = (const u16*)d_in[8];
    const u16* Wu = (const u16*)d_in[9];
    const u16* Wd = (const u16*)d_in[10];
    (void)d_ws;
    (void)hipFuncSetAttribute((const void*)s4_conv_kernel, hipFuncAttributeMaxDynamicSharedMemorySize, CV_LDS);
    (void)hipFuncSetAttribute((const void*)s4_mlp_kernel, hipFuncAttributeMaxDynamicSharedMemorySize, ML_LDS);

    s4_prep_kernel<<<PREP_BLOCKS, 256, PREP_LDS, stream>>>(x0, logA, Aim, Cre, Cim, logdt, Dsk);
    for (int layer = 0; layer < 2; ++layer) {
        s4_conv_kernel<<<512, layer ? dim3(1, 256) : dim3(256), CV_LDS, stream>>>(Wg, Wu, Wd);
        s4_mlp_kernel<<<256, layer ? dim3(1, 512) : dim3(512), ML_LDS, stream>>>((u16*)d_out);
    }
}
```
